```python
import torch

def _softmax(x, axis=-1):
    return torch.softmax(x, dim=axis)
def _gelu(x):
    return torch.nn.functional.gelu(x, approximate="tanh")
def _silu(x):
    return torch.nn.functional.silu(x)
def _sigmoid(x):
    return torch.sigmoid(x)
def _layernorm(x, axis=-1, eps=1e-5):
    m = x.mean(dim=axis, keepdim=True)
    v = x.var(dim=axis, keepdim=True, unbiased=False)
    return (x - m) / torch.sqrt(v + eps)
def _scatter_add(tgt, idx, src, axis=0):
    return tgt.clone().index_add_(axis, idx, src)
def _segment_sum(src, seg, n):
    out = torch.zeros((n,) + src.shape[1:], dtype=src.dtype)
    return out.index_add_(0, seg, src)
def _pad(x, pw):
    flat = tuple(v for pair in reversed(pw) for v in pair)
    return torch.nn.functional.pad(x, flat)

kernel_name = 'gated_mix__ssm_conv_h3__glu_gated_glu__prompt'

def setup_inputs():

    _w = (lambda a: (lambda t: t.to(torch.bfloat16) if t.is_floating_point() else t)(torch.as_tensor(a)))
    def _A_setup():
        B, S, D = 1, 2048, 576
        ks = 4
        return {
            "q": _w((lambda *s: torch.randn(*s, dtype=torch.bfloat16) * 0.05)(B, S, D)),
            "k": _w((lambda *s: torch.randn(*s, dtype=torch.bfloat16) * 0.8)(B, S, D)),
            "v": _w((lambda *s: torch.randn(*s, dtype=torch.bfloat16) * 0.8)(B, S, D)),
            "shift_k": _w((lambda *s: torch.randn(*s, dtype=torch.bfloat16) * 0.8)(D, ks) * 0.5),
            "ssm_k": _w((lambda *s: torch.randn(*s, dtype=torch.bfloat16) * 0.8)(D, S) * (1.0 / S ** 0.5)),
            "ssm_D": _w((lambda *s: torch.randn(*s, dtype=torch.bfloat16) * 0.8)(D)),
        }
    _A_in = _A_setup()

    def _B_setup():
        B, S, D, F = 1, 2048, 576, 4608
        return {
            "x": _w((lambda *s: torch.randn(*s, dtype=torch.bfloat16) * 0.05)(B, S, D)),
            "w_gate": _w((lambda *s: torch.randn(*s, dtype=torch.bfloat16) * 0.4)(D, F)),
            "w_up": _w((lambda *s: torch.randn(*s, dtype=torch.bfloat16) * 0.05)(D, F)),
            "w_down": _w((lambda *s: torch.randn(*s, dtype=torch.bfloat16) * 0.05)(F, D)),
        }
    _B_in = _B_setup()

    return {
        "A_q": _A_in["q"],
        "A_k": _A_in["k"],
        "A_v": _A_in["v"],
        "A_shift_k": _A_in["shift_k"],
        "A_ssm_k": _A_in["ssm_k"],
        "A_ssm_D": _A_in["ssm_D"],
        "B_x": _B_in["x"],
        "B_w_gate": _B_in["w_gate"],
        "B_w_up": _B_in["w_up"],
        "B_w_down": _B_in["w_down"],
        "gate_w": _w((lambda *s: torch.randn(*s, dtype=torch.bfloat16) * 0.05)(576) * 16.0),
    }

def reference(A_q, A_k, A_v, A_shift_k, A_ssm_k, A_ssm_D, B_x, B_w_gate, B_w_up, B_w_down, gate_w):
    def _A_ref(q, k, v, shift_k, ssm_k, ssm_D):
        B, S, D = q.shape
        ks = shift_k.shape[-1]
        k_pad = _pad(k, ((0, 0), (ks - 1, 0), (0, 0)))
        k_shift = torch.zeros_like(k)
        for t in range(ks):
            k_shift = k_shift + k_pad[:, t:t + S, :] * shift_k[:, ks - 1 - t][None, None, :]
        kv = k_shift * v
        L = 2 * S
        kv_pad = _pad(kv, ((0, 0), (0, S), (0, 0)))
        kf_pad = _pad(ssm_k, ((0, 0), (0, S)))
        kv_f = (lambda x, axes=(-2,-1): torch.fft.rfft2(x.float(), dim=axes))(kv_pad[:, None, :, :], axes=(1, 2))
        kf_f = (lambda x, axes=(-2,-1): torch.fft.rfft2(x.float(), dim=axes))(kf_pad[:, None, :], axes=(1, 2))
        kf_f = (lambda x, *d: x.permute(*d))(kf_f, 1, 2, 0)
        y_f = kv_f * kf_f[None, :, :, :]
        y = (lambda x, s, axes=(-2,-1): torch.fft.irfft2(x, s=s, dim=axes).to(torch.bfloat16))(y_f, (1, L), axes=(1, 2))[:, 0, :S, :]
        y = y + kv * ssm_D[None, None, :]
        return q * y

    def _B_ref(x, w_gate, w_up, w_down):
        g = torch.einsum("bsd,df->bsf", x, w_gate)
        u = torch.einsum("bsd,df->bsf", x, w_up)
        h = _sigmoid(g) * u
        return torch.einsum("bsf,fd->bsd", h, w_down)

    x = A_q
    g = _sigmoid((x * gate_w).sum(-1, keepdims=True))
    a = _A_ref(x, A_k, A_v, A_shift_k, A_ssm_k, A_ssm_D)
    b = _B_ref(x, B_w_gate, B_w_up, B_w_down)
    return g * a + (1 - g) * b
```

```cpp
#include <hip/hip_runtime.h>
#include <hip/hip_bf16.h>
typedef unsigned short u16;
constexpr int S=2048, D=576, F=4608;
__device__ __forceinline__ float b2f(u16 v){ return __uint_as_float(((unsigned)v)<<16); }
__device__ __forceinline__ u16 f2b(float f){ __hip_bfloat16 b=__float2bfloat16(f); return *(u16*)&b; }
__device__ __forceinline__ float rb(float f){ return b2f(f2b(f)); }

__global__ void k_kv(const u16* k,const u16* v,const u16* sk,u16* kvT){
  int i=blockIdx.x*256+threadIdx.x; if(i>=S*D) return; int s=i/D,d=i%D;
  float acc=0.f;
  for(int t=0;t<4;t++){ int ss=s-3+t; float kk= ss>=0? b2f(k[ss*D+d]):0.f; acc=rb(acc+rb(kk*b2f(sk[d*4+3-t]))); }
  kvT[d*S+s]=f2b(acc*b2f(v[i]));
}
__global__ void k_conv(const u16* kvT,const u16* ssmk,const u16* ssmD,const u16* q,float* a){
  __shared__ float u[S]; __shared__ float kk[S];
  int d=blockIdx.x;
  for(int i=threadIdx.x;i<S;i+=256){u[i]=b2f(kvT[d*S+i]); kk[i]=b2f(ssmk[d*S+i]);}
  __syncthreads();
  for(int t=threadIdx.x;t<S;t+=256){ float acc=0.f; for(int s=0;s<=t;s++) acc+=kk[t-s]*u[s];
    float y=rb(acc); y=rb(y+rb(u[t]*b2f(ssmD[d]))); a[t*D+d]=rb(b2f(q[t*D+d])*y); }
}
__global__ void k_h(const u16* x,const u16* wg,const u16* wu,u16* h){
  __shared__ float xs[8][D];
  int s0=blockIdx.y*8; int f=blockIdx.x*256+threadIdx.x;
  for(int i=threadIdx.x;i<8*D;i+=256) xs[i/D][i%D]=b2f(x[s0*D+i]);
  __syncthreads();
  float g[8]={},u[8]={};
  for(int d=0;d<D;d++){ float a=b2f(wg[d*F+f]),b=b2f(wu[d*F+f]);
    #pragma unroll
    for(int r=0;r<8;r++){g[r]+=xs[r][d]*a;u[r]+=xs[r][d]*b;} }
  for(int r=0;r<8;r++){ float gg=rb(g[r]),uu=rb(u[r]); float sg=rb(1.f/(1.f+__expf(-gg))); h[(s0+r)*F+f]=f2b(sg*uu);}
}
__global__ void k_out(const u16* x,const u16* gw,const u16* h,const u16* wd,const float* a,u16* out){
  __shared__ float hs[4][F]; __shared__ float red[4][64*9];__shared__ float gs[4];
  int s0=blockIdx.x*4; int d=threadIdx.x;
  for(int i=threadIdx.x;i<4*F;i+=576) hs[i/F][i%F]=b2f(h[s0*F+i]);
  for(int r=0;r<4;r++) red[r][d]=rb(b2f(x[(s0+r)*D+d])*b2f(gw[d]));
  __syncthreads();
  if(d<4){ float t=0; for(int i=0;i<D;i++) t+=red[d][i]; t=rb(t); gs[d]=rb(1.f/(1.f+__expf(-t))); }
  __syncthreads();
  float acc[4]={};
  for(int f=0;f<F;f++){ float w=b2f(wd[f*D+d]);
    #pragma unroll
    for(int r=0;r<4;r++) acc[r]+=hs[r][f]*w; }
  for(int r=0;r<4;r++){ float b=rb(acc[r]); float g=gs[r]; out[(s0+r)*D+d]=f2b(rb(g*a[(s0+r)*D+d])+rb(rb(1.f-g)*b)); }
}
extern "C" void kernel_launch(void* const* d_in, const int* in_sizes, int n_in, void* d_out, int out_size, void* d_ws, size_t ws_size, hipStream_t stream) {
  const u16* q=(const u16*)d_in[0];const u16* k=(const u16*)d_in[1];const u16* v=(const u16*)d_in[2];
  const u16* sk=(const u16*)d_in[3];const u16* ssmk=(const u16*)d_in[4];const u16* ssmD=(const u16*)d_in[5];
  const u16* wg=(const u16*)d_in[7];const u16* wu=(const u16*)d_in[8];const u16* wd=(const u16*)d_in[9];const u16* gw=(const u16*)d_in[10];
  char* ws=(char*)d_ws; u16* kvT=(u16*)ws; float* a=(float*)(ws+(4<<20)); u16* h=(u16*)(ws+(16<<20));
  k_kv<<<(S*D+255)/256,256,0,stream>>>(k,v,sk,kvT);
  k_conv<<<D,256,0,stream>>>(kvT,ssmk,ssmD,q,a);
  k_h<<<dim3(F/256,S/8),256,0,stream>>>(q,wg,wu,h);
  k_out<<<S/4,576,0,stream>>>(q,gw,h,wd,a,(u16*)d_out);
}
```

```cpp
#include <hip/hip_runtime.h>

__global__ __launch_bounds__(256, 1) void gated_mix_fused(
    const void* q, unsigned n0, unsigned f0, const void* k, unsigned n1, unsigned f1, const void* v, unsigned n2, unsigned f2,
    const void* ssmk, unsigned n3, unsigned f3, const void* wg, unsigned n4, unsigned f4, const void* wu, unsigned n5, unsigned f5,
    void* ws, unsigned n6, unsigned f6, const void* wd, unsigned n7, unsigned f7, void* out, unsigned n8, unsigned f8,
    const void* sk, const void* gw, const void* ssmD) {
  __shared__ __attribute__((aligned(16))) char smem[163840];
  asm volatile(
"s_load_dwordx16 s[4:19], %[ka], 0\n"
"s_load_dwordx4 s[44:47], %[ka], 144\n"
"s_load_dwordx8 s[20:27], %[ka], 64\n"
"s_load_dwordx4 s[28:31], %[ka], 96\n"
"s_load_dwordx8 s[36:43], %[ka], 112\n"
"s_load_dwordx2 s[48:49], %[ka], 160\n"
"s_mov_b32 s33, %[bid]\n"
"s_getpc_b64 s[50:51]\n"
"pFpc_ok_gated_mix_fused:\n"
"s_bfe_u32 s83, s33, 196611\n"
"s_lshl_b32 s83, s83, 7\n"
"v_lshl_add_u32 v248, v0, 10, s83\n"
"v_min_u32 v248, pFend_ok_gated_mix_fused-pFpc_ok_gated_mix_fused-4, v248\n"
"global_load_dword v249, v248, s[50:51] offset:0\n"
"v_lshrrev_b32 v221, 6, v0\n"
"v_bfe_u32 v220, v0, 3, 3\n"
"v_readfirstlane_b32 s52, v221\n"
"s_waitcnt lgkmcnt(0)\n"
"s_load_dwordx2 s[34:35], s[28:29], 262144\n"
"s_lshl_b32 s61, s33, 2\n"
"s_load_dword s85, s[48:49], s61\n"
"s_lshr_b32 s57, s33, 3\n"
"s_lshl_b32 s62, s57, 2\n"
"s_add_u32 s62, s62, 1024\n"
"s_load_dword s87, s[48:49], s62\n"
"s_bfe_u32 s58, s33, 65536\n"
"s_and_b32 s59, s57, 3\n"
"s_lshl2_add_u32 s88, s58, s59\n"
"s_bfe_u32 s58, s33, 131073\n"
"s_lshr_b32 s56, s57, 2\n"
"s_lshl3_add_u32 s89, s58, s56\n"
"s_add_u32 s63, s59, s56\n"
"s_mul_i32 s62, s63, 43\n"
"s_lshr_b32 s62, s62, 7\n"
"s_mul_i32 s62, s62, 3\n"
"s_sub_u32 s63, s63, s62\n"
"s_mul_i32 s91, s63, 3\n"
"v_bfe_u32 v253, v0, 3, 3\n"
"v_and_b32 v254, 7, v0\n"
"v_bfe_u32 v255, v0, 4, 2\n"
"v_xor_b32 v252, v254, v255\n"
"v_lshlrev_b32 v252, 4, v252\n"
"v_mul_u32_u24 v238, 1152, v253\n"
"v_add_u32 v238, v238, v252\n"
"v_add_u32 v239, 9216, v238\n"
"v_and_b32 v252, 2, v253\n"
"v_xor_b32 v252, v254, v252\n"
"v_mul_u32_u24 v240, 9216, v253\n"
"v_lshl_add_u32 v240, v252, 4, v240\n"
"v_add_u32 v241, 73728, v240\n"
"s_mul_i32 s54, s52, 2176\n"
"s_and_b32 s60, s52, 1\n"
"s_lshl2_add_u32 s60, s91, s60\n"
"s_mul_i32 s60, s60, 147456\n"
"s_lshl_b32 s61, s89, 7\n"
"s_add_u32 s68, s60, s61\n"
"s_mul_i32 s55, s52, 26112\n"
"s_add_u32 s55, s55, 50176\n"
"s_lshl2_add_u32 s59, s88, s52\n"
"s_mul_i32 s61, s59, 73728\n"
"s_lshl_b32 s62, s91, 7\n"
"s_add_u32 s61, s61, s62\n"
"s_and_b32 s90, s89, 1\n"
"s_mul_i32 s62, s90, 36864\n"
"s_add_u32 s64, s61, s62\n"
"s_add_u32 s65, s64, 18432\n"
"s_sub_u32 s61, s61, s62\n"
"s_add_u32 s66, s61, 36864\n"
"s_add_u32 s67, s61, 55296\n"
"s_cmp_ge_u32 s52, 2\n"
"s_cselect_b64 s[20:21], s[24:25], s[20:21]\n"
"s_mov_b32 s80, 3216550459\n"
"s_mov_b32 s81, 4294901760\n"
"s_add_u32 m0, s54, 0\n"
"s_nop 0\n"
"buffer_load_dwordx4 v240, s[20:23], s68 offen offset:0 lds\n"
"s_add_u32 m0, s54, 1088\n"
"s_nop 0\n"
"buffer_load_dwordx4 v241, s[20:23], s68 offen offset:0 lds\n"
"s_add_u32 m0, s55, 2048\n"
"s_nop 0\n"
"buffer_load_dwordx4 v238, s[4:7], s64 offen offset:0 lds\n"
"s_add_u32 m0, s55, 3136\n"
"s_nop 0\n"
"buffer_load_dwordx4 v239, s[4:7], s64 offen offset:0 lds\n"
"s_add_u32 m0, s55, 4224\n"
"s_nop 0\n"
"buffer_load_dwordx4 v238, s[4:7], s65 offen offset:0 lds\n"
"s_add_u32 m0, s55, 5312\n"
"s_nop 0\n"
"buffer_load_dwordx4 v239, s[4:7], s65 offen offset:0 lds\n"
"s_add_u32 m0, s55, 6400\n"
"s_nop 0\n"
"buffer_load_dwordx4 v238, s[4:7], s66 offen offset:0 lds\n"
"s_add_u32 m0, s55, 7488\n"
"s_nop 0\n"
"buffer_load_dwordx4 v239, s[4:7], s66 offen offset:0 lds\n"
"s_add_u32 m0, s55, 8576\n"
"s_nop 0\n"
"buffer_load_dwordx4 v238, s[4:7], s67 offen offset:0 lds\n"
"s_add_u32 m0, s55, 9664\n"
"s_nop 0\n"
"buffer_load_dwordx4 v239, s[4:7], s67 offen offset:0 lds\n"
"s_add_u32 s68, s68, 294912\n"
"s_add_u32 m0, s54, 8704\n"
"s_nop 0\n"
"buffer_load_dwordx4 v240, s[20:23], s68 offen offset:0 lds\n"
"s_add_u32 m0, s54, 9792\n"
"s_nop 0\n"
"buffer_load_dwordx4 v241, s[20:23], s68 offen offset:0 lds\n"
"s_add_u32 s68, s68, 294912\n"
"s_add_u32 m0, s54, 17408\n"
"s_nop 0\n"
"buffer_load_dwordx4 v240, s[20:23], s68 offen offset:0 lds\n"
"s_add_u32 m0, s54, 18496\n"
"s_nop 0\n"
"buffer_load_dwordx4 v241, s[20:23], s68 offen offset:0 lds\n"
"s_add_u32 m0, s55, 10624\n"
"s_nop 0\n"
"buffer_load_dwordx4 v238, s[4:7], s64 offen offset:128 lds\n"
"s_add_u32 m0, s55, 11712\n"
"s_nop 0\n"
"buffer_load_dwordx4 v239, s[4:7], s64 offen offset:128 lds\n"
"s_add_u32 m0, s55, 12800\n"
"s_nop 0\n"
"buffer_load_dwordx4 v238, s[4:7], s65 offen offset:128 lds\n"
"s_add_u32 m0, s55, 13888\n"
"s_nop 0\n"
"buffer_load_dwordx4 v239, s[4:7], s65 offen offset:128 lds\n"
"s_add_u32 m0, s55, 14976\n"
"s_nop 0\n"
"buffer_load_dwordx4 v238, s[4:7], s66 offen offset:128 lds\n"
"s_add_u32 m0, s55, 16064\n"
"s_nop 0\n"
"buffer_load_dwordx4 v239, s[4:7], s66 offen offset:128 lds\n"
"s_add_u32 m0, s55, 17152\n"
"s_nop 0\n"
"buffer_load_dwordx4 v238, s[4:7], s67 offen offset:128 lds\n"
"s_add_u32 m0, s55, 18240\n"
"s_nop 0\n"
"buffer_load_dwordx4 v239, s[4:7], s67 offen offset:128 lds\n"
"s_add_u32 s68, s68, 294912\n"
"s_add_u32 m0, s54, 26112\n"
"s_nop 0\n"
"buffer_load_dwordx4 v240, s[20:23], s68 offen offset:0 lds\n"
"s_add_u32 m0, s54, 27200\n"
"s_nop 0\n"
"buffer_load_dwordx4 v241, s[20:23], s68 offen offset:0 lds\n"
"s_mov_b32 s71, s58\n"
"s_mov_b32 s73, s59\n"
"s_mov_b32 s74, s60\n"
"s_and_b32 s61, s33, 7\n"
"s_bfe_u32 s62, s33, 131075\n"
"s_lshl2_add_u32 s61, s61, s62\n"
"s_lshr_b32 s62, s33, 5\n"
"v_and_b32 v221, 7, v0\n"
"s_lshl_b32 s63, s52, 3\n"
"v_add_u32 v221, s63, v221\n"
"s_mul_i32 s60, s61, 73728\n"
"s_lshl_b32 s58, s62, 7\n"
"s_add_u32 s63, s60, s58\n"
"v_mul_u32_u24 v223, 9216, v220\n"
"v_lshl_add_u32 v222, v221, 2, v223\n"
"v_add_u32 v222, s63, v222\n"
"v_subrev_u32 v223, 3456, v222\n"
"s_movk_i32 s84, 4608\n"
"s_lshl_b32 s63, s62, 9\n"
"v_lshl_add_u32 v224, v221, 4, s63\n"
"v_lshlrev_b32 v41, 13, v221\n"
"v_lshlrev_b32 v220, 4, v220\n"
"v_sub_u32 v41, v41, v220\n"
"v_add_u32 v41, 4080, v41\n"
"s_lshl_b32 s63, s61, 7\n"
"s_lshl_b32 s62, s62, 18\n"
"s_sub_u32 s62, s62, s63\n"
"s_add_u32 s48, s62, 31068160\n"
"s_add_u32 s49, s48, 4096\n"
"s_and_b32 s61, s33, 7\n"
"s_bfe_u32 s62, s33, 131075\n"
"s_lshl2_add_u32 s61, s61, s62\n"
"s_lshr_b32 s62, s33, 5\n"
"s_lshl2_add_u32 s59, s62, s52\n"
"s_mul_i32 s60, s61, 73728\n"
"s_lshl2_add_u32 s63, s59, s60\n"
"s_add_u32 s63, s63, 1024\n"
"v_and_b32 v212, 63, v0\n"
"v_mul_u32_u24 v213, 1152, v212\n"
"v_add_u32 v213, s63, v213\n"
"v_subrev_u32 v214, 3456, v213\n"
"s_lshl_b32 s62, s59, 4\n"
"s_add_u32 s62, s62, 4096\n"
"v_mov_b32 v215, s62\n"
"v_lshlrev_b32 v212, 1, v212\n"
"v_sub_u32 v42, 126, v212\n"
"s_lshl_b32 s59, s59, 13\n"
"s_lshl_b32 s61, s61, 7\n"
"s_sub_u32 s59, s59, s61\n"
"s_add_u32 s96, s59, 33169280\n"
"s_add_u32 s97, s96, 4096\n"
"v_lshrrev_b32 v216, 5, v0\n"
"v_and_b32 v217, 31, v0\n"
"v_mul_u32_u24 v217, 36, v217\n"
"v_mul_u32_u24 v218, 1152, v216\n"
"v_add_u32 v218, v218, v217\n"
"s_mul_i32 s98, s33, 9216\n"
"v_and_b32 v43, 15, v0\n"
"v_lshlrev_b32 v43, 2, v43\n"
"v_lshl_add_u32 v43, v216, 6, v43\n"
"s_mov_b32 s58, s71\n"
"s_mov_b32 s59, s73\n"
"s_mov_b32 s60, s74\n"
"v_bfe_u32 v253, v0, 2, 2\n"
"v_and_b32 v254, 3, v0\n"
"v_mul_u32_u24 v252, 1088, v255\n"
"v_lshl_add_u32 v252, v254, 3, v252\n"
"v_lshl_add_u32 v252, v253, 7, v252\n"
"v_lshrrev_b32 v253, 1, v253\n"
"v_lshl_add_u32 v236, v253, 5, v252\n"
"v_xor_b32 v237, 32, v236\n"
"v_bfe_u32 v252, v0, 1, 2\n"
"v_xor_b32 v252, v252, v255\n"
"v_and_b32 v254, 7, v0\n"
"v_lshlrev_b32 v254, 7, v254\n"
"v_lshl_add_u32 v252, v252, 4, v254\n"
"v_bfe_u32 v253, v0, 3, 1\n"
"v_mul_u32_u24 v253, 1088, v253\n"
"s_add_u32 s63, s55, 2048\n"
"v_add3_u32 v7, v252, v253, s63\n"
"s_waitcnt lgkmcnt(0)\n"
"s_waitcnt vmcnt(14)\n"
"s_barrier\n"
"ds_read_b128 a[0:3], v7 offset:0\n"
"ds_read_b128 a[4:7], v7 offset:2176\n"
"ds_read_b128 a[8:11], v7 offset:4352\n"
"ds_read_b128 a[12:15], v7 offset:6528\n"
"ds_read_b64_tr_b16 v[44:45], v236 offset:0\n"
"ds_read_b64_tr_b16 v[46:47], v236 offset:512\n"
"ds_read_b64_tr_b16 v[48:49], v236 offset:4352\n"
"ds_read_b64_tr_b16 v[50:51], v236 offset:4864\n"
"ds_read_b64_tr_b16 v[52:53], v237 offset:0\n"
"ds_read_b64_tr_b16 v[54:55], v237 offset:512\n"
"s_waitcnt lgkmcnt(4)\n"
"v_mfma_f32_16x16x32_bf16 v[60:63], v[44:47], a[0:3], 0\n"
"s_mov_b32 s92, 294912\n"
"ds_read_b64_tr_b16 v[56:57], v237 offset:4352\n"
"ds_read_b64_tr_b16 v[58:59], v237 offset:4864\n"
"v_mfma_f32_16x16x32_bf16 v[64:67], v[44:47], a[4:7], 0\n"
"s_movk_i32 s94, 384\n"
"buffer_load_dword v31, v213, s[8:11], 0 offen offset:0\n"
"v_mfma_f32_16x16x32_bf16 v[68:71], v[44:47], a[8:11], 0\n"
"s_cmp_eq_u32 s91, 6\ns_cselect_b32 s92, 4289953792, s92\ns_cselect_b32 s94, 4294966528, s94\n"
"ds_read_b128 a[16:19], v7 offset:64\n"
"ds_read_b128 a[20:23], v7 offset:2240\n"
"v_mfma_f32_16x16x32_bf16 v[72:75], v[44:47], a[12:15], 0\n"
"s_waitcnt lgkmcnt(6)\n"
"buffer_load_dword v34, v214, s[8:11], 0 offen offset:0\n"
"v_mfma_f32_16x16x32_bf16 v[92:95], v[48:51], a[0:3], 0\n"
"s_mov_b32 s93, 294912\n"
"ds_read_b64_tr_b16 v[44:45], v236 offset:64\n"
"ds_read_b64_tr_b16 v[46:47], v236 offset:576\n"
"v_mfma_f32_16x16x32_bf16 v[96:99], v[48:51], a[4:7], 0\n"
"s_movk_i32 s95, 384\n"
"buffer_load_dword v35, v213, s[12:15], 0 offen offset:0\n"
"v_mfma_f32_16x16x32_bf16 v[100:103], v[48:51], a[8:11], 0\n"
"s_cmp_eq_u32 s91, 3\ns_cselect_b32 s93, 4289953792, s93\ns_cselect_b32 s95, 4294966528, s95\n"
"ds_read_b128 a[24:27], v7 offset:4416\n"
"ds_read_b128 a[28:31], v7 offset:6592\n"
"v_mfma_f32_16x16x32_bf16 v[104:107], v[48:51], a[12:15], 0\n"
"s_waitcnt lgkmcnt(8)\n"
"global_load_dwordx4 v[36:39], v215, s[44:45] offset:0\n"
"v_mfma_f32_16x16x32_bf16 v[76:79], v[52:55], a[0:3], 0\n"
"s_lshl_b32 s61, s89, 7\n"
"ds_read_b64_tr_b16 v[48:49], v236 offset:4416\n"
"ds_read_b64_tr_b16 v[50:51], v236 offset:4928\n"
"v_mfma_f32_16x16x32_bf16 v[80:83], v[52:55], a[4:7], 0\n"
"s_add_u32 s61, s61, s60\n"
"buffer_load_dwordx4 v[12:15], v218, s[4:7], s98 offen offset:0\n"
"v_mfma_f32_16x16x32_bf16 v[84:87], v[52:55], a[8:11], 0\n"
"s_add_u32 s69, s61, 4096\n"
"buffer_load_dwordx4 v[16:19], v218, s[4:7], s98 offen offset:16\n"
"v_mfma_f32_16x16x32_bf16 v[88:91], v[52:55], a[12:15], 0\n"
"s_waitcnt lgkmcnt(2)\n"
"buffer_load_dword v20, v218, s[4:7], s98 offen offset:32\n"
"v_mfma_f32_16x16x32_bf16 v[108:111], v[56:59], a[0:3], 0\n"
"s_lshr_b32 s61, s89, 1\n"
"ds_read_b64_tr_b16 v[52:53], v237 offset:64\n"
"ds_read_b64_tr_b16 v[54:55], v237 offset:576\n"
"v_mfma_f32_16x16x32_bf16 v[112:115], v[56:59], a[4:7], 0\n"
"s_lshl_b32 s61, s61, 6\n"
"global_load_dwordx4 v[22:25], v217, s[46:47] offset:0\n"
"v_mfma_f32_16x16x32_bf16 v[116:119], v[56:59], a[8:11], 0\n"
"s_add_u32 s61, s61, 8192\n"
"global_load_dwordx4 v[26:29], v217, s[46:47] offset:16\n"
"v_mfma_f32_16x16x32_bf16 v[120:123], v[56:59], a[12:15], 0\n"
"global_load_dword v30, v217, s[46:47] offset:32\n"
"v_mfma_f32_16x16x32_bf16 v[124:127], v[44:47], a[0:3], 0\n"
"s_add_u32 s70, s60, s61\n"
"ds_read_b64_tr_b16 v[56:57], v237 offset:4416\n"
"ds_read_b64_tr_b16 v[58:59], v237 offset:4928\n"
"v_mfma_f32_16x16x32_bf16 v[128:131], v[44:47], a[4:7], 0\n"
"s_lshl_b32 s53, s52, 10\n"
"buffer_load_dword v188, v223, s[8:11], 0 offen offset:0\n"
"v_mfma_f32_16x16x32_bf16 v[132:135], v[44:47], a[8:11], 0\n"
"s_lshl_b32 s82, s33, 10\n"
"buffer_load_dword v189, v223, s[8:11], 0 offen offset:1152\n"
"v_mfma_f32_16x16x32_bf16 v[136:139], v[44:47], a[12:15], 0\n"
"s_waitcnt vmcnt(24) lgkmcnt(4)\n"
"s_barrier\n"
"s_add_u32 s34, s34, 1\ns_addc_u32 s35, s35, 0\n"
"v_mfma_f32_16x16x32_bf16 v[156:159], v[48:51], a[0:3], 0\n"
"s_mul_i32 s59, s59, 144\n"
"ds_read_b64_tr_b16 v[44:45], v236 offset:8704\n"
"ds_read_b64_tr_b16 v[46:47], v236 offset:9216\n"
"v_mfma_f32_16x16x32_bf16 v[160:163], v[48:51], a[4:7], 0\n"
"s_mul_i32 s61, s58, 36\n"
"buffer_load_dword v190, v223, s[8:11], 0 offen offset:2304\n"
"v_mfma_f32_16x16x32_bf16 v[164:167], v[48:51], a[8:11], 0\n"
"s_lshr_b32 s62, s56, 2\n"
"buffer_load_dword v191, v222, s[8:11], 0 offen offset:0\n"
"v_mfma_f32_16x16x32_bf16 v[168:171], v[48:51], a[12:15], 0\n"
"s_waitcnt lgkmcnt(4)\n"
"buffer_load_dword v192, v222, s[8:11], 0 offen offset:1152\n"
"v_mfma_f32_16x16x32_bf16 v[140:143], v[52:55], a[0:3], 0\n"
"s_mul_i32 s62, s62, 18\n"
"ds_read_b64_tr_b16 v[48:49], v236 offset:13056\n"
"ds_read_b64_tr_b16 v[50:51], v236 offset:13568\n"
"v_mfma_f32_16x16x32_bf16 v[144:147], v[52:55], a[4:7], 0\n"
"s_add_u32 s61, s61, s62\n"
"buffer_load_dword v193, v222, s[8:11], 0 offen offset:2304\n"
"v_mfma_f32_16x16x32_bf16 v[148:151], v[52:55], a[8:11], 0\n"
"s_add_u32 s61, s61, s59\n"
"buffer_load_dword v194, v222, s[8:11], 0 offen offset:3456\n"
"v_mfma_f32_16x16x32_bf16 v[152:155], v[52:55], a[12:15], 0\n"
"s_waitcnt lgkmcnt(4)\n"
"buffer_load_dword v195, v222, s[8:11], s84 offen offset:0\n"
"v_mfma_f32_16x16x32_bf16 v[172:175], v[56:59], a[0:3], 0\n"
"s_and_b32 s62, s56, 3\n"
"ds_read_b64_tr_b16 v[52:53], v237 offset:8704\n"
"ds_read_b64_tr_b16 v[54:55], v237 offset:9216\n"
"v_mfma_f32_16x16x32_bf16 v[176:179], v[56:59], a[4:7], 0\n"
"s_lshl2_add_u32 s62, s62, s61\n"
"buffer_load_dword v196, v222, s[8:11], s84 offen offset:1152\n"
"v_mfma_f32_16x16x32_bf16 v[180:183], v[56:59], a[8:11], 0\n"
"s_lshl_b32 s62, s62, 12\n"
"buffer_load_dword v197, v222, s[8:11], s84 offen offset:2304\n"
"v_mfma_f32_16x16x32_bf16 v[184:187], v[56:59], a[12:15], 0\n"
"s_waitcnt lgkmcnt(4)\n"
"buffer_load_dword v198, v222, s[8:11], s84 offen offset:3456\n"
"v_mfma_f32_16x16x32_bf16 v[60:63], v[44:47], a[16:19], v[60:63]\n"
"s_lshl_b32 s63, s90, 11\n"
"ds_read_b64_tr_b16 v[56:57], v237 offset:13056\n"
"ds_read_b64_tr_b16 v[58:59], v237 offset:13568\n"
"v_mfma_f32_16x16x32_bf16 v[64:67], v[44:47], a[20:23], v[64:67]\n"
"s_add_u32 s63, s63, 397312\n"
"global_load_dwordx4 v[208:211], v224, s[44:45] offset:0\n"
"v_mfma_f32_16x16x32_bf16 v[68:71], v[44:47], a[24:27], v[68:71]\n"
"s_add_u32 s71, s62, s63\n"
"buffer_load_dword v200, v222, s[12:15], 0 offen offset:0\n"
"v_mfma_f32_16x16x32_bf16 v[72:75], v[44:47], a[28:31], v[72:75]\n"
"s_waitcnt lgkmcnt(4)\n"
"buffer_load_dword v201, v222, s[12:15], 0 offen offset:1152\n"
"v_mfma_f32_16x16x32_bf16 v[92:95], v[48:51], a[16:19], v[92:95]\n"
"s_xor_b32 s72, s71, 2048\n"
"ds_read_b64_tr_b16 v[44:45], v236 offset:8768\n"
"ds_read_b64_tr_b16 v[46:47], v236 offset:9280\n"
"v_mfma_f32_16x16x32_bf16 v[96:99], v[48:51], a[20:23], v[96:99]\n"
"s_add_u32 s73, s71, 4096\n"
"buffer_load_dword v202, v222, s[12:15], 0 offen offset:2304\n"
"v_mfma_f32_16x16x32_bf16 v[100:103], v[48:51], a[24:27], v[100:103]\n"
"s_add_u32 s74, s72, 4096\n"
"buffer_load_dword v203, v222, s[12:15], 0 offen offset:3456\n"
"v_mfma_f32_16x16x32_bf16 v[104:107], v[48:51], a[28:31], v[104:107]\n"
"s_waitcnt lgkmcnt(4)\n"
"buffer_load_dword v204, v222, s[12:15], s84 offen offset:0\n"
"v_mfma_f32_16x16x32_bf16 v[76:79], v[52:55], a[16:19], v[76:79]\n"
"s_add_u32 s75, s71, 8192\n"
"ds_read_b64_tr_b16 v[48:49], v236 offset:13120\n"
"ds_read_b64_tr_b16 v[50:51], v236 offset:13632\n"
"v_mfma_f32_16x16x32_bf16 v[80:83], v[52:55], a[20:23], v[80:83]\n"
"s_add_u32 s76, s72, 8192\n"
"buffer_load_dword v205, v222, s[12:15], s84 offen offset:1152\n"
"v_mfma_f32_16x16x32_bf16 v[84:87], v[52:55], a[24:27], v[84:87]\n"
"s_add_u32 s77, s71, 12288\n"
"buffer_load_dword v206, v222, s[12:15], s84 offen offset:2304\n"
"v_mfma_f32_16x16x32_bf16 v[88:91], v[52:55], a[28:31], v[88:91]\n"
"s_waitcnt lgkmcnt(4)\n"
"buffer_load_dword v207, v222, s[12:15], s84 offen offset:3456\n"
"v_mfma_f32_16x16x32_bf16 v[108:111], v[56:59], a[16:19], v[108:111]\n"
"s_add_u32 s78, s72, 12288\n"
"ds_read_b64_tr_b16 v[52:53], v237 offset:8768\n"
"ds_read_b64_tr_b16 v[54:55], v237 offset:9280\n"
"v_mfma_f32_16x16x32_bf16 v[112:115], v[56:59], a[20:23], v[112:115]\n"
"s_add_u32 m0, s54, 34816\n"
"s_bfe_u32 s62, s56, 65537\n"
"s_and_b32 s86, s85, s81\n"
"v_mfma_f32_16x16x32_bf16 v[116:119], v[56:59], a[24:27], v[116:119]\n"
"s_add_u32 s68, s68, 294912\n"
"buffer_load_dwordx4 v240, s[20:23], s68 offen offset:0 lds\n"
"v_mfma_f32_16x16x32_bf16 v[120:123], v[56:59], a[28:31], v[120:123]\n"
"s_waitcnt lgkmcnt(4)\n"
"s_add_u32 s62, s62, s61\n"
"s_lshl_b32 s85, s85, 16\n"
"v_mfma_f32_16x16x32_bf16 v[124:127], v[44:47], a[16:19], v[124:127]\n"
"s_add_u32 m0, s54, 35904\n"
"ds_read_b64_tr_b16 v[56:57], v237 offset:13120\n"
"ds_read_b64_tr_b16 v[58:59], v237 offset:13632\n"
"v_mfma_f32_16x16x32_bf16 v[128:131], v[44:47], a[20:23], v[128:131]\n"
"s_add_u32 s62, s62, 16\n"
"buffer_load_dwordx4 v241, s[20:23], s68 offen offset:0 lds\n"
"v_mfma_f32_16x16x32_bf16 v[132:135], v[44:47], a[24:27], v[132:135]\n"
"s_lshl_b32 s62, s62, 12\n"
"s_lshl_b32 s61, s87, 16\n"
"v_and_b32 v251, 63, v0\n"
"v_mfma_f32_16x16x32_bf16 v[136:139], v[44:47], a[28:31], v[136:139]\n"
"s_waitcnt vmcnt(34) lgkmcnt(4)\n"
"s_barrier\n"
"s_add_u32 s79, s62, s63\n"
"v_mfma_f32_16x16x32_bf16 v[156:159], v[48:51], a[16:19], v[156:159]\n"
"s_add_u32 m0, s55, 19200\n"
"ds_read_b64_tr_b16 v[44:45], v236 offset:17408\n"
"ds_read_b64_tr_b16 v[46:47], v236 offset:17920\n"
"v_mfma_f32_16x16x32_bf16 v[160:163], v[48:51], a[20:23], v[160:163]\n"
"s_and_b32 s87, s87, s81\n"
"buffer_load_dwordx4 v238, s[4:7], s64 offen offset:256 lds\n"
"v_mfma_f32_16x16x32_bf16 v[164:167], v[48:51], a[24:27], v[164:167]\n"
"s_bitcmp1_b32 s33, 2\ns_cselect_b32 s87, s87, s61\n"
"ds_read_b128 a[32:35], v7 offset:8704\n"
"ds_read_b128 a[36:39], v7 offset:10880\n"
"v_mfma_f32_16x16x32_bf16 v[168:171], v[48:51], a[28:31], v[168:171]\n"
"s_waitcnt lgkmcnt(6)\n"
"v_lshlrev_b32 v10, 4, v251\n"
"v_lshrrev_b32 v252, 2, v251\n"
"v_mfma_f32_16x16x32_bf16 v[140:143], v[52:55], a[16:19], v[140:143]\n"
"s_add_u32 m0, s55, 20288\n"
"ds_read_b64_tr_b16 v[48:49], v236 offset:21760\n"
"ds_read_b64_tr_b16 v[50:51], v236 offset:22272\n"
"v_mfma_f32_16x16x32_bf16 v[144:147], v[52:55], a[20:23], v[144:147]\n"
"buffer_load_dwordx4 v239, s[4:7], s64 offen offset:256 lds\n"
"v_mfma_f32_16x16x32_bf16 v[148:151], v[52:55], a[24:27], v[148:151]\n"
"ds_read_b128 a[40:43], v7 offset:13056\n"
"ds_read_b128 a[44:47], v7 offset:15232\n"
"v_mfma_f32_16x16x32_bf16 v[152:155], v[52:55], a[28:31], v[152:155]\n"
"s_waitcnt lgkmcnt(8)\n"
"v_and_b32 v253, 3, v251\n"
"v_lshrrev_b32 v254, 4, v251\n"
"v_mfma_f32_16x16x32_bf16 v[172:175], v[56:59], a[16:19], v[172:175]\n"
"s_add_u32 m0, s55, 21376\n"
"ds_read_b64_tr_b16 v[52:53], v237 offset:17408\n"
"ds_read_b64_tr_b16 v[54:55], v237 offset:17920\n"
"v_mfma_f32_16x16x32_bf16 v[176:179], v[56:59], a[20:23], v[176:179]\n"
"buffer_load_dwordx4 v238, s[4:7], s65 offen offset:256 lds\n"
"v_mfma_f32_16x16x32_bf16 v[180:183], v[56:59], a[24:27], v[180:183]\n"
"v_and_b32 v255, 2, v254\n"
"v_mul_u32_u24 v9, 9216, v252\n"
"v_mfma_f32_16x16x32_bf16 v[184:187], v[56:59], a[28:31], v[184:187]\n"
"s_waitcnt lgkmcnt(2)\n"
"v_xor_b32 v255, v253, v255\n"
"v_and_b32 v252, 3, v252\n"
"v_mfma_f32_16x16x32_bf16 v[60:63], v[44:47], a[32:35], v[60:63]\n"
"s_add_u32 m0, s55, 22464\n"
"ds_read_b64_tr_b16 v[56:57], v237 offset:21760\n"
"ds_read_b64_tr_b16 v[58:59], v237 offset:22272\n"
"v_mfma_f32_16x16x32_bf16 v[64:67], v[44:47], a[36:39], v[64:67]\n"
"s_waitcnt vmcnt(25)\n"
"buffer_load_dwordx4 v239, s[4:7], s65 offen offset:256 lds\n"
"v_mfma_f32_16x16x32_bf16 v[68:71], v[44:47], a[40:43], v[68:71]\n"
"ds_read_b128 a[48:51], v7 offset:8768\n"
"ds_read_b128 a[52:55], v7 offset:10944\n"
"v_dot2_f32_bf16 v246, v12, v22, 0\n"
"v_mfma_f32_16x16x32_bf16 v[72:75], v[44:47], a[44:47], v[72:75]\n"
"v_lshl_add_u32 v9, v255, 4, v9\n"
"v_mov_b32_dpp v33, v34 wave_shl:1 row_mask:0xf bank_mask:0xf\n"
"v_dot2_f32_bf16 v247, v13, v23, 0\n"
"v_mfma_f32_16x16x32_bf16 v[92:95], v[48:51], a[32:35], v[92:95]\n"
"s_add_u32 m0, s55, 23552\n"
"ds_read_b64_tr_b16 v[44:45], v236 offset:17472\n"
"ds_read_b64_tr_b16 v[46:47], v236 offset:17984\n"
"v_mov_b32_dpp v32, v33 wave_shl:1 row_mask:0xf bank_mask:0xf\n"
"v_mfma_f32_16x16x32_bf16 v[96:99], v[48:51], a[36:39], v[96:99]\n"
"buffer_load_dwordx4 v238, s[4:7], s66 offen offset:256 lds\n"
"v_dot2_f32_bf16 v246, v14, v24, v246\n"
"v_mfma_f32_16x16x32_bf16 v[100:103], v[48:51], a[40:43], v[100:103]\n"
"ds_read_b128 a[56:59], v7 offset:13120\n"
"ds_read_b128 a[60:63], v7 offset:15296\n"
"v_mov_b32_dpp v32, v31 wave_shr:1 row_mask:0xf bank_mask:0xf\n"
"v_mfma_f32_16x16x32_bf16 v[104:107], v[48:51], a[44:47], v[104:107]\n"
"s_waitcnt lgkmcnt(8)\n"
"v_lshl_add_u32 v255, v254, 3, v252\n"
"v_add_u32 v8, 65536, v236\n"
"v_dot2_f32_bf16 v247, v15, v25, v247\n"
"v_mfma_f32_16x16x32_bf16 v[76:79], v[52:55], a[32:35], v[76:79]\n"
"s_add_u32 m0, s55, 24640\n"
"ds_read_b64_tr_b16 v[48:49], v236 offset:21824\n"
"ds_read_b64_tr_b16 v[50:51], v236 offset:22336\n"
"v_mov_b32_dpp v33, v32 wave_shr:1 row_mask:0xf bank_mask:0xf\n"
"v_mfma_f32_16x16x32_bf16 v[80:83], v[52:55], a[36:39], v[80:83]\n"
"s_lshl_b32 s98, s33, 9\n"
"buffer_load_dwordx4 v239, s[4:7], s66 offen offset:256 lds\n"
"v_dot2_f32_bf16 v246, v16, v26, v246\n"
"v_mfma_f32_16x16x32_bf16 v[84:87], v[52:55], a[40:43], v[84:87]\n"
"s_add_u32 s98, s98, 266240\n"
"v_lshlrev_b32 v255, 6, v255\n"
"v_and_b32 v254, 1, v254\n"
"v_lshlrev_b32 v212, 16, v36\n"
"v_mfma_f32_16x16x32_bf16 v[88:91], v[52:55], a[44:47], v[88:91]\n"
"s_waitcnt lgkmcnt(2)\n"
"v_lshl_add_u32 v255, v253, 3, v255\n"
"v_dot2_f32_bf16 v247, v17, v27, v247\n"
"v_and_b32 v214, s81, v36\n"
"v_mfma_f32_16x16x32_bf16 v[108:111], v[56:59], a[32:35], v[108:111]\n"
"s_add_u32 m0, s55, 25728\n"
"ds_read_b64_tr_b16 v[52:53], v237 offset:17472\n"
"ds_read_b64_tr_b16 v[54:55], v237 offset:17984\n"
"v_dot2_f32_bf16 v246, v18, v28, v246\n"
"v_mfma_f32_16x16x32_bf16 v[112:115], v[56:59], a[36:39], v[112:115]\n"
"buffer_load_dwordx4 v238, s[4:7], s67 offen offset:256 lds\n"
"v_lshlrev_b32 v216, 16, v37\n"
"v_mfma_f32_16x16x32_bf16 v[116:119], v[56:59], a[40:43], v[116:119]\n"
"v_lshl_add_u32 v1, v254, 5, v255\n"
"v_dot2_f32_bf16 v247, v19, v29, v247\n"
"v_and_b32 v218, s81, v37\n"
"v_mfma_f32_16x16x32_bf16 v[120:123], v[56:59], a[44:47], v[120:123]\n"
"v_xor_b32 v4, 32, v1\n"
"v_add_u32 v2, 65536, v1\n"
"v_dot2_f32_bf16 v246, v20, v30, v246\n"
"v_mfma_f32_16x16x32_bf16 v[124:127], v[44:47], a[32:35], v[124:127]\n"
"s_add_u32 m0, s55, 26816\n"
"ds_read_b64_tr_b16 v[56:57], v237 offset:21824\n"
"ds_read_b64_tr_b16 v[58:59], v237 offset:22336\n"
"v_lshlrev_b32 v213, 16, v38\n"
"v_mfma_f32_16x16x32_bf16 v[128:131], v[44:47], a[36:39], v[128:131]\n"
"buffer_load_dwordx4 v239, s[4:7], s67 offen offset:256 lds\n"
"s_nop 2\n"
"v_mfma_f32_16x16x32_bf16 v[132:135], v[44:47], a[40:43], v[132:135]\n"
"v_add_u32 v3, 131072, v1\n"
"v_add_u32 v5, 65536, v4\n"
"v_and_b32 v215, s81, v38\n"
"v_mfma_f32_16x16x32_bf16 v[136:139], v[44:47], a[44:47], v[136:139]\n"
"s_waitcnt lgkmcnt(4)\n"
"s_barrier\n"
"v_add_u32 v6, 131072, v4\n"
"s_nop 0\n"
"v_mfma_f32_16x16x32_bf16 v[156:159], v[48:51], a[32:35], v[156:159]\n"
"s_add_u32 m0, s54, 43520\n"
"ds_read_b64_tr_b16 v[44:45], v236 offset:26112\n"
"ds_read_b64_tr_b16 v[46:47], v236 offset:26624\n"
"v_lshlrev_b32 v217, 16, v39\n"
"v_mfma_f32_16x16x32_bf16 v[160:163], v[48:51], a[36:39], v[160:163]\n"
"s_add_u32 s68, s68, 294912\n"
"buffer_load_dwordx4 v240, s[20:23], s68 offen offset:0 lds\n"
"v_add_f32 v246, v246, v247\n"
"v_mfma_f32_16x16x32_bf16 v[164:167], v[48:51], a[40:43], v[164:167]\n"
"s_mul_i32 s61, s33, 36864\n"
"v_mov_b32 v252, -1\n"
"v_mov_b32 v253, -1\n"
"v_and_b32 v219, s81, v39\n"
"v_mfma_f32_16x16x32_bf16 v[168:171], v[48:51], a[44:47], v[168:171]\n"
"s_waitcnt lgkmcnt(4)\n"
"v_mov_b32 v254, -1\n"
"v_mov_b32 v255, -1\n"
"v_add_f32_dpp v246, v246, v246 quad_perm:[1,0,3,2] row_mask:0xf bank_mask:0xf\n"
"v_mfma_f32_16x16x32_bf16 v[140:143], v[52:55], a[32:35], v[140:143]\n"
"s_add_u32 m0, s54, 44608\n"
"ds_read_b64_tr_b16 v[48:49], v236 offset:30464\n"
"ds_read_b64_tr_b16 v[50:51], v236 offset:30976\n"
"v_lshlrev_b32 v220, 16, v31\n"
"v_mfma_f32_16x16x32_bf16 v[144:147], v[52:55], a[36:39], v[144:147]\n"
"s_add_u32 s61, s61, s53\n"
"buffer_load_dwordx4 v241, s[20:23], s68 offen offset:0 lds\n"
"v_add_f32_dpp v246, v246, v246 quad_perm:[2,3,0,1] row_mask:0xf bank_mask:0xf\n"
"v_mfma_f32_16x16x32_bf16 v[148:151], v[52:55], a[40:43], v[148:151]\n"
"s_add_u32 s61, s61, 19271680\n"
"s_xor_b32 s62, s58, 1\n"
"v_and_b32 v221, s81, v31\n"
"v_add_f32_dpp v246, v246, v246 row_half_mirror row_mask:0xf bank_mask:0xf\n"
"v_mfma_f32_16x16x32_bf16 v[152:155], v[52:55], a[44:47], v[152:155]\n"
"s_waitcnt lgkmcnt(4)\n"
"s_add_u32 s27, s61, 32768\n"
"s_lshl_b32 s62, s62, 13\n"
"v_lshlrev_b32 v222, 16, v32\n"
"v_mfma_f32_16x16x32_bf16 v[172:175], v[56:59], a[32:35], v[172:175]\n"
"s_add_u32 m0, s54, 0\n"
"ds_read_b64_tr_b16 v[52:53], v237 offset:26112\n"
"ds_read_b64_tr_b16 v[54:55], v237 offset:26624\n"
"v_add_f32_dpp v246, v246, v246 row_mirror row_mask:0xf bank_mask:0xf\n"
"v_mfma_f32_16x16x32_bf16 v[176:179], v[56:59], a[36:39], v[176:179]\n"
"s_add_u32 s68, s68, s92\n"
"buffer_load_dwordx4 v240, s[20:23], s68 offen offset:0 lds\n"
"v_and_b32 v223, s81, v32\n"
"v_mfma_f32_16x16x32_bf16 v[180:183], v[56:59], a[40:43], v[180:183]\n"
"s_add_u32 s24, s62, s61\n"
"v_mov_b32 v247, v246\n"
"v_lshlrev_b32 v224, 16, v33\n"
"v_and_b32 v225, s81, v33\n"
"v_mfma_f32_16x16x32_bf16 v[184:187], v[56:59], a[44:47], v[184:187]\n"
"s_waitcnt lgkmcnt(4)\n"
"s_xor_b32 s62, s58, 2\n"
"v_permlane16_swap_b32 v246, v247\n"
"v_lshlrev_b32 v226, 16, v34\n"
"v_mfma_f32_16x16x32_bf16 v[60:63], v[44:47], a[48:51], v[60:63]\n"
"s_add_u32 m0, s54, 1088\n"
"ds_read_b64_tr_b16 v[56:57], v237 offset:30464\n"
"ds_read_b64_tr_b16 v[58:59], v237 offset:30976\n"
"v_add_f32 v246, v246, v247\n"
"v_mfma_f32_16x16x32_bf16 v[64:67], v[44:47], a[52:55], v[64:67]\n"
"s_lshl_b32 s62, s62, 13\n"
"buffer_load_dwordx4 v241, s[20:23], s68 offen offset:0 lds\n"
"v_mul_f32 v246, s80, v246\n"
"v_mfma_f32_16x16x32_bf16 v[68:71], v[44:47], a[56:59], v[68:71]\n"
"s_add_u32 s25, s62, s61\n"
"v_exp_f32 v246, v246\n"
"v_and_b32 v227, s81, v34\n"
"v_mfma_f32_16x16x32_bf16 v[72:75], v[44:47], a[60:63], v[72:75]\n"
"s_waitcnt lgkmcnt(4)\n"
"s_xor_b32 s62, s58, 3\n"
"v_lshlrev_b32 v228, 16, v35\n"
"v_add_f32 v246, 1.0, v246\n"
"v_mfma_f32_16x16x32_bf16 v[92:95], v[48:51], a[48:51], v[92:95]\n"
"s_add_u32 m0, s55, 2048\n"
"ds_read_b64_tr_b16 v[44:45], v236 offset:26176\n"
"ds_read_b64_tr_b16 v[46:47], v236 offset:26688\n"
"v_and_b32 v229, s81, v35\n"
"v_mfma_f32_16x16x32_bf16 v[96:99], v[48:51], a[52:55], v[96:99]\n"
"s_add_u32 s64, s64, s94\n"
"buffer_load_dwordx4 v238, s[4:7], s64 offen offset:0 lds\n"
"v_mul_f32 v230, v218, v226\n"
"v_mfma_f32_16x16x32_bf16 v[100:103], v[48:51], a[56:59], v[100:103]\n"
"s_lshl_b32 s62, s62, 13\n"
"v_rcp_f32 v246, v246\n"
"v_mul_f32 v231, v219, v227\n"
"v_mfma_f32_16x16x32_bf16 v[104:107], v[48:51], a[60:63], v[104:107]\n"
"s_waitcnt lgkmcnt(4)\n"
"s_add_u32 s26, s62, s61\n"
"v_cvt_pk_bf16_f32 v234, v230, v231\n"
"v_mul_f32 v232, v216, v224\n"
"v_mfma_f32_16x16x32_bf16 v[76:79], v[52:55], a[48:51], v[76:79]\n"
"s_add_u32 m0, s55, 3136\n"
"ds_read_b64_tr_b16 v[48:49], v236 offset:30528\n"
"ds_read_b64_tr_b16 v[50:51], v236 offset:31040\n"
"v_lshlrev_b32 v230, 16, v234\n"
"v_mfma_f32_16x16x32_bf16 v[80:83], v[52:55], a[52:55], v[80:83]\n"
"buffer_load_dwordx4 v239, s[4:7], s64 offen offset:0 lds\n"
"v_and_b32 v231, s81, v234\n"
"v_mfma_f32_16x16x32_bf16 v[84:87], v[52:55], a[56:59], v[84:87]\n"
"v_mov_b32 v242, s34\n"
"v_mov_b32 v243, s35\n"
"v_mul_f32 v233, v217, v225\n"
"v_mfma_f32_16x16x32_bf16 v[88:91], v[52:55], a[60:63], v[88:91]\n"
"s_waitcnt lgkmcnt(4)\n"
"v_lshrrev_b32 v244, 4, v0\n"
"v_cvt_pk_bf16_f32 v234, v232, v233\n"
"v_mfma_f32_16x16x32_bf16 v[108:111], v[56:59], a[48:51], v[108:111]\n"
"s_add_u32 m0, s55, 4224\n"
"ds_read_b64_tr_b16 v[52:53], v237 offset:26176\n"
"ds_read_b64_tr_b16 v[54:55], v237 offset:26688\n"
"v_lshlrev_b32 v232, 16, v234\n"
"v_mfma_f32_16x16x32_bf16 v[112:115], v[56:59], a[52:55], v[112:115]\n"
"s_add_u32 s65, s65, s94\n"
"buffer_load_dwordx4 v238, s[4:7], s65 offen offset:0 lds\n"
"v_and_b32 v233, s81, v234\n"
"v_mfma_f32_16x16x32_bf16 v[116:119], v[56:59], a[56:59], v[116:119]\n"
"v_min_u32 v244, 1, v244\n"
"v_add_f32 v230, v230, v232\n"
"v_add_f32 v231, v231, v233\n"
"v_mfma_f32_16x16x32_bf16 v[120:123], v[56:59], a[60:63], v[120:123]\n"
"s_waitcnt lgkmcnt(4)\n"
"v_lshlrev_b32 v244, 27, v244\n"
"v_cvt_pk_bf16_f32 v234, v230, v231\n"
"v_mul_f32 v232, v214, v222\n"
"v_mfma_f32_16x16x32_bf16 v[124:127], v[44:47], a[48:51], v[124:127]\n"
"s_add_u32 m0, s55, 5312\n"
"ds_read_b64_tr_b16 v[56:57], v237 offset:30528\n"
"ds_read_b64_tr_b16 v[58:59], v237 offset:31040\n"
"v_lshlrev_b32 v230, 16, v234\n"
"v_mfma_f32_16x16x32_bf16 v[128:131], v[44:47], a[52:55], v[128:131]\n"
"buffer_load_dwordx4 v239, s[4:7], s65 offen offset:0 lds\n"
"v_and_b32 v231, s81, v234\n"
"v_mfma_f32_16x16x32_bf16 v[132:135], v[44:47], a[56:59], v[132:135]\n"
"v_add_lshl_u32 v244, v244, v0, 3\n"
"v_mul_f32 v233, v215, v223\n"
"v_mfma_f32_16x16x32_bf16 v[136:139], v[44:47], a[60:63], v[136:139]\n"
"s_waitcnt vmcnt(8) lgkmcnt(4)\n"
"s_barrier\n"
"buffer_store_dword v246, v43, s[28:31], s98 offen offset:0 sc1\n"
"v_mfma_f32_16x16x32_bf16 v[156:159], v[48:51], a[48:51], v[156:159]\n"
"s_add_u32 m0, s55, 6400\n"
"ds_read_b64_tr_b16 v[44:45], v236 offset:34816\n"
"ds_read_b64_tr_b16 v[46:47], v236 offset:35328\n"
"v_cvt_pk_bf16_f32 v234, v232, v233\n"
"v_mfma_f32_16x16x32_bf16 v[160:163], v[48:51], a[52:55], v[160:163]\n"
"s_add_u32 s66, s66, s94\n"
"buffer_load_dwordx4 v238, s[4:7], s66 offen offset:0 lds\n"
"v_lshlrev_b32 v232, 16, v234\n"
"v_mfma_f32_16x16x32_bf16 v[164:167], v[48:51], a[56:59], v[164:167]\n"
"ds_read_b128 a[64:67], v7 offset:17408\n"
"ds_read_b128 a[68:71], v7 offset:19584\n"
"v_and_b32 v233, s81, v234\n"
"v_mfma_f32_16x16x32_bf16 v[168:171], v[48:51], a[60:63], v[168:171]\n"
"s_waitcnt lgkmcnt(6)\n"
"v_add_f32 v230, v230, v232\n"
"v_add_f32 v231, v231, v233\n"
"v_mfma_f32_16x16x32_bf16 v[140:143], v[52:55], a[48:51], v[140:143]\n"
"s_add_u32 m0, s55, 7488\n"
"ds_read_b64_tr_b16 v[48:49], v236 offset:39168\n"
"ds_read_b64_tr_b16 v[50:51], v236 offset:39680\n"
"v_cvt_pk_bf16_f32 v234, v230, v231\n"
"v_mfma_f32_16x16x32_bf16 v[144:147], v[52:55], a[52:55], v[144:147]\n"
"buffer_load_dwordx4 v239, s[4:7], s66 offen offset:0 lds\n"
"v_lshlrev_b32 v230, 16, v234\n"
"v_mfma_f32_16x16x32_bf16 v[148:151], v[52:55], a[56:59], v[148:151]\n"
"ds_read_b128 a[72:75], v7 offset:21760\n"
"ds_read_b128 a[76:79], v7 offset:23936\n"
"v_and_b32 v231, s81, v234\n"
"v_mfma_f32_16x16x32_bf16 v[152:155], v[52:55], a[60:63], v[152:155]\n"
"s_waitcnt lgkmcnt(8)\n"
"v_mul_f32 v232, v212, v220\n"
"v_mul_f32 v233, v213, v221\n"
"v_mfma_f32_16x16x32_bf16 v[172:175], v[56:59], a[48:51], v[172:175]\n"
"s_add_u32 m0, s55, 8576\n"
"ds_read_b64_tr_b16 v[52:53], v237 offset:34816\n"
"ds_read_b64_tr_b16 v[54:55], v237 offset:35328\n"
"v_cvt_pk_bf16_f32 v234, v232, v233\n"
"v_mfma_f32_16x16x32_bf16 v[176:179], v[56:59], a[52:55], v[176:179]\n"
"s_add_u32 s67, s67, s94\n"
"buffer_load_dwordx4 v238, s[4:7], s67 offen offset:0 lds\n"
"v_lshlrev_b32 v232, 16, v234\n"
"v_mfma_f32_16x16x32_bf16 v[180:183], v[56:59], a[56:59], v[180:183]\n"
"v_and_b32 v233, s81, v234\n"
"v_add_f32 v230, v230, v232\n"
"v_mfma_f32_16x16x32_bf16 v[184:187], v[56:59], a[60:63], v[184:187]\n"
"s_waitcnt lgkmcnt(2)\n"
"v_add_f32 v231, v231, v233\n"
"v_mfma_f32_16x16x32_bf16 v[60:63], v[44:47], a[64:67], v[60:63]\n"
"s_add_u32 m0, s55, 9664\n"
"ds_read_b64_tr_b16 v[56:57], v237 offset:39168\n"
"ds_read_b64_tr_b16 v[58:59], v237 offset:39680\n"
"v_cvt_pk_bf16_f32 v234, v230, v231\n"
"v_mfma_f32_16x16x32_bf16 v[64:67], v[44:47], a[68:71], v[64:67]\n"
"buffer_load_dwordx4 v239, s[4:7], s67 offen offset:0 lds\n"
"v_lshlrev_b32 v230, 16, v234\n"
"v_mfma_f32_16x16x32_bf16 v[68:71], v[44:47], a[72:75], v[68:71]\n"
"ds_read_b128 a[80:83], v7 offset:17472\n"
"ds_read_b128 a[84:87], v7 offset:19648\n"
"v_and_b32 v231, s81, v234\n"
"v_mfma_f32_16x16x32_bf16 v[72:75], v[44:47], a[76:79], v[72:75]\n"
"v_mul_f32 v230, v230, v228\n"
"v_mul_f32 v231, v231, v229\n"
"v_mfma_f32_16x16x32_bf16 v[92:95], v[48:51], a[64:67], v[92:95]\n"
"s_add_u32 m0, s54, 8704\n"
"ds_read_b64_tr_b16 v[44:45], v236 offset:34880\n"
"ds_read_b64_tr_b16 v[46:47], v236 offset:35392\n"
"v_cvt_pk_bf16_f32 v228, v230, v231\n"
"v_mfma_f32_16x16x32_bf16 v[96:99], v[48:51], a[68:71], v[96:99]\n"
"s_add_u32 s68, s68, 294912\n"
"buffer_load_dwordx4 v240, s[20:23], s68 offen offset:0 lds\n"
"v_mfma_f32_16x16x32_bf16 v[100:103], v[48:51], a[72:75], v[100:103]\n"
"ds_read_b128 a[88:91], v7 offset:21824\n"
"ds_read_b128 a[92:95], v7 offset:24000\n"
"v_mfma_f32_16x16x32_bf16 v[104:107], v[48:51], a[76:79], v[104:107]\n"
"s_waitcnt lgkmcnt(8)\n"
"buffer_store_short v228, v42, s[28:31], s96 offen offset:0 sc1\n"
"v_mfma_f32_16x16x32_bf16 v[76:79], v[52:55], a[64:67], v[76:79]\n"
"s_add_u32 m0, s54, 9792\n"
"ds_read_b64_tr_b16 v[48:49], v236 offset:39232\n"
"ds_read_b64_tr_b16 v[50:51], v236 offset:39744\n"
"v_mfma_f32_16x16x32_bf16 v[80:83], v[52:55], a[68:71], v[80:83]\n"
"buffer_load_dwordx4 v241, s[20:23], s68 offen offset:0 lds\n"
"v_mfma_f32_16x16x32_bf16 v[84:87], v[52:55], a[72:75], v[84:87]\n"
"buffer_store_short_d16_hi v228, v42, s[28:31], s97 offen offset:0 sc1\n"
"v_mfma_f32_16x16x32_bf16 v[88:91], v[52:55], a[76:79], v[88:91]\n"
"s_waitcnt lgkmcnt(2)\n"
"v_mfma_f32_16x16x32_bf16 v[108:111], v[56:59], a[64:67], v[108:111]\n"
"ds_read_b64_tr_b16 v[52:53], v237 offset:34880\n"
"ds_read_b64_tr_b16 v[54:55], v237 offset:35392\n"
"v_lshlrev_b32 v212, 16, v208\n"
"v_mfma_f32_16x16x32_bf16 v[112:115], v[56:59], a[68:71], v[112:115]\n"
"buffer_store_dwordx4 v[252:255], v10, s[28:31], s24 offen offset:0 sc1\n"
"v_and_b32 v214, s81, v208\n"
"v_mfma_f32_16x16x32_bf16 v[116:119], v[56:59], a[72:75], v[116:119]\n"
"v_lshlrev_b32 v216, 16, v209\n"
"v_and_b32 v218, s81, v209\n"
"v_lshlrev_b32 v213, 16, v210\n"
"v_mfma_f32_16x16x32_bf16 v[120:123], v[56:59], a[76:79], v[120:123]\n"
"v_and_b32 v215, s81, v210\n"
"v_lshlrev_b32 v217, 16, v211\n"
"v_and_b32 v219, s81, v211\n"
"v_mfma_f32_16x16x32_bf16 v[124:127], v[44:47], a[64:67], v[124:127]\n"
"s_add_u32 m0, s54, 17408\n"
"ds_read_b64_tr_b16 v[56:57], v237 offset:39232\n"
"ds_read_b64_tr_b16 v[58:59], v237 offset:39744\n"
"v_lshlrev_b32 v220, 16, v188\n"
"v_mfma_f32_16x16x32_bf16 v[128:131], v[44:47], a[68:71], v[128:131]\n"
"s_add_u32 s68, s68, 294912\n"
"buffer_load_dwordx4 v240, s[20:23], s68 offen offset:0 lds\n"
"v_and_b32 v221, s81, v188\n"
"v_mfma_f32_16x16x32_bf16 v[132:135], v[44:47], a[72:75], v[132:135]\n"
"v_lshlrev_b32 v222, 16, v189\n"
"v_and_b32 v223, s81, v189\n"
"v_lshlrev_b32 v224, 16, v190\n"
"v_mfma_f32_16x16x32_bf16 v[136:139], v[44:47], a[76:79], v[136:139]\n"
"s_waitcnt vmcnt(17) lgkmcnt(4)\n"
"s_barrier\n"
"v_and_b32 v225, s81, v190\n"
"v_lshlrev_b32 v226, 16, v191\n"
"v_mfma_f32_16x16x32_bf16 v[156:159], v[48:51], a[64:67], v[156:159]\n"
"s_add_u32 m0, s54, 18496\n"
"ds_read_b64_tr_b16 v[44:45], v236 offset:43520\n"
"ds_read_b64_tr_b16 v[46:47], v236 offset:44032\n"
"v_and_b32 v227, s81, v191\n"
"v_mfma_f32_16x16x32_bf16 v[160:163], v[48:51], a[68:71], v[160:163]\n"
"buffer_load_dwordx4 v241, s[20:23], s68 offen offset:0 lds\n"
"v_lshlrev_b32 v232, 16, v200\n"
"v_mfma_f32_16x16x32_bf16 v[164:167], v[48:51], a[72:75], v[164:167]\n"
"v_and_b32 v233, s81, v200\n"
"v_mul_f32 v12, v218, v220\n"
"v_mul_f32 v13, v219, v221\n"
"v_mfma_f32_16x16x32_bf16 v[168:171], v[48:51], a[76:79], v[168:171]\n"
"s_waitcnt lgkmcnt(4)\n"
"v_cvt_pk_bf16_f32 v30, v12, v13\n"
"v_mul_f32 v28, v216, v222\n"
"v_mul_f32 v29, v217, v223\n"
"v_mfma_f32_16x16x32_bf16 v[140:143], v[52:55], a[64:67], v[140:143]\n"
"s_add_u32 m0, s55, 10624\n"
"ds_read_b64_tr_b16 v[48:49], v236 offset:47872\n"
"ds_read_b64_tr_b16 v[50:51], v236 offset:48384\n"
"v_lshlrev_b32 v12, 16, v30\n"
"v_mfma_f32_16x16x32_bf16 v[144:147], v[52:55], a[68:71], v[144:147]\n"
"buffer_load_dwordx4 v238, s[4:7], s64 offen offset:128 lds\n"
"v_and_b32 v13, s81, v30\n"
"v_mfma_f32_16x16x32_bf16 v[148:151], v[52:55], a[72:75], v[148:151]\n"
"v_cvt_pk_bf16_f32 v30, v28, v29\n"
"v_lshlrev_b32 v228, 16, v192\n"
"v_and_b32 v229, s81, v192\n"
"v_mfma_f32_16x16x32_bf16 v[152:155], v[52:55], a[76:79], v[152:155]\n"
"s_waitcnt lgkmcnt(4)\n"
"v_lshlrev_b32 v28, 16, v30\n"
"v_and_b32 v29, s81, v30\n"
"v_lshlrev_b32 v234, 16, v201\n"
"v_mfma_f32_16x16x32_bf16 v[172:175], v[56:59], a[64:67], v[172:175]\n"
"s_add_u32 m0, s55, 11712\n"
"ds_read_b64_tr_b16 v[52:53], v237 offset:43520\n"
"ds_read_b64_tr_b16 v[54:55], v237 offset:44032\n"
"v_add_f32 v12, v12, v28\n"
"v_mfma_f32_16x16x32_bf16 v[176:179], v[56:59], a[68:71], v[176:179]\n"
"buffer_load_dwordx4 v239, s[4:7], s64 offen offset:128 lds\n"
"v_add_f32 v13, v13, v29\n"
"v_mfma_f32_16x16x32_bf16 v[180:183], v[56:59], a[72:75], v[180:183]\n"
"v_cvt_pk_bf16_f32 v30, v12, v13\n"
"v_mul_f32 v28, v214, v224\n"
"v_mul_f32 v29, v215, v225\n"
"v_mfma_f32_16x16x32_bf16 v[184:187], v[56:59], a[76:79], v[184:187]\n"
"s_waitcnt lgkmcnt(4)\n"
"v_lshlrev_b32 v12, 16, v30\n"
"v_and_b32 v13, s81, v30\n"
"v_and_b32 v235, s81, v201\n"
"v_mfma_f32_16x16x32_bf16 v[60:63], v[44:47], a[80:83], v[60:63]\n"
"s_add_u32 m0, s55, 12800\n"
"ds_read_b64_tr_b16 v[56:57], v237 offset:47872\n"
"ds_read_b64_tr_b16 v[58:59], v237 offset:48384\n"
"v_cvt_pk_bf16_f32 v30, v28, v29\n"
"v_mfma_f32_16x16x32_bf16 v[64:67], v[44:47], a[84:87], v[64:67]\n"
"buffer_load_dwordx4 v238, s[4:7], s65 offen offset:128 lds\n"
"v_lshlrev_b32 v28, 16, v30\n"
"v_mfma_f32_16x16x32_bf16 v[68:71], v[44:47], a[88:91], v[68:71]\n"
"v_and_b32 v29, s81, v30\n"
"v_add_f32 v12, v12, v28\n"
"v_mul_f32 v14, v218, v222\n"
"v_mfma_f32_16x16x32_bf16 v[72:75], v[44:47], a[92:95], v[72:75]\n"
"s_waitcnt lgkmcnt(4)\n"
"v_add_f32 v13, v13, v29\n"
"v_mul_f32 v28, v212, v226\n"
"v_mul_f32 v15, v219, v223\n"
"v_mfma_f32_16x16x32_bf16 v[92:95], v[48:51], a[80:83], v[92:95]\n"
"s_add_u32 m0, s55, 13888\n"
"ds_read_b64_tr_b16 v[44:45], v236 offset:43584\n"
"ds_read_b64_tr_b16 v[46:47], v236 offset:44096\n"
"v_cvt_pk_bf16_f32 v30, v12, v13\n"
"v_mfma_f32_16x16x32_bf16 v[96:99], v[48:51], a[84:87], v[96:99]\n"
"buffer_load_dwordx4 v239, s[4:7], s65 offen offset:128 lds\n"
"v_lshlrev_b32 v12, 16, v30\n"
"v_mfma_f32_16x16x32_bf16 v[100:103], v[48:51], a[88:91], v[100:103]\n"
"v_and_b32 v13, s81, v30\n"
"v_mul_f32 v29, v213, v227\n"
"v_cvt_pk_bf16_f32 v33, v14, v15\n"
"v_mfma_f32_16x16x32_bf16 v[104:107], v[48:51], a[92:95], v[104:107]\n"
"s_waitcnt lgkmcnt(4)\n"
"v_cvt_pk_bf16_f32 v30, v28, v29\n"
"v_lshlrev_b32 v14, 16, v33\n"
"v_and_b32 v15, s81, v33\n"
"v_mfma_f32_16x16x32_bf16 v[76:79], v[52:55], a[80:83], v[76:79]\n"
"s_add_u32 m0, s55, 14976\n"
"ds_read_b64_tr_b16 v[48:49], v236 offset:47936\n"
"ds_read_b64_tr_b16 v[50:51], v236 offset:48448\n"
"v_lshlrev_b32 v28, 16, v30\n"
"v_mfma_f32_16x16x32_bf16 v[80:83], v[52:55], a[84:87], v[80:83]\n"
"buffer_load_dwordx4 v238, s[4:7], s66 offen offset:128 lds\n"
"v_and_b32 v29, s81, v30\n"
"v_mfma_f32_16x16x32_bf16 v[84:87], v[52:55], a[88:91], v[84:87]\n"
"v_add_f32 v12, v12, v28\n"
"v_add_f32 v13, v13, v29\n"
"v_mul_f32 v31, v216, v224\n"
"v_mfma_f32_16x16x32_bf16 v[88:91], v[52:55], a[92:95], v[88:91]\n"
"s_waitcnt lgkmcnt(4)\n"
"v_cvt_pk_bf16_f32 v30, v12, v13\n"
"v_mul_f32 v32, v217, v225\n"
"v_lshlrev_b32 v230, 16, v193\n"
"v_mfma_f32_16x16x32_bf16 v[108:111], v[56:59], a[80:83], v[108:111]\n"
"s_add_u32 m0, s55, 16064\n"
"ds_read_b64_tr_b16 v[52:53], v237 offset:43584\n"
"ds_read_b64_tr_b16 v[54:55], v237 offset:44096\n"
"v_lshlrev_b32 v12, 16, v30\n"
"v_mfma_f32_16x16x32_bf16 v[112:115], v[56:59], a[84:87], v[112:115]\n"
"buffer_load_dwordx4 v239, s[4:7], s66 offen offset:128 lds\n"
"v_and_b32 v13, s81, v30\n"
"v_mfma_f32_16x16x32_bf16 v[116:119], v[56:59], a[88:91], v[116:119]\n"
"v_mul_f32 v12, v12, v232\n"
"v_mul_f32 v13, v13, v233\n"
"v_cvt_pk_bf16_f32 v33, v31, v32\n"
"v_mfma_f32_16x16x32_bf16 v[120:123], v[56:59], a[92:95], v[120:123]\n"
"s_waitcnt lgkmcnt(4)\n"
"v_lshlrev_b32 v31, 16, v33\n"
"v_and_b32 v32, s81, v33\n"
"v_and_b32 v231, s81, v193\n"
"v_mfma_f32_16x16x32_bf16 v[124:127], v[44:47], a[80:83], v[124:127]\n"
"s_add_u32 m0, s55, 17152\n"
"ds_read_b64_tr_b16 v[56:57], v237 offset:47936\n"
"ds_read_b64_tr_b16 v[58:59], v237 offset:48448\n"
"v_add_f32 v14, v14, v31\n"
"v_mfma_f32_16x16x32_bf16 v[128:131], v[44:47], a[84:87], v[128:131]\n"
"buffer_load_dwordx4 v238, s[4:7], s67 offen offset:128 lds\n"
"v_add_f32 v15, v15, v32\n"
"v_mfma_f32_16x16x32_bf16 v[132:135], v[44:47], a[88:91], v[132:135]\n"
"v_cvt_pk_bf16_f32 v33, v14, v15\n"
"v_mul_f32 v31, v214, v226\n"
"v_mul_f32 v32, v215, v227\n"
"v_mfma_f32_16x16x32_bf16 v[136:139], v[44:47], a[92:95], v[136:139]\n"
"s_waitcnt vmcnt(14) lgkmcnt(4)\n"
"s_barrier\n"
"v_lshlrev_b32 v14, 16, v33\n"
"v_and_b32 v15, s81, v33\n"
"v_mfma_f32_16x16x32_bf16 v[156:159], v[48:51], a[80:83], v[156:159]\n"
"s_add_u32 m0, s55, 18240\n"
"ds_read_b64_tr_b16 v[44:45], v236 offset:0\n"
"ds_read_b64_tr_b16 v[46:47], v236 offset:512\n"
"v_cvt_pk_bf16_f32 v33, v31, v32\n"
"v_mfma_f32_16x16x32_bf16 v[160:163], v[48:51], a[84:87], v[160:163]\n"
"buffer_load_dwordx4 v239, s[4:7], s67 offen offset:128 lds\n"
"v_lshlrev_b32 v31, 16, v33\n"
"v_mfma_f32_16x16x32_bf16 v[164:167], v[48:51], a[88:91], v[164:167]\n"
"ds_read_b128 a[96:99], v7 offset:0\n"
"ds_read_b128 a[100:103], v7 offset:2176\n"
"v_and_b32 v32, s81, v33\n"
"v_mfma_f32_16x16x32_bf16 v[168:171], v[48:51], a[92:95], v[168:171]\n"
"s_waitcnt lgkmcnt(6)\n"
"v_add_f32 v14, v14, v31\n"
"v_add_f32 v15, v15, v32\n"
"v_lshlrev_b32 v232, 16, v202\n"
"v_mfma_f32_16x16x32_bf16 v[140:143], v[52:55], a[80:83], v[140:143]\n"
"ds_read_b64_tr_b16 v[48:49], v236 offset:4352\n"
"ds_read_b64_tr_b16 v[50:51], v236 offset:4864\n"
"v_cvt_pk_bf16_f32 v33, v14, v15\n"
"v_mfma_f32_16x16x32_bf16 v[144:147], v[52:55], a[84:87], v[144:147]\n"
"s_add_u32 s24, s24, 4096\n"
"buffer_store_dwordx4 v[252:255], v10, s[28:31], s24 offen offset:0 sc1\n"
"v_lshlrev_b32 v14, 16, v33\n"
"v_mfma_f32_16x16x32_bf16 v[148:151], v[52:55], a[88:91], v[148:151]\n"
"ds_read_b128 a[104:107], v7 offset:4352\n"
"ds_read_b128 a[108:111], v7 offset:6528\n"
"v_and_b32 v15, s81, v33\n"
"v_mfma_f32_16x16x32_bf16 v[152:155], v[52:55], a[92:95], v[152:155]\n"
"s_waitcnt lgkmcnt(8)\n"
"v_mul_f32 v31, v212, v228\n"
"v_mul_f32 v32, v213, v229\n"
"v_and_b32 v233, s81, v202\n"
"v_mfma_f32_16x16x32_bf16 v[172:175], v[56:59], a[80:83], v[172:175]\n"
"s_add_u32 m0, s54, 26112\n"
"ds_read_b64_tr_b16 v[52:53], v237 offset:0\n"
"ds_read_b64_tr_b16 v[54:55], v237 offset:512\n"
"v_cvt_pk_bf16_f32 v33, v31, v32\n"
"v_mfma_f32_16x16x32_bf16 v[176:179], v[56:59], a[84:87], v[176:179]\n"
"s_add_u32 s68, s68, 294912\n"
"buffer_load_dwordx4 v240, s[20:23], s68 offen offset:0 lds\n"
"v_lshlrev_b32 v31, 16, v33\n"
"v_mfma_f32_16x16x32_bf16 v[180:183], v[56:59], a[88:91], v[180:183]\n"
"v_and_b32 v32, s81, v33\n"
"v_add_f32 v14, v14, v31\n"
"v_mul_f32 v16, v218, v224\n"
"v_mfma_f32_16x16x32_bf16 v[184:187], v[56:59], a[92:95], v[184:187]\n"
"s_waitcnt lgkmcnt(2)\n"
"v_add_f32 v15, v15, v32\n"
"v_mul_f32 v17, v219, v225\n"
"v_mul_f32 v34, v216, v226\n"
"v_mfma_f32_16x16x32_bf16 v[60:63], v[44:47], a[96:99], v[60:63]\n"
"s_add_u32 m0, s54, 27200\n"
"ds_read_b64_tr_b16 v[56:57], v237 offset:4352\n"
"ds_read_b64_tr_b16 v[58:59], v237 offset:4864\n"
"v_cvt_pk_bf16_f32 v33, v14, v15\n"
"v_mfma_f32_16x16x32_bf16 v[64:67], v[44:47], a[100:103], v[64:67]\n"
"buffer_load_dwordx4 v241, s[20:23], s68 offen offset:0 lds\n"
"v_lshlrev_b32 v14, 16, v33\n"
"v_mfma_f32_16x16x32_bf16 v[68:71], v[44:47], a[104:107], v[68:71]\n"
"ds_read_b128 a[112:115], v7 offset:64\n"
"ds_read_b128 a[116:119], v7 offset:2240\n"
"v_and_b32 v15, s81, v33\n"
"v_mfma_f32_16x16x32_bf16 v[72:75], v[44:47], a[108:111], v[72:75]\n"
"v_mul_f32 v14, v14, v234\n"
"v_mul_f32 v15, v15, v235\n"
"v_cvt_pk_bf16_f32 v36, v16, v17\n"
"v_mfma_f32_16x16x32_bf16 v[92:95], v[48:51], a[96:99], v[92:95]\n"
"ds_read_b64_tr_b16 v[44:45], v236 offset:64\n"
"ds_read_b64_tr_b16 v[46:47], v236 offset:576\n"
"v_lshlrev_b32 v16, 16, v36\n"
"v_mfma_f32_16x16x32_bf16 v[96:99], v[48:51], a[100:103], v[96:99]\n"
"buffer_store_dwordx4 v[252:255], v10, s[28:31], s25 offen offset:0 sc1\n"
"v_and_b32 v17, s81, v36\n"
"v_mfma_f32_16x16x32_bf16 v[100:103], v[48:51], a[104:107], v[100:103]\n"
"ds_read_b128 a[120:123], v7 offset:4416\n"
"ds_read_b128 a[124:127], v7 offset:6592\n"
"v_mul_f32 v35, v217, v227\n"
"v_mfma_f32_16x16x32_bf16 v[104:107], v[48:51], a[108:111], v[104:107]\n"
"s_waitcnt lgkmcnt(8)\n"
"v_cvt_pk_bf16_f32 v36, v34, v35\n"
"v_lshlrev_b32 v220, 16, v194\n"
"v_and_b32 v221, s81, v194\n"
"v_mfma_f32_16x16x32_bf16 v[76:79], v[52:55], a[96:99], v[76:79]\n"
"s_add_u32 m0, s54, 34816\n"
"ds_read_b64_tr_b16 v[48:49], v236 offset:4416\n"
"ds_read_b64_tr_b16 v[50:51], v236 offset:4928\n"
"v_lshlrev_b32 v34, 16, v36\n"
"v_mfma_f32_16x16x32_bf16 v[80:83], v[52:55], a[100:103], v[80:83]\n"
"s_add_u32 s68, s68, 294912\n"
"buffer_load_dwordx4 v240, s[20:23], s68 offen offset:0 lds\n"
"v_and_b32 v35, s81, v36\n"
"v_mfma_f32_16x16x32_bf16 v[84:87], v[52:55], a[104:107], v[84:87]\n"
"v_add_f32 v16, v16, v34\n"
"v_add_f32 v17, v17, v35\n"
"v_lshlrev_b32 v234, 16, v203\n"
"v_mfma_f32_16x16x32_bf16 v[88:91], v[52:55], a[108:111], v[88:91]\n"
"s_waitcnt lgkmcnt(2)\n"
"v_cvt_pk_bf16_f32 v36, v16, v17\n"
"v_mul_f32 v34, v214, v228\n"
"v_mul_f32 v35, v215, v229\n"
"v_mfma_f32_16x16x32_bf16 v[108:111], v[56:59], a[96:99], v[108:111]\n"
"s_add_u32 m0, s54, 35904\n"
"ds_read_b64_tr_b16 v[52:53], v237 offset:64\n"
"ds_read_b64_tr_b16 v[54:55], v237 offset:576\n"
"v_lshlrev_b32 v16, 16, v36\n"
"v_mfma_f32_16x16x32_bf16 v[112:115], v[56:59], a[100:103], v[112:115]\n"
"buffer_load_dwordx4 v241, s[20:23], s68 offen offset:0 lds\n"
"v_and_b32 v17, s81, v36\n"
"v_mfma_f32_16x16x32_bf16 v[116:119], v[56:59], a[104:107], v[116:119]\n"
"v_cvt_pk_bf16_f32 v36, v34, v35\n"
"v_and_b32 v235, s81, v203\n"
"v_mul_f32 v18, v218, v226\n"
"v_mfma_f32_16x16x32_bf16 v[120:123], v[56:59], a[108:111], v[120:123]\n"
"v_lshlrev_b32 v34, 16, v36\n"
"v_and_b32 v35, s81, v36\n"
"v_mul_f32 v19, v219, v227\n"
"v_mfma_f32_16x16x32_bf16 v[124:127], v[44:47], a[96:99], v[124:127]\n"
"s_add_u32 m0, s55, 19200\n"
"ds_read_b64_tr_b16 v[56:57], v237 offset:4416\n"
"ds_read_b64_tr_b16 v[58:59], v237 offset:4928\n"
"v_add_f32 v16, v16, v34\n"
"v_mfma_f32_16x16x32_bf16 v[128:131], v[44:47], a[100:103], v[128:131]\n"
"buffer_load_dwordx4 v238, s[4:7], s64 offen offset:256 lds\n"
"v_add_f32 v17, v17, v35\n"
"v_mfma_f32_16x16x32_bf16 v[132:135], v[44:47], a[104:107], v[132:135]\n"
"v_cvt_pk_bf16_f32 v36, v16, v17\n"
"v_mul_f32 v34, v212, v230\n"
"v_mul_f32 v35, v213, v231\n"
"v_mfma_f32_16x16x32_bf16 v[136:139], v[44:47], a[108:111], v[136:139]\n"
"s_waitcnt vmcnt(19) lgkmcnt(4)\n"
"s_barrier\n"
"v_lshlrev_b32 v16, 16, v36\n"
"v_and_b32 v17, s81, v36\n"
"v_mfma_f32_16x16x32_bf16 v[156:159], v[48:51], a[96:99], v[156:159]\n"
"s_add_u32 m0, s55, 20288\n"
"ds_read_b64_tr_b16 v[44:45], v236 offset:8704\n"
"ds_read_b64_tr_b16 v[46:47], v236 offset:9216\n"
"v_cvt_pk_bf16_f32 v36, v34, v35\n"
"v_mfma_f32_16x16x32_bf16 v[160:163], v[48:51], a[100:103], v[160:163]\n"
"buffer_load_dwordx4 v239, s[4:7], s64 offen offset:256 lds\n"
"v_lshlrev_b32 v34, 16, v36\n"
"v_mfma_f32_16x16x32_bf16 v[164:167], v[48:51], a[104:107], v[164:167]\n"
"v_and_b32 v35, s81, v36\n"
"v_add_f32 v16, v16, v34\n"
"v_cvt_pk_bf16_f32 v39, v18, v19\n"
"v_mfma_f32_16x16x32_bf16 v[168:171], v[48:51], a[108:111], v[168:171]\n"
"s_waitcnt lgkmcnt(4)\n"
"v_add_f32 v17, v17, v35\n"
"v_lshlrev_b32 v18, 16, v39\n"
"v_and_b32 v19, s81, v39\n"
"v_mfma_f32_16x16x32_bf16 v[140:143], v[52:55], a[96:99], v[140:143]\n"
"s_add_u32 m0, s55, 21376\n"
"ds_read_b64_tr_b16 v[48:49], v236 offset:13056\n"
"ds_read_b64_tr_b16 v[50:51], v236 offset:13568\n"
"v_cvt_pk_bf16_f32 v36, v16, v17\n"
"v_mfma_f32_16x16x32_bf16 v[144:147], v[52:55], a[100:103], v[144:147]\n"
"buffer_load_dwordx4 v238, s[4:7], s65 offen offset:256 lds\n"
"v_lshlrev_b32 v16, 16, v36\n"
"v_mfma_f32_16x16x32_bf16 v[148:151], v[52:55], a[104:107], v[148:151]\n"
"v_and_b32 v17, s81, v36\n"
"v_mul_f32 v16, v16, v232\n"
"v_mul_f32 v37, v216, v228\n"
"v_mfma_f32_16x16x32_bf16 v[152:155], v[52:55], a[108:111], v[152:155]\n"
"s_waitcnt lgkmcnt(4)\n"
"v_mul_f32 v17, v17, v233\n"
"v_mul_f32 v38, v217, v229\n"
"v_lshlrev_b32 v222, 16, v195\n"
"v_mfma_f32_16x16x32_bf16 v[172:175], v[56:59], a[96:99], v[172:175]\n"
"s_add_u32 m0, s55, 22464\n"
"ds_read_b64_tr_b16 v[52:53], v237 offset:8704\n"
"ds_read_b64_tr_b16 v[54:55], v237 offset:9216\n"
"v_cvt_pk_bf16_f32 v39, v37, v38\n"
"v_mfma_f32_16x16x32_bf16 v[176:179], v[56:59], a[100:103], v[176:179]\n"
"buffer_load_dwordx4 v239, s[4:7], s65 offen offset:256 lds\n"
"v_lshlrev_b32 v37, 16, v39\n"
"v_mfma_f32_16x16x32_bf16 v[180:183], v[56:59], a[104:107], v[180:183]\n"
"v_and_b32 v38, s81, v39\n"
"v_add_f32 v18, v18, v37\n"
"v_and_b32 v223, s81, v195\n"
"v_mfma_f32_16x16x32_bf16 v[184:187], v[56:59], a[108:111], v[184:187]\n"
"s_waitcnt lgkmcnt(4)\n"
"v_add_f32 v19, v19, v38\n"
"v_mul_f32 v37, v214, v230\n"
"v_lshlrev_b32 v232, 16, v204\n"
"v_mfma_f32_16x16x32_bf16 v[60:63], v[44:47], a[112:115], v[60:63]\n"
"s_add_u32 m0, s55, 23552\n"
"ds_read_b64_tr_b16 v[56:57], v237 offset:13056\n"
"ds_read_b64_tr_b16 v[58:59], v237 offset:13568\n"
"v_cvt_pk_bf16_f32 v39, v18, v19\n"
"v_mfma_f32_16x16x32_bf16 v[64:67], v[44:47], a[116:119], v[64:67]\n"
"buffer_load_dwordx4 v238, s[4:7], s66 offen offset:256 lds\n"
"v_lshlrev_b32 v18, 16, v39\n"
"v_mfma_f32_16x16x32_bf16 v[68:71], v[44:47], a[120:123], v[68:71]\n"
"v_and_b32 v19, s81, v39\n"
"v_mul_f32 v38, v215, v231\n"
"v_and_b32 v233, s81, v204\n"
"v_mfma_f32_16x16x32_bf16 v[72:75], v[44:47], a[124:127], v[72:75]\n"
"s_waitcnt lgkmcnt(4)\n"
"v_cvt_pk_bf16_f32 v39, v37, v38\n"
"v_mul_f32 v20, v218, v228\n"
"v_mul_f32 v21, v219, v229\n"
"v_mfma_f32_16x16x32_bf16 v[92:95], v[48:51], a[112:115], v[92:95]\n"
"s_add_u32 m0, s55, 24640\n"
"ds_read_b64_tr_b16 v[44:45], v236 offset:8768\n"
"ds_read_b64_tr_b16 v[46:47], v236 offset:9280\n"
"v_lshlrev_b32 v37, 16, v39\n"
"v_mfma_f32_16x16x32_bf16 v[96:99], v[48:51], a[116:119], v[96:99]\n"
"buffer_load_dwordx4 v239, s[4:7], s66 offen offset:256 lds\n"
"v_and_b32 v38, s81, v39\n"
"v_mfma_f32_16x16x32_bf16 v[100:103], v[48:51], a[120:123], v[100:103]\n"
"v_add_f32 v18, v18, v37\n"
"v_add_f32 v19, v19, v38\n"
"v_cvt_pk_bf16_f32 v30, v20, v21\n"
"v_mfma_f32_16x16x32_bf16 v[104:107], v[48:51], a[124:127], v[104:107]\n"
"s_waitcnt lgkmcnt(4)\n"
"v_cvt_pk_bf16_f32 v39, v18, v19\n"
"v_mul_f32 v37, v212, v220\n"
"v_mul_f32 v38, v213, v221\n"
"v_mfma_f32_16x16x32_bf16 v[76:79], v[52:55], a[112:115], v[76:79]\n"
"s_add_u32 m0, s55, 25728\n"
"ds_read_b64_tr_b16 v[48:49], v236 offset:13120\n"
"ds_read_b64_tr_b16 v[50:51], v236 offset:13632\n"
"v_lshlrev_b32 v18, 16, v39\n"
"v_mfma_f32_16x16x32_bf16 v[80:83], v[52:55], a[116:119], v[80:83]\n"
"buffer_load_dwordx4 v238, s[4:7], s67 offen offset:256 lds\n"
"v_and_b32 v19, s81, v39\n"
"v_mfma_f32_16x16x32_bf16 v[84:87], v[52:55], a[120:123], v[84:87]\n"
"v_cvt_pk_bf16_f32 v39, v37, v38\n"
"v_lshlrev_b32 v20, 16, v30\n"
"v_and_b32 v21, s81, v30\n"
"v_mfma_f32_16x16x32_bf16 v[88:91], v[52:55], a[124:127], v[88:91]\n"
"s_waitcnt lgkmcnt(4)\n"
"v_lshlrev_b32 v37, 16, v39\n"
"v_and_b32 v38, s81, v39\n"
"v_mul_f32 v28, v216, v230\n"
"v_mfma_f32_16x16x32_bf16 v[108:111], v[56:59], a[112:115], v[108:111]\n"
"s_add_u32 m0, s55, 26816\n"
"ds_read_b64_tr_b16 v[52:53], v237 offset:8768\n"
"ds_read_b64_tr_b16 v[54:55], v237 offset:9280\n"
"v_add_f32 v18, v18, v37\n"
"v_mfma_f32_16x16x32_bf16 v[112:115], v[56:59], a[116:119], v[112:115]\n"
"buffer_load_dwordx4 v239, s[4:7], s67 offen offset:256 lds\n"
"v_add_f32 v19, v19, v38\n"
"v_mfma_f32_16x16x32_bf16 v[116:119], v[56:59], a[120:123], v[116:119]\n"
"v_cvt_pk_bf16_f32 v39, v18, v19\n"
"v_mul_f32 v29, v217, v231\n"
"v_lshlrev_b32 v224, 16, v196\n"
"v_mfma_f32_16x16x32_bf16 v[120:123], v[56:59], a[124:127], v[120:123]\n"
"s_waitcnt lgkmcnt(4)\n"
"v_lshlrev_b32 v18, 16, v39\n"
"v_and_b32 v19, s81, v39\n"
"v_cvt_pk_bf16_f32 v30, v28, v29\n"
"v_mfma_f32_16x16x32_bf16 v[124:127], v[44:47], a[112:115], v[124:127]\n"
"ds_read_b64_tr_b16 v[56:57], v237 offset:13120\n"
"ds_read_b64_tr_b16 v[58:59], v237 offset:13632\n"
"v_mul_f32 v18, v18, v234\n"
"v_mfma_f32_16x16x32_bf16 v[128:131], v[44:47], a[116:119], v[128:131]\n"
"s_add_u32 s25, s25, 4096\n"
"buffer_store_dwordx4 v[252:255], v10, s[28:31], s25 offen offset:0 sc1\n"
"v_mul_f32 v19, v19, v235\n"
"v_mfma_f32_16x16x32_bf16 v[132:135], v[44:47], a[120:123], v[132:135]\n"
"v_lshlrev_b32 v28, 16, v30\n"
"v_and_b32 v29, s81, v30\n"
"v_and_b32 v225, s81, v196\n"
"v_mfma_f32_16x16x32_bf16 v[136:139], v[44:47], a[124:127], v[136:139]\n"
"s_waitcnt vmcnt(15) lgkmcnt(4)\n"
"s_barrier\n"
"v_add_f32 v20, v20, v28\n"
"v_add_f32 v21, v21, v29\n"
"v_mfma_f32_16x16x32_bf16 v[156:159], v[48:51], a[112:115], v[156:159]\n"
"s_add_u32 m0, s54, 43520\n"
"ds_read_b64_tr_b16 v[44:45], v236 offset:17408\n"
"ds_read_b64_tr_b16 v[46:47], v236 offset:17920\n"
"v_cvt_pk_bf16_f32 v30, v20, v21\n"
"v_mfma_f32_16x16x32_bf16 v[160:163], v[48:51], a[116:119], v[160:163]\n"
"s_add_u32 s68, s68, 294912\n"
"buffer_load_dwordx4 v240, s[20:23], s68 offen offset:0 lds\n"
"v_lshlrev_b32 v20, 16, v30\n"
"v_mfma_f32_16x16x32_bf16 v[164:167], v[48:51], a[120:123], v[164:167]\n"
"ds_read_b128 a[128:131], v7 offset:8704\n"
"ds_read_b128 a[132:135], v7 offset:10880\n"
"v_and_b32 v21, s81, v30\n"
"v_mfma_f32_16x16x32_bf16 v[168:171], v[48:51], a[124:127], v[168:171]\n"
"s_waitcnt lgkmcnt(6)\n"
"v_mul_f32 v28, v214, v220\n"
"v_mul_f32 v29, v215, v221\n"
"v_lshlrev_b32 v234, 16, v205\n"
"v_mfma_f32_16x16x32_bf16 v[140:143], v[52:55], a[112:115], v[140:143]\n"
"s_add_u32 m0, s54, 44608\n"
"ds_read_b64_tr_b16 v[48:49], v236 offset:21760\n"
"ds_read_b64_tr_b16 v[50:51], v236 offset:22272\n"
"v_cvt_pk_bf16_f32 v30, v28, v29\n"
"v_mfma_f32_16x16x32_bf16 v[144:147], v[52:55], a[116:119], v[144:147]\n"
"buffer_load_dwordx4 v241, s[20:23], s68 offen offset:0 lds\n"
"v_lshlrev_b32 v28, 16, v30\n"
"v_mfma_f32_16x16x32_bf16 v[148:151], v[52:55], a[120:123], v[148:151]\n"
"ds_read_b128 a[136:139], v7 offset:13056\n"
"ds_read_b128 a[140:143], v7 offset:15232\n"
"v_and_b32 v29, s81, v30\n"
"v_mfma_f32_16x16x32_bf16 v[152:155], v[52:55], a[124:127], v[152:155]\n"
"s_waitcnt lgkmcnt(8)\n"
"v_add_f32 v20, v20, v28\n"
"v_add_f32 v21, v21, v29\n"
"v_and_b32 v235, s81, v205\n"
"v_mfma_f32_16x16x32_bf16 v[172:175], v[56:59], a[112:115], v[172:175]\n"
"ds_read_b64_tr_b16 v[52:53], v237 offset:17408\n"
"ds_read_b64_tr_b16 v[54:55], v237 offset:17920\n"
"v_cvt_pk_bf16_f32 v30, v20, v21\n"
"v_mfma_f32_16x16x32_bf16 v[176:179], v[56:59], a[116:119], v[176:179]\n"
"buffer_store_dwordx4 v[252:255], v10, s[28:31], s26 offen offset:0 sc1\n"
"v_lshlrev_b32 v20, 16, v30\n"
"v_mfma_f32_16x16x32_bf16 v[180:183], v[56:59], a[120:123], v[180:183]\n"
"v_and_b32 v21, s81, v30\n"
"v_mul_f32 v28, v212, v222\n"
"v_mul_f32 v29, v213, v223\n"
"v_mfma_f32_16x16x32_bf16 v[184:187], v[56:59], a[124:127], v[184:187]\n"
"s_waitcnt lgkmcnt(2)\n"
"v_cvt_pk_bf16_f32 v30, v28, v29\n"
"v_mul_f32 v22, v218, v230\n"
"v_mul_f32 v23, v219, v231\n"
"v_mfma_f32_16x16x32_bf16 v[60:63], v[44:47], a[128:131], v[60:63]\n"
"s_add_u32 m0, s54, 0\n"
"ds_read_b64_tr_b16 v[56:57], v237 offset:21760\n"
"ds_read_b64_tr_b16 v[58:59], v237 offset:22272\n"
"v_lshlrev_b32 v28, 16, v30\n"
"v_mfma_f32_16x16x32_bf16 v[64:67], v[44:47], a[132:135], v[64:67]\n"
"s_add_u32 s68, s68, s93\n"
"buffer_load_dwordx4 v240, s[20:23], s68 offen offset:0 lds\n"
"v_and_b32 v29, s81, v30\n"
"v_mfma_f32_16x16x32_bf16 v[68:71], v[44:47], a[136:139], v[68:71]\n"
"ds_read_b128 a[144:147], v7 offset:8768\n"
"ds_read_b128 a[148:151], v7 offset:10944\n"
"v_add_f32 v20, v20, v28\n"
"v_mfma_f32_16x16x32_bf16 v[72:75], v[44:47], a[140:143], v[72:75]\n"
"v_add_f32 v21, v21, v29\n"
"v_cvt_pk_bf16_f32 v33, v22, v23\n"
"v_mul_f32 v31, v216, v220\n"
"v_mfma_f32_16x16x32_bf16 v[92:95], v[48:51], a[128:131], v[92:95]\n"
"s_add_u32 m0, s54, 1088\n"
"ds_read_b64_tr_b16 v[44:45], v236 offset:17472\n"
"ds_read_b64_tr_b16 v[46:47], v236 offset:17984\n"
"v_mfma_f32_16x16x32_bf16 v[96:99], v[48:51], a[132:135], v[96:99]\n"
"buffer_load_dwordx4 v241, s[20:23], s68 offen offset:0 lds\n"
"v_mfma_f32_16x16x32_bf16 v[100:103], v[48:51], a[136:139], v[100:103]\n"
"ds_read_b128 a[152:155], v7 offset:13120\n"
"ds_read_b128 a[156:159], v7 offset:15296\n"
"v_mfma_f32_16x16x32_bf16 v[104:107], v[48:51], a[140:143], v[104:107]\n"
"s_waitcnt lgkmcnt(8)\n"
"v_cvt_pk_bf16_f32 v30, v20, v21\n"
"v_lshlrev_b32 v22, 16, v33\n"
"v_mfma_f32_16x16x32_bf16 v[76:79], v[52:55], a[128:131], v[76:79]\n"
"s_add_u32 m0, s55, 2048\n"
"ds_read_b64_tr_b16 v[48:49], v236 offset:21824\n"
"ds_read_b64_tr_b16 v[50:51], v236 offset:22336\n"
"v_mfma_f32_16x16x32_bf16 v[80:83], v[52:55], a[132:135], v[80:83]\n"
"s_add_u32 s64, s64, s95\n"
"buffer_load_dwordx4 v238, s[4:7], s64 offen offset:0 lds\n"
"v_mfma_f32_16x16x32_bf16 v[84:87], v[52:55], a[136:139], v[84:87]\n"
"v_lshlrev_b32 v20, 16, v30\n"
"v_and_b32 v21, s81, v30\n"
"v_mfma_f32_16x16x32_bf16 v[88:91], v[52:55], a[140:143], v[88:91]\n"
"s_waitcnt lgkmcnt(2)\n"
"v_mul_f32 v20, v20, v232\n"
"v_mul_f32 v21, v21, v233\n"
"v_mfma_f32_16x16x32_bf16 v[108:111], v[56:59], a[128:131], v[108:111]\n"
"s_add_u32 m0, s55, 3136\n"
"ds_read_b64_tr_b16 v[52:53], v237 offset:17472\n"
"ds_read_b64_tr_b16 v[54:55], v237 offset:17984\n"
"v_mfma_f32_16x16x32_bf16 v[112:115], v[56:59], a[132:135], v[112:115]\n"
"buffer_load_dwordx4 v239, s[4:7], s64 offen offset:0 lds\n"
"v_mfma_f32_16x16x32_bf16 v[116:119], v[56:59], a[136:139], v[116:119]\n"
"v_and_b32 v23, s81, v33\n"
"v_mul_f32 v32, v217, v221\n"
"v_mfma_f32_16x16x32_bf16 v[120:123], v[56:59], a[140:143], v[120:123]\n"
"v_cvt_pk_bf16_f32 v33, v31, v32\n"
"v_lshlrev_b32 v226, 16, v197\n"
"v_mfma_f32_16x16x32_bf16 v[124:127], v[44:47], a[128:131], v[124:127]\n"
"s_add_u32 m0, s55, 4224\n"
"ds_read_b64_tr_b16 v[56:57], v237 offset:21824\n"
"ds_read_b64_tr_b16 v[58:59], v237 offset:22336\n"
"v_mfma_f32_16x16x32_bf16 v[128:131], v[44:47], a[132:135], v[128:131]\n"
"s_add_u32 s65, s65, s95\n"
"buffer_load_dwordx4 v238, s[4:7], s65 offen offset:0 lds\n"
"v_mfma_f32_16x16x32_bf16 v[132:135], v[44:47], a[136:139], v[132:135]\n"
"v_lshlrev_b32 v31, 16, v33\n"
"v_and_b32 v32, s81, v33\n"
"v_mfma_f32_16x16x32_bf16 v[136:139], v[44:47], a[140:143], v[136:139]\n"
"s_waitcnt vmcnt(20) lgkmcnt(4)\n"
"s_barrier\n"
"v_add_f32 v22, v22, v31\n"
"v_mfma_f32_16x16x32_bf16 v[156:159], v[48:51], a[128:131], v[156:159]\n"
"s_add_u32 m0, s55, 5312\n"
"ds_read_b64_tr_b16 v[44:45], v236 offset:26112\n"
"ds_read_b64_tr_b16 v[46:47], v236 offset:26624\n"
"v_mfma_f32_16x16x32_bf16 v[160:163], v[48:51], a[132:135], v[160:163]\n"
"buffer_load_dwordx4 v239, s[4:7], s65 offen offset:0 lds\n"
"v_mfma_f32_16x16x32_bf16 v[164:167], v[48:51], a[136:139], v[164:167]\n"
"v_add_f32 v23, v23, v32\n"
"v_mul_f32 v31, v214, v222\n"
"v_mfma_f32_16x16x32_bf16 v[168:171], v[48:51], a[140:143], v[168:171]\n"
"s_waitcnt lgkmcnt(4)\n"
"v_cvt_pk_bf16_f32 v33, v22, v23\n"
"v_mul_f32 v32, v215, v223\n"
"v_mfma_f32_16x16x32_bf16 v[140:143], v[52:55], a[128:131], v[140:143]\n"
"s_add_u32 m0, s55, 6400\n"
"ds_read_b64_tr_b16 v[48:49], v236 offset:30464\n"
"ds_read_b64_tr_b16 v[50:51], v236 offset:30976\n"
"v_mfma_f32_16x16x32_bf16 v[144:147], v[52:55], a[132:135], v[144:147]\n"
"s_add_u32 s66, s66, s95\n"
"buffer_load_dwordx4 v238, s[4:7], s66 offen offset:0 lds\n"
"v_mfma_f32_16x16x32_bf16 v[148:151], v[52:55], a[136:139], v[148:151]\n"
"v_lshlrev_b32 v22, 16, v33\n"
"v_and_b32 v23, s81, v33\n"
"v_mfma_f32_16x16x32_bf16 v[152:155], v[52:55], a[140:143], v[152:155]\n"
"s_waitcnt lgkmcnt(4)\n"
"v_cvt_pk_bf16_f32 v33, v31, v32\n"
"v_and_b32 v227, s81, v197\n"
"v_mfma_f32_16x16x32_bf16 v[172:175], v[56:59], a[128:131], v[172:175]\n"
"s_add_u32 m0, s55, 7488\n"
"ds_read_b64_tr_b16 v[52:53], v237 offset:26112\n"
"ds_read_b64_tr_b16 v[54:55], v237 offset:26624\n"
"v_mfma_f32_16x16x32_bf16 v[176:179], v[56:59], a[132:135], v[176:179]\n"
"buffer_load_dwordx4 v239, s[4:7], s66 offen offset:0 lds\n"
"v_mfma_f32_16x16x32_bf16 v[180:183], v[56:59], a[136:139], v[180:183]\n"
"v_lshlrev_b32 v31, 16, v33\n"
"v_and_b32 v32, s81, v33\n"
"v_mfma_f32_16x16x32_bf16 v[184:187], v[56:59], a[140:143], v[184:187]\n"
"s_waitcnt lgkmcnt(4)\n"
"v_add_f32 v22, v22, v31\n"
"v_add_f32 v23, v23, v32\n"
"v_mfma_f32_16x16x32_bf16 v[60:63], v[44:47], a[144:147], v[60:63]\n"
"s_add_u32 m0, s55, 8576\n"
"ds_read_b64_tr_b16 v[56:57], v237 offset:30464\n"
"ds_read_b64_tr_b16 v[58:59], v237 offset:30976\n"
"v_mfma_f32_16x16x32_bf16 v[64:67], v[44:47], a[148:151], v[64:67]\n"
"s_add_u32 s67, s67, s95\n"
"buffer_load_dwordx4 v238, s[4:7], s67 offen offset:0 lds\n"
"v_mfma_f32_16x16x32_bf16 v[68:71], v[44:47], a[152:155], v[68:71]\n"
"v_cvt_pk_bf16_f32 v33, v22, v23\n"
"v_mul_f32 v31, v212, v224\n"
"v_mfma_f32_16x16x32_bf16 v[72:75], v[44:47], a[156:159], v[72:75]\n"
"s_waitcnt lgkmcnt(4)\n"
"v_lshlrev_b32 v22, 16, v33\n"
"v_and_b32 v23, s81, v33\n"
"v_mfma_f32_16x16x32_bf16 v[92:95], v[48:51], a[144:147], v[92:95]\n"
"s_add_u32 m0, s55, 9664\n"
"ds_read_b64_tr_b16 v[44:45], v236 offset:26176\n"
"ds_read_b64_tr_b16 v[46:47], v236 offset:26688\n"
"v_mfma_f32_16x16x32_bf16 v[96:99], v[48:51], a[148:151], v[96:99]\n"
"buffer_load_dwordx4 v239, s[4:7], s67 offen offset:0 lds\n"
"v_mfma_f32_16x16x32_bf16 v[100:103], v[48:51], a[152:155], v[100:103]\n"
"v_mul_f32 v32, v213, v225\n"
"v_lshlrev_b32 v232, 16, v206\n"
"v_mfma_f32_16x16x32_bf16 v[104:107], v[48:51], a[156:159], v[104:107]\n"
"s_waitcnt lgkmcnt(4)\n"
"v_cvt_pk_bf16_f32 v33, v31, v32\n"
"v_and_b32 v233, s81, v206\n"
"v_mfma_f32_16x16x32_bf16 v[76:79], v[52:55], a[144:147], v[76:79]\n"
"ds_read_b64_tr_b16 v[48:49], v236 offset:30528\n"
"ds_read_b64_tr_b16 v[50:51], v236 offset:31040\n"
"v_mfma_f32_16x16x32_bf16 v[80:83], v[52:55], a[148:151], v[80:83]\n"
"s_add_u32 s26, s26, 4096\n"
"buffer_store_dwordx4 v[252:255], v10, s[28:31], s26 offen offset:0 sc1\n"
"v_mfma_f32_16x16x32_bf16 v[84:87], v[52:55], a[152:155], v[84:87]\n"
"v_lshlrev_b32 v31, 16, v33\n"
"v_and_b32 v32, s81, v33\n"
"v_mfma_f32_16x16x32_bf16 v[88:91], v[52:55], a[156:159], v[88:91]\n"
"s_waitcnt lgkmcnt(4)\n"
"v_add_f32 v22, v22, v31\n"
"v_add_f32 v23, v23, v32\n"
"v_mfma_f32_16x16x32_bf16 v[108:111], v[56:59], a[144:147], v[108:111]\n"
"s_add_u32 m0, s54, 8704\n"
"ds_read_b64_tr_b16 v[52:53], v237 offset:26176\n"
"ds_read_b64_tr_b16 v[54:55], v237 offset:26688\n"
"v_mfma_f32_16x16x32_bf16 v[112:115], v[56:59], a[148:151], v[112:115]\n"
"s_add_u32 s68, s68, 294912\n"
"buffer_load_dwordx4 v240, s[20:23], s68 offen offset:0 lds\n"
"v_mfma_f32_16x16x32_bf16 v[116:119], v[56:59], a[152:155], v[116:119]\n"
"v_cvt_pk_bf16_f32 v33, v22, v23\n"
"v_mul_f32 v24, v218, v220\n"
"v_mfma_f32_16x16x32_bf16 v[120:123], v[56:59], a[156:159], v[120:123]\n"
"s_waitcnt lgkmcnt(4)\n"
"v_lshlrev_b32 v22, 16, v33\n"
"v_and_b32 v23, s81, v33\n"
"v_mfma_f32_16x16x32_bf16 v[124:127], v[44:47], a[144:147], v[124:127]\n"
"s_add_u32 m0, s54, 9792\n"
"ds_read_b64_tr_b16 v[56:57], v237 offset:30528\n"
"ds_read_b64_tr_b16 v[58:59], v237 offset:31040\n"
"v_mfma_f32_16x16x32_bf16 v[128:131], v[44:47], a[148:151], v[128:131]\n"
"buffer_load_dwordx4 v241, s[20:23], s68 offen offset:0 lds\n"
"v_mfma_f32_16x16x32_bf16 v[132:135], v[44:47], a[152:155], v[132:135]\n"
"v_mul_f32 v22, v22, v234\n"
"v_mul_f32 v23, v23, v235\n"
"v_mfma_f32_16x16x32_bf16 v[136:139], v[44:47], a[156:159], v[136:139]\n"
"s_waitcnt vmcnt(17) lgkmcnt(4)\n"
"s_barrier\n"
"v_mul_f32 v25, v219, v221\n"
"v_mfma_f32_16x16x32_bf16 v[156:159], v[48:51], a[144:147], v[156:159]\n"
"ds_read_b64_tr_b16 v[44:45], v236 offset:34816\n"
"ds_read_b64_tr_b16 v[46:47], v236 offset:35328\n"
"v_mfma_f32_16x16x32_bf16 v[160:163], v[48:51], a[148:151], v[160:163]\n"
"buffer_store_dwordx4 v[252:255], v10, s[28:31], s27 offen offset:0 sc1\n"
"v_mfma_f32_16x16x32_bf16 v[164:167], v[48:51], a[152:155], v[164:167]\n"
"ds_read_b128 a[160:163], v7 offset:17408\n"
"ds_read_b128 a[164:167], v7 offset:19584\n"
"v_mfma_f32_16x16x32_bf16 v[168:171], v[48:51], a[156:159], v[168:171]\n"
"s_waitcnt lgkmcnt(6)\n"
"v_cvt_pk_bf16_f32 v36, v24, v25\n"
"v_mul_f32 v34, v216, v222\n"
"v_mfma_f32_16x16x32_bf16 v[140:143], v[52:55], a[144:147], v[140:143]\n"
"s_add_u32 m0, s54, 17408\n"
"ds_read_b64_tr_b16 v[48:49], v236 offset:39168\n"
"ds_read_b64_tr_b16 v[50:51], v236 offset:39680\n"
"v_mfma_f32_16x16x32_bf16 v[144:147], v[52:55], a[148:151], v[144:147]\n"
"s_add_u32 s68, s68, 294912\n"
"buffer_load_dwordx4 v240, s[20:23], s68 offen offset:0 lds\n"
"v_mfma_f32_16x16x32_bf16 v[148:151], v[52:55], a[152:155], v[148:151]\n"
"ds_read_b128 a[168:171], v7 offset:21760\n"
"ds_read_b128 a[172:175], v7 offset:23936\n"
"v_mfma_f32_16x16x32_bf16 v[152:155], v[52:55], a[156:159], v[152:155]\n"
"s_waitcnt lgkmcnt(8)\n"
"v_lshlrev_b32 v24, 16, v36\n"
"v_and_b32 v25, s81, v36\n"
"v_mfma_f32_16x16x32_bf16 v[172:175], v[56:59], a[144:147], v[172:175]\n"
"s_add_u32 m0, s54, 18496\n"
"ds_read_b64_tr_b16 v[52:53], v237 offset:34816\n"
"ds_read_b64_tr_b16 v[54:55], v237 offset:35328\n"
"v_mfma_f32_16x16x32_bf16 v[176:179], v[56:59], a[148:151], v[176:179]\n"
"buffer_load_dwordx4 v241, s[20:23], s68 offen offset:0 lds\n"
"v_mfma_f32_16x16x32_bf16 v[180:183], v[56:59], a[152:155], v[180:183]\n"
"v_mul_f32 v35, v217, v223\n"
"v_lshlrev_b32 v228, 16, v198\n"
"v_mfma_f32_16x16x32_bf16 v[184:187], v[56:59], a[156:159], v[184:187]\n"
"s_waitcnt lgkmcnt(2)\n"
"v_cvt_pk_bf16_f32 v36, v34, v35\n"
"v_and_b32 v229, s81, v198\n"
"v_mfma_f32_16x16x32_bf16 v[60:63], v[44:47], a[160:163], v[60:63]\n"
"s_add_u32 m0, s55, 10624\n"
"ds_read_b64_tr_b16 v[56:57], v237 offset:39168\n"
"ds_read_b64_tr_b16 v[58:59], v237 offset:39680\n"
"v_mfma_f32_16x16x32_bf16 v[64:67], v[44:47], a[164:167], v[64:67]\n"
"buffer_load_dwordx4 v238, s[4:7], s64 offen offset:128 lds\n"
"v_mfma_f32_16x16x32_bf16 v[68:71], v[44:47], a[168:171], v[68:71]\n"
"ds_read_b128 a[176:179], v7 offset:17472\n"
"ds_read_b128 a[180:183], v7 offset:19648\n"
"v_mfma_f32_16x16x32_bf16 v[72:75], v[44:47], a[172:175], v[72:75]\n"
"v_lshlrev_b32 v34, 16, v36\n"
"v_and_b32 v35, s81, v36\n"
"v_mfma_f32_16x16x32_bf16 v[92:95], v[48:51], a[160:163], v[92:95]\n"
"s_add_u32 m0, s55, 11712\n"
"ds_read_b64_tr_b16 v[44:45], v236 offset:34880\n"
"ds_read_b64_tr_b16 v[46:47], v236 offset:35392\n"
"v_mfma_f32_16x16x32_bf16 v[96:99], v[48:51], a[164:167], v[96:99]\n"
"buffer_load_dwordx4 v239, s[4:7], s64 offen offset:128 lds\n"
"v_mfma_f32_16x16x32_bf16 v[100:103], v[48:51], a[168:171], v[100:103]\n"
"ds_read_b128 a[184:187], v7 offset:21824\n"
"ds_read_b128 a[188:191], v7 offset:24000\n"
"v_mfma_f32_16x16x32_bf16 v[104:107], v[48:51], a[172:175], v[104:107]\n"
"s_waitcnt lgkmcnt(8)\n"
"v_add_f32 v24, v24, v34\n"
"v_add_f32 v25, v25, v35\n"
"v_mfma_f32_16x16x32_bf16 v[76:79], v[52:55], a[160:163], v[76:79]\n"
"s_add_u32 m0, s55, 12800\n"
"ds_read_b64_tr_b16 v[48:49], v236 offset:39232\n"
"ds_read_b64_tr_b16 v[50:51], v236 offset:39744\n"
"v_mfma_f32_16x16x32_bf16 v[80:83], v[52:55], a[164:167], v[80:83]\n"
"buffer_load_dwordx4 v238, s[4:7], s65 offen offset:128 lds\n"
"v_mfma_f32_16x16x32_bf16 v[84:87], v[52:55], a[168:171], v[84:87]\n"
"v_cvt_pk_bf16_f32 v36, v24, v25\n"
"v_mul_f32 v34, v214, v224\n"
"v_mfma_f32_16x16x32_bf16 v[88:91], v[52:55], a[172:175], v[88:91]\n"
"s_waitcnt lgkmcnt(2)\n"
"v_lshlrev_b32 v24, 16, v36\n"
"v_and_b32 v25, s81, v36\n"
"v_mfma_f32_16x16x32_bf16 v[108:111], v[56:59], a[160:163], v[108:111]\n"
"s_add_u32 m0, s55, 13888\n"
"ds_read_b64_tr_b16 v[52:53], v237 offset:34880\n"
"ds_read_b64_tr_b16 v[54:55], v237 offset:35392\n"
"v_mfma_f32_16x16x32_bf16 v[112:115], v[56:59], a[164:167], v[112:115]\n"
"buffer_load_dwordx4 v239, s[4:7], s65 offen offset:128 lds\n"
"v_mfma_f32_16x16x32_bf16 v[116:119], v[56:59], a[168:171], v[116:119]\n"
"v_mul_f32 v35, v215, v225\n"
"v_lshlrev_b32 v234, 16, v207\n"
"v_mfma_f32_16x16x32_bf16 v[120:123], v[56:59], a[172:175], v[120:123]\n"
"v_cvt_pk_bf16_f32 v36, v34, v35\n"
"v_and_b32 v235, s81, v207\n"
"v_mfma_f32_16x16x32_bf16 v[124:127], v[44:47], a[160:163], v[124:127]\n"
"s_add_u32 m0, s55, 14976\n"
"ds_read_b64_tr_b16 v[56:57], v237 offset:39232\n"
"ds_read_b64_tr_b16 v[58:59], v237 offset:39744\n"
"v_mfma_f32_16x16x32_bf16 v[128:131], v[44:47], a[164:167], v[128:131]\n"
"buffer_load_dwordx4 v238, s[4:7], s66 offen offset:128 lds\n"
"v_mfma_f32_16x16x32_bf16 v[132:135], v[44:47], a[168:171], v[132:135]\n"
"v_lshlrev_b32 v34, 16, v36\n"
"v_and_b32 v35, s81, v36\n"
"v_mfma_f32_16x16x32_bf16 v[136:139], v[44:47], a[172:175], v[136:139]\n"
"s_waitcnt vmcnt(22) lgkmcnt(4)\n"
"s_barrier\n"
"v_add_f32 v24, v24, v34\n"
"v_mfma_f32_16x16x32_bf16 v[156:159], v[48:51], a[160:163], v[156:159]\n"
"s_add_u32 m0, s55, 16064\n"
"ds_read_b64_tr_b16 v[44:45], v236 offset:43520\n"
"ds_read_b64_tr_b16 v[46:47], v236 offset:44032\n"
"v_mfma_f32_16x16x32_bf16 v[160:163], v[48:51], a[164:167], v[160:163]\n"
"buffer_load_dwordx4 v239, s[4:7], s66 offen offset:128 lds\n"
"v_mfma_f32_16x16x32_bf16 v[164:167], v[48:51], a[168:171], v[164:167]\n"
"v_add_f32 v25, v25, v35\n"
"v_mul_f32 v34, v212, v226\n"
"v_mfma_f32_16x16x32_bf16 v[168:171], v[48:51], a[172:175], v[168:171]\n"
"s_waitcnt lgkmcnt(4)\n"
"v_cvt_pk_bf16_f32 v36, v24, v25\n"
"v_mul_f32 v35, v213, v227\n"
"v_mfma_f32_16x16x32_bf16 v[140:143], v[52:55], a[160:163], v[140:143]\n"
"s_add_u32 m0, s55, 17152\n"
"ds_read_b64_tr_b16 v[48:49], v236 offset:47872\n"
"ds_read_b64_tr_b16 v[50:51], v236 offset:48384\n"
"v_mfma_f32_16x16x32_bf16 v[144:147], v[52:55], a[164:167], v[144:147]\n"
"buffer_load_dwordx4 v238, s[4:7], s67 offen offset:128 lds\n"
"v_mfma_f32_16x16x32_bf16 v[148:151], v[52:55], a[168:171], v[148:151]\n"
"v_lshlrev_b32 v24, 16, v36\n"
"v_and_b32 v25, s81, v36\n"
"v_mfma_f32_16x16x32_bf16 v[152:155], v[52:55], a[172:175], v[152:155]\n"
"s_waitcnt lgkmcnt(4)\n"
"v_cvt_pk_bf16_f32 v36, v34, v35\n"
"v_mul_f32 v26, v218, v222\n"
"v_mfma_f32_16x16x32_bf16 v[172:175], v[56:59], a[160:163], v[172:175]\n"
"s_add_u32 m0, s55, 18240\n"
"ds_read_b64_tr_b16 v[52:53], v237 offset:43520\n"
"ds_read_b64_tr_b16 v[54:55], v237 offset:44032\n"
"v_mfma_f32_16x16x32_bf16 v[176:179], v[56:59], a[164:167], v[176:179]\n"
"buffer_load_dwordx4 v239, s[4:7], s67 offen offset:128 lds\n"
"v_mfma_f32_16x16x32_bf16 v[180:183], v[56:59], a[168:171], v[180:183]\n"
"v_lshlrev_b32 v34, 16, v36\n"
"v_and_b32 v35, s81, v36\n"
"v_mfma_f32_16x16x32_bf16 v[184:187], v[56:59], a[172:175], v[184:187]\n"
"s_waitcnt lgkmcnt(4)\n"
"v_add_f32 v24, v24, v34\n"
"v_add_f32 v25, v25, v35\n"
"v_mfma_f32_16x16x32_bf16 v[60:63], v[44:47], a[176:179], v[60:63]\n"
"s_add_u32 m0, s54, 26112\n"
"ds_read_b64_tr_b16 v[56:57], v237 offset:47872\n"
"ds_read_b64_tr_b16 v[58:59], v237 offset:48384\n"
"v_mfma_f32_16x16x32_bf16 v[64:67], v[44:47], a[180:183], v[64:67]\n"
"s_add_u32 s68, s68, 294912\n"
"buffer_load_dwordx4 v240, s[20:23], s68 offen offset:0 lds\n"
"v_mfma_f32_16x16x32_bf16 v[68:71], v[44:47], a[184:187], v[68:71]\n"
"v_cvt_pk_bf16_f32 v36, v24, v25\n"
"v_mul_f32 v27, v219, v223\n"
"v_mfma_f32_16x16x32_bf16 v[72:75], v[44:47], a[188:191], v[72:75]\n"
"s_waitcnt lgkmcnt(4)\n"
"v_lshlrev_b32 v24, 16, v36\n"
"v_and_b32 v25, s81, v36\n"
"v_mfma_f32_16x16x32_bf16 v[92:95], v[48:51], a[176:179], v[92:95]\n"
"s_add_u32 m0, s54, 27200\n"
"ds_read_b64_tr_b16 v[44:45], v236 offset:43584\n"
"ds_read_b64_tr_b16 v[46:47], v236 offset:44096\n"
"v_mfma_f32_16x16x32_bf16 v[96:99], v[48:51], a[180:183], v[96:99]\n"
"buffer_load_dwordx4 v241, s[20:23], s68 offen offset:0 lds\n"
"v_mfma_f32_16x16x32_bf16 v[100:103], v[48:51], a[184:187], v[100:103]\n"
"v_mul_f32 v24, v24, v232\n"
"v_mul_f32 v25, v25, v233\n"
"v_mfma_f32_16x16x32_bf16 v[104:107], v[48:51], a[188:191], v[104:107]\n"
"s_waitcnt lgkmcnt(4)\n"
"v_cvt_pk_bf16_f32 v39, v26, v27\n"
"v_mul_f32 v37, v216, v224\n"
"v_mfma_f32_16x16x32_bf16 v[76:79], v[52:55], a[176:179], v[76:79]\n"
"ds_read_b64_tr_b16 v[48:49], v236 offset:47936\n"
"ds_read_b64_tr_b16 v[50:51], v236 offset:48448\n"
"v_mfma_f32_16x16x32_bf16 v[80:83], v[52:55], a[180:183], v[80:83]\n"
"v_lshlrev_b32 v26, 16, v39\n"
"v_and_b32 v27, s81, v39\n"
"v_mfma_f32_16x16x32_bf16 v[84:87], v[52:55], a[184:187], v[84:87]\n"
"v_mul_f32 v38, v217, v225\n"
"v_cvt_pk_bf16_f32 v189, v22, v20\n"
"v_mfma_f32_16x16x32_bf16 v[88:91], v[52:55], a[188:191], v[88:91]\n"
"s_waitcnt lgkmcnt(4)\n"
"v_cvt_pk_bf16_f32 v39, v37, v38\n"
"v_cvt_pk_bf16_f32 v190, v18, v16\n"
"v_mfma_f32_16x16x32_bf16 v[108:111], v[56:59], a[176:179], v[108:111]\n"
"ds_read_b64_tr_b16 v[52:53], v237 offset:43584\n"
"ds_read_b64_tr_b16 v[54:55], v237 offset:44096\n"
"v_mfma_f32_16x16x32_bf16 v[112:115], v[56:59], a[180:183], v[112:115]\n"
"v_lshlrev_b32 v37, 16, v39\n"
"v_and_b32 v38, s81, v39\n"
"v_mfma_f32_16x16x32_bf16 v[116:119], v[56:59], a[184:187], v[116:119]\n"
"v_add_f32 v26, v26, v37\n"
"v_add_f32 v27, v27, v38\n"
"v_mfma_f32_16x16x32_bf16 v[120:123], v[56:59], a[188:191], v[120:123]\n"
"s_waitcnt lgkmcnt(4)\n"
"v_cvt_pk_bf16_f32 v39, v26, v27\n"
"v_mul_f32 v37, v214, v226\n"
"v_mfma_f32_16x16x32_bf16 v[124:127], v[44:47], a[176:179], v[124:127]\n"
"ds_read_b64_tr_b16 v[56:57], v237 offset:47936\n"
"ds_read_b64_tr_b16 v[58:59], v237 offset:48448\n"
"v_mfma_f32_16x16x32_bf16 v[128:131], v[44:47], a[180:183], v[128:131]\n"
"v_lshlrev_b32 v26, 16, v39\n"
"v_and_b32 v27, s81, v39\n"
"v_mfma_f32_16x16x32_bf16 v[132:135], v[44:47], a[184:187], v[132:135]\n"
"v_mul_f32 v38, v215, v227\n"
"v_cvt_pk_bf16_f32 v191, v14, v12\n"
"v_mfma_f32_16x16x32_bf16 v[136:139], v[44:47], a[188:191], v[136:139]\n"
"s_waitcnt vmcnt(16) lgkmcnt(4)\n"
"s_barrier\n"
"v_cvt_pk_bf16_f32 v39, v37, v38\n"
"v_mfma_f32_16x16x32_bf16 v[156:159], v[48:51], a[176:179], v[156:159]\n"
"s_add_u32 m0, s54, 34816\n"
"ds_read_b64_tr_b16 v[44:45], v236 offset:0\n"
"ds_read_b64_tr_b16 v[46:47], v236 offset:512\n"
"v_mfma_f32_16x16x32_bf16 v[160:163], v[48:51], a[180:183], v[160:163]\n"
"s_add_u32 s68, s68, 294912\n"
"buffer_load_dwordx4 v240, s[20:23], s68 offen offset:0 lds\n"
"v_mfma_f32_16x16x32_bf16 v[164:167], v[48:51], a[184:187], v[164:167]\n"
"ds_read_b128 a[192:195], v7 offset:0\n"
"ds_read_b128 a[196:199], v7 offset:2176\n"
"v_mfma_f32_16x16x32_bf16 v[168:171], v[48:51], a[188:191], v[168:171]\n"
"s_waitcnt lgkmcnt(6)\n"
"v_lshlrev_b32 v37, 16, v39\n"
"v_and_b32 v38, s81, v39\n"
"v_mfma_f32_16x16x32_bf16 v[140:143], v[52:55], a[176:179], v[140:143]\n"
"s_add_u32 m0, s54, 35904\n"
"ds_read_b64_tr_b16 v[48:49], v236 offset:4352\n"
"ds_read_b64_tr_b16 v[50:51], v236 offset:4864\n"
"v_mfma_f32_16x16x32_bf16 v[144:147], v[52:55], a[180:183], v[144:147]\n"
"buffer_load_dwordx4 v241, s[20:23], s68 offen offset:0 lds\n"
"v_mfma_f32_16x16x32_bf16 v[148:151], v[52:55], a[184:187], v[148:151]\n"
"ds_read_b128 a[200:203], v7 offset:4352\n"
"ds_read_b128 a[204:207], v7 offset:6528\n"
"v_mfma_f32_16x16x32_bf16 v[152:155], v[52:55], a[188:191], v[152:155]\n"
"s_waitcnt lgkmcnt(8)\n"
"v_add_f32 v26, v26, v37\n"
"v_add_f32 v27, v27, v38\n"
"v_mfma_f32_16x16x32_bf16 v[172:175], v[56:59], a[176:179], v[172:175]\n"
"s_add_u32 m0, s55, 19200\n"
"ds_read_b64_tr_b16 v[52:53], v237 offset:0\n"
"ds_read_b64_tr_b16 v[54:55], v237 offset:512\n"
"v_mfma_f32_16x16x32_bf16 v[176:179], v[56:59], a[180:183], v[176:179]\n"
"buffer_load_dwordx4 v238, s[4:7], s64 offen offset:256 lds\n"
"v_mfma_f32_16x16x32_bf16 v[180:183], v[56:59], a[184:187], v[180:183]\n"
"v_cvt_pk_bf16_f32 v39, v26, v27\n"
"v_mul_f32 v37, v212, v228\n"
"v_mfma_f32_16x16x32_bf16 v[184:187], v[56:59], a[188:191], v[184:187]\n"
"s_waitcnt lgkmcnt(2)\n"
"v_lshlrev_b32 v26, 16, v39\n"
"v_and_b32 v27, s81, v39\n"
"v_mfma_f32_16x16x32_bf16 v[60:63], v[44:47], a[192:195], v[60:63]\n"
"s_add_u32 m0, s55, 20288\n"
"ds_read_b64_tr_b16 v[56:57], v237 offset:4352\n"
"ds_read_b64_tr_b16 v[58:59], v237 offset:4864\n"
"v_mfma_f32_16x16x32_bf16 v[64:67], v[44:47], a[196:199], v[64:67]\n"
"buffer_load_dwordx4 v239, s[4:7], s64 offen offset:256 lds\n"
"v_mfma_f32_16x16x32_bf16 v[68:71], v[44:47], a[200:203], v[68:71]\n"
"ds_read_b128 a[208:211], v7 offset:64\n"
"ds_read_b128 a[212:215], v7 offset:2240\n"
"v_mfma_f32_16x16x32_bf16 v[72:75], v[44:47], a[204:207], v[72:75]\n"
"v_mul_f32 v38, v213, v229\n"
"v_cvt_pk_bf16_f32 v193, v23, v21\n"
"v_mfma_f32_16x16x32_bf16 v[92:95], v[48:51], a[192:195], v[92:95]\n"
"s_add_u32 m0, s55, 21376\n"
"ds_read_b64_tr_b16 v[44:45], v236 offset:64\n"
"ds_read_b64_tr_b16 v[46:47], v236 offset:576\n"
"v_mfma_f32_16x16x32_bf16 v[96:99], v[48:51], a[196:199], v[96:99]\n"
"buffer_load_dwordx4 v238, s[4:7], s65 offen offset:256 lds\n"
"v_mfma_f32_16x16x32_bf16 v[100:103], v[48:51], a[200:203], v[100:103]\n"
"ds_read_b128 a[216:219], v7 offset:4416\n"
"ds_read_b128 a[220:223], v7 offset:6592\n"
"v_mfma_f32_16x16x32_bf16 v[104:107], v[48:51], a[204:207], v[104:107]\n"
"s_waitcnt lgkmcnt(8)\n"
"v_cvt_pk_bf16_f32 v39, v37, v38\n"
"v_cvt_pk_bf16_f32 v194, v19, v17\n"
"v_mfma_f32_16x16x32_bf16 v[76:79], v[52:55], a[192:195], v[76:79]\n"
"s_add_u32 m0, s55, 22464\n"
"ds_read_b64_tr_b16 v[48:49], v236 offset:4416\n"
"ds_read_b64_tr_b16 v[50:51], v236 offset:4928\n"
"v_mfma_f32_16x16x32_bf16 v[80:83], v[52:55], a[196:199], v[80:83]\n"
"buffer_load_dwordx4 v239, s[4:7], s65 offen offset:256 lds\n"
"v_mfma_f32_16x16x32_bf16 v[84:87], v[52:55], a[200:203], v[84:87]\n"
"v_lshlrev_b32 v37, 16, v39\n"
"v_and_b32 v38, s81, v39\n"
"v_mfma_f32_16x16x32_bf16 v[88:91], v[52:55], a[204:207], v[88:91]\n"
"s_waitcnt lgkmcnt(2)\n"
"v_add_f32 v26, v26, v37\n"
"v_add_f32 v27, v27, v38\n"
"v_mfma_f32_16x16x32_bf16 v[108:111], v[56:59], a[192:195], v[108:111]\n"
"s_add_u32 m0, s55, 23552\n"
"ds_read_b64_tr_b16 v[52:53], v237 offset:64\n"
"ds_read_b64_tr_b16 v[54:55], v237 offset:576\n"
"v_mfma_f32_16x16x32_bf16 v[112:115], v[56:59], a[196:199], v[112:115]\n"
"buffer_load_dwordx4 v238, s[4:7], s66 offen offset:256 lds\n"
"v_mfma_f32_16x16x32_bf16 v[116:119], v[56:59], a[200:203], v[116:119]\n"
"v_cvt_pk_bf16_f32 v39, v26, v27\n"
"v_cvt_pk_bf16_f32 v195, v15, v13\n"
"v_mfma_f32_16x16x32_bf16 v[120:123], v[56:59], a[204:207], v[120:123]\n"
"v_lshlrev_b32 v26, 16, v39\n"
"v_and_b32 v27, s81, v39\n"
"v_mfma_f32_16x16x32_bf16 v[124:127], v[44:47], a[192:195], v[124:127]\n"
"s_add_u32 m0, s55, 24640\n"
"ds_read_b64_tr_b16 v[56:57], v237 offset:4416\n"
"ds_read_b64_tr_b16 v[58:59], v237 offset:4928\n"
"v_mfma_f32_16x16x32_bf16 v[128:131], v[44:47], a[196:199], v[128:131]\n"
"buffer_load_dwordx4 v239, s[4:7], s66 offen offset:256 lds\n"
"v_mfma_f32_16x16x32_bf16 v[132:135], v[44:47], a[200:203], v[132:135]\n"
"v_mul_f32 v26, v26, v234\n"
"v_mul_f32 v27, v27, v235\n"
"v_mfma_f32_16x16x32_bf16 v[136:139], v[44:47], a[204:207], v[136:139]\n"
"s_waitcnt vmcnt(21) lgkmcnt(4)\n"
"s_barrier\n"
"v_cvt_pk_bf16_f32 v188, v26, v24\n"
"v_mfma_f32_16x16x32_bf16 v[156:159], v[48:51], a[192:195], v[156:159]\n"
"s_add_u32 m0, s55, 25728\n"
"ds_read_b64_tr_b16 v[44:45], v236 offset:8704\n"
"ds_read_b64_tr_b16 v[46:47], v236 offset:9216\n"
"v_mfma_f32_16x16x32_bf16 v[160:163], v[48:51], a[196:199], v[160:163]\n"
"buffer_load_dwordx4 v238, s[4:7], s67 offen offset:256 lds\n"
"v_mfma_f32_16x16x32_bf16 v[164:167], v[48:51], a[200:203], v[164:167]\n"
"buffer_store_dwordx4 v[188:191], v41, s[28:31], s48 offen offset:0 sc1\n"
"v_mfma_f32_16x16x32_bf16 v[168:171], v[48:51], a[204:207], v[168:171]\n"
"s_waitcnt lgkmcnt(4)\n"
"v_cvt_pk_bf16_f32 v192, v27, v25\n"
"v_mfma_f32_16x16x32_bf16 v[140:143], v[52:55], a[192:195], v[140:143]\n"
"s_add_u32 m0, s55, 26816\n"
"ds_read_b64_tr_b16 v[48:49], v236 offset:13056\n"
"ds_read_b64_tr_b16 v[50:51], v236 offset:13568\n"
"v_mfma_f32_16x16x32_bf16 v[144:147], v[52:55], a[196:199], v[144:147]\n"
"buffer_load_dwordx4 v239, s[4:7], s67 offen offset:256 lds\n"
"v_mfma_f32_16x16x32_bf16 v[148:151], v[52:55], a[200:203], v[148:151]\n"
"buffer_store_dwordx4 v[192:195], v41, s[28:31], s49 offen offset:0 sc1\n"
"v_mfma_f32_16x16x32_bf16 v[152:155], v[52:55], a[204:207], v[152:155]\n"
"s_waitcnt lgkmcnt(4)\n"
"v_mov_b32 v200, 1073741824\n"
"v_lshlrev_b32 v196, 7, v0\n"
"v_mfma_f32_16x16x32_bf16 v[172:175], v[56:59], a[192:195], v[172:175]\n"
"s_add_u32 m0, s54, 43520\n"
"ds_read_b64_tr_b16 v[52:53], v237 offset:8704\n"
"ds_read_b64_tr_b16 v[54:55], v237 offset:9216\n"
"v_mfma_f32_16x16x32_bf16 v[176:179], v[56:59], a[196:199], v[176:179]\n"
"s_add_u32 s68, s68, 294912\n"
"buffer_load_dwordx4 v240, s[20:23], s68 offen offset:0 lds\n"
"v_mfma_f32_16x16x32_bf16 v[180:183], v[56:59], a[200:203], v[180:183]\n"
"s_lshl_b32 s61, s33, 13\n"
"s_lshr_b32 s62, s33, 2\n"
"v_cmp_lt_u32 s[50:51], 63, v0\n"
"v_mfma_f32_16x16x32_bf16 v[184:187], v[56:59], a[204:207], v[184:187]\n"
"s_waitcnt lgkmcnt(4)\n"
"v_add_u32 v197, s61, v196\n"
"s_add_u32 s62, s62, 512\n"
"v_mfma_f32_16x16x32_bf16 v[60:63], v[44:47], a[208:211], v[60:63]\n"
"s_add_u32 m0, s54, 44608\n"
"ds_read_b64_tr_b16 v[56:57], v237 offset:13056\n"
"ds_read_b64_tr_b16 v[58:59], v237 offset:13568\n"
"v_mfma_f32_16x16x32_bf16 v[64:67], v[44:47], a[212:215], v[64:67]\n"
"s_lshl_b32 s62, s62, 12\n"
"buffer_load_dwordx4 v241, s[20:23], s68 offen offset:0 lds\n"
"v_mfma_f32_16x16x32_bf16 v[68:71], v[44:47], a[216:219], v[68:71]\n"
"s_sub_u32 s62, s62, 8192\n"
"s_movk_i32 s61, 95\n"
"v_mfma_f32_16x16x32_bf16 v[72:75], v[44:47], a[220:223], v[72:75]\n"
"s_waitcnt lgkmcnt(4)\n"
"v_add_u32 v199, s62, v196\n"
"v_and_b32 v198, 127, v0\n"
"v_mfma_f32_16x16x32_bf16 v[92:95], v[48:51], a[208:211], v[92:95]\n"
"s_mul_i32 s62, s33, 20736\n"
"ds_read_b64_tr_b16 v[44:45], v236 offset:8768\n"
"ds_read_b64_tr_b16 v[46:47], v236 offset:9280\n"
"v_mfma_f32_16x16x32_bf16 v[96:99], v[48:51], a[212:215], v[96:99]\n"
"v_cndmask_b32 v197, v197, v199, s[50:51]\n"
"v_add_u32 v196, s62, v196\n"
"v_mfma_f32_16x16x32_bf16 v[100:103], v[48:51], a[216:219], v[100:103]\n"
"s_mul_i32 s62, s33, 90\n"
"v_cmp_lt_u32 s[50:51], s61, v0\n"
"v_mfma_f32_16x16x32_bf16 v[104:107], v[48:51], a[220:223], v[104:107]\n"
"s_waitcnt lgkmcnt(4)\n"
"v_cndmask_b32 v197, v197, v200, s[50:51]\n"
"s_movk_i32 s61, 161\n"
"v_mfma_f32_16x16x32_bf16 v[76:79], v[52:55], a[208:211], v[76:79]\n"
"ds_read_b64_tr_b16 v[48:49], v236 offset:13120\n"
"ds_read_b64_tr_b16 v[50:51], v236 offset:13632\n"
"v_mfma_f32_16x16x32_bf16 v[80:83], v[52:55], a[212:215], v[80:83]\n"
"v_cmp_lt_u32 s[50:51], s61, v0\n"
"v_mfma_f32_16x16x32_bf16 v[84:87], v[52:55], a[216:219], v[84:87]\n"
"s_movk_i32 s61, 89\n"
"v_cndmask_b32 v196, v196, v200, s[50:51]\n"
"v_mfma_f32_16x16x32_bf16 v[88:91], v[52:55], a[220:223], v[88:91]\n"
"s_waitcnt lgkmcnt(4)\n"
"v_cmp_lt_u32 s[50:51], s61, v198\n"
"v_mfma_f32_16x16x32_bf16 v[108:111], v[56:59], a[208:211], v[108:111]\n"
"ds_read_b64_tr_b16 v[52:53], v237 offset:8768\n"
"ds_read_b64_tr_b16 v[54:55], v237 offset:9280\n"
"v_mfma_f32_16x16x32_bf16 v[112:115], v[56:59], a[212:215], v[112:115]\n"
"v_add_u32 v198, s62, v198\n"
"v_mfma_f32_16x16x32_bf16 v[116:119], v[56:59], a[216:219], v[116:119]\n"
"v_mul_u32_u24 v199, 52429, v198\n"
"v_mfma_f32_16x16x32_bf16 v[120:123], v[56:59], a[220:223], v[120:123]\n"
"s_waitcnt lgkmcnt(4)\n"
"v_lshrrev_b32 v199, 21, v199\n"
"v_mfma_f32_16x16x32_bf16 v[124:127], v[44:47], a[208:211], v[124:127]\n"
"ds_read_b64_tr_b16 v[56:57], v237 offset:13120\n"
"ds_read_b64_tr_b16 v[58:59], v237 offset:13632\n"
"v_mfma_f32_16x16x32_bf16 v[128:131], v[44:47], a[212:215], v[128:131]\n"
"v_mul_u32_u24 v199, 4096, v199\n"
"v_mfma_f32_16x16x32_bf16 v[132:135], v[44:47], a[216:219], v[132:135]\n"
"v_lshl_add_u32 v198, v198, 7, v199\n"
"v_mfma_f32_16x16x32_bf16 v[136:139], v[44:47], a[220:223], v[136:139]\n"
"s_waitcnt vmcnt(16) lgkmcnt(4)\n"
"s_barrier\n"
"v_add_u32 v198, 4096, v198\n"
"v_mfma_f32_16x16x32_bf16 v[156:159], v[48:51], a[208:211], v[156:159]\n"
"ds_read_b64_tr_b16 v[44:45], v236 offset:17408\n"
"ds_read_b64_tr_b16 v[46:47], v236 offset:17920\n"
"v_mfma_f32_16x16x32_bf16 v[160:163], v[48:51], a[212:215], v[160:163]\n"
"v_cndmask_b32 v198, v198, v200, s[50:51]\n"
"v_mfma_f32_16x16x32_bf16 v[164:167], v[48:51], a[216:219], v[164:167]\n"
"s_lshl_b32 s61, s33, 13\n"
"ds_read_b128 a[224:227], v7 offset:8704\n"
"ds_read_b128 a[228:231], v7 offset:10880\n"
"v_mfma_f32_16x16x32_bf16 v[168:171], v[48:51], a[220:223], v[168:171]\n"
"s_waitcnt lgkmcnt(6)\n"
"s_lshl_b32 s62, s52, 1\n"
"s_mul_i32 s91, s52, 4160\n"
"v_mfma_f32_16x16x32_bf16 v[140:143], v[52:55], a[208:211], v[140:143]\n"
"s_add_u32 s62, s62, 2\n"
"ds_read_b64_tr_b16 v[48:49], v236 offset:21760\n"
"ds_read_b64_tr_b16 v[50:51], v236 offset:22272\n"
"v_mfma_f32_16x16x32_bf16 v[144:147], v[52:55], a[212:215], v[144:147]\n"
"s_add_u32 s96, s61, s62\n"
"v_mfma_f32_16x16x32_bf16 v[148:151], v[52:55], a[216:219], v[148:151]\n"
"s_lshr_b32 s61, s33, 2\n"
"ds_read_b128 a[232:235], v7 offset:13056\n"
"ds_read_b128 a[236:239], v7 offset:15232\n"
"v_mfma_f32_16x16x32_bf16 v[152:155], v[52:55], a[220:223], v[152:155]\n"
"s_waitcnt lgkmcnt(8)\n"
"s_add_u32 s61, s61, 512\n"
"v_mfma_f32_16x16x32_bf16 v[172:175], v[56:59], a[208:211], v[172:175]\n"
"s_lshl_b32 s61, s61, 12\n"
"ds_read_b64_tr_b16 v[52:53], v237 offset:17408\n"
"ds_read_b64_tr_b16 v[54:55], v237 offset:17920\n"
"v_mfma_f32_16x16x32_bf16 v[176:179], v[56:59], a[212:215], v[176:179]\n"
"s_add_u32 s97, s61, s62\n"
"v_mfma_f32_16x16x32_bf16 v[180:183], v[56:59], a[216:219], v[180:183]\n"
"v_mfma_f32_16x16x32_bf16 v[184:187], v[56:59], a[220:223], v[184:187]\n"
"s_waitcnt lgkmcnt(2)\n"
"s_and_b32 s56, s33, 7\n"
"s_lshr_b32 s57, s33, 3\n"
"v_mfma_f32_16x16x32_bf16 v[60:63], v[44:47], a[224:227], v[60:63]\n"
"s_lshr_b32 s8, s56, 1\n"
"ds_read_b64_tr_b16 v[56:57], v237 offset:21760\n"
"ds_read_b64_tr_b16 v[58:59], v237 offset:22272\n"
"v_mfma_f32_16x16x32_bf16 v[64:67], v[44:47], a[228:231], v[64:67]\n"
"s_and_b32 s58, s56, 1\n"
"s_lshr_b32 s59, s57, 2\n"
"s_and_b32 s10, s57, 3\n"
"v_mfma_f32_16x16x32_bf16 v[68:71], v[44:47], a[232:235], v[68:71]\n"
"s_lshl_b32 s58, s58, 3\n"
"ds_read_b128 a[240:243], v7 offset:8768\n"
"ds_read_b128 a[244:247], v7 offset:10944\n"
"v_mfma_f32_16x16x32_bf16 v[72:75], v[44:47], a[236:239], v[72:75]\n"
"s_add_u32 s9, s58, s59\n"
"s_and_b32 s11, s10, 1\n"
"v_mfma_f32_16x16x32_bf16 v[92:95], v[48:51], a[224:227], v[92:95]\n"
"s_and_b32 s60, s52, 1\n"
"ds_read_b64_tr_b16 v[44:45], v236 offset:17472\n"
"ds_read_b64_tr_b16 v[46:47], v236 offset:17984\n"
"v_mfma_f32_16x16x32_bf16 v[96:99], v[48:51], a[228:231], v[96:99]\n"
"s_lshr_b32 s61, s52, 1\n"
"s_lshl_b32 s62, s9, 1\n"
"s_mul_i32 s63, s8, 36\n"
"v_mfma_f32_16x16x32_bf16 v[100:103], v[48:51], a[232:235], v[100:103]\n"
"s_add_u32 s62, s62, s61\n"
"ds_read_b128 a[248:251], v7 offset:13120\n"
"ds_read_b128 a[252:255], v7 offset:15296\n"
"v_mfma_f32_16x16x32_bf16 v[104:107], v[48:51], a[236:239], v[104:107]\n"
"s_waitcnt lgkmcnt(8)\n"
"s_mul_i32 s62, s62, 144\n"
"v_mfma_f32_16x16x32_bf16 v[76:79], v[52:55], a[224:227], v[76:79]\n"
"s_add_u32 s62, s62, s63\n"
"ds_read_b64_tr_b16 v[48:49], v236 offset:21824\n"
"ds_read_b64_tr_b16 v[50:51], v236 offset:22336\n"
"v_mfma_f32_16x16x32_bf16 v[80:83], v[52:55], a[228:231], v[80:83]\n"
"s_lshl_b32 s62, s62, 1\n"
"v_mfma_f32_16x16x32_bf16 v[84:87], v[52:55], a[232:235], v[84:87]\n"
"s_add_u32 s62, s62, s60\n"
"v_mfma_f32_16x16x32_bf16 v[88:91], v[52:55], a[236:239], v[88:91]\n"
"s_waitcnt lgkmcnt(2)\n"
"s_lshl_b32 s62, s62, 11\n"
"s_lshl_b32 s60, s60, 3\n"
"v_mfma_f32_16x16x32_bf16 v[108:111], v[56:59], a[224:227], v[108:111]\n"
"s_add_u32 s57, s62, 397312\n"
"ds_read_b64_tr_b16 v[52:53], v237 offset:17472\n"
"ds_read_b64_tr_b16 v[54:55], v237 offset:17984\n"
"v_mfma_f32_16x16x32_bf16 v[112:115], v[56:59], a[228:231], v[112:115]\n"
"s_lshl_b32 s62, s8, 1\n"
"v_mfma_f32_16x16x32_bf16 v[116:119], v[56:59], a[232:235], v[116:119]\n"
"s_add_u32 s62, s62, s61\n"
"v_mfma_f32_16x16x32_bf16 v[120:123], v[56:59], a[236:239], v[120:123]\n"
"s_lshl_b32 s63, s62, 8\n"
"v_mfma_f32_16x16x32_bf16 v[124:127], v[44:47], a[224:227], v[124:127]\n"
"s_add_u32 s63, s63, s60\n"
"ds_read_b64_tr_b16 v[56:57], v237 offset:21824\n"
"ds_read_b64_tr_b16 v[58:59], v237 offset:22336\n"
"v_mfma_f32_16x16x32_bf16 v[128:131], v[44:47], a[228:231], v[128:131]\n"
"s_mul_i32 s58, s63, 1152\n"
"s_mul_i32 s44, s10, 0\n"
"v_mfma_f32_16x16x32_bf16 v[132:135], v[44:47], a[232:235], v[132:135]\n"
"s_lshl_b32 s63, s62, 6\n"
"s_add_u32 s44, s44, s9\n"
"v_mfma_f32_16x16x32_bf16 v[136:139], v[44:47], a[236:239], v[136:139]\n"
"s_waitcnt vmcnt(14) lgkmcnt(4)\n"
"s_barrier\n"
"s_add_u32 s63, s63, s60\n"
"v_mfma_f32_16x16x32_bf16 v[156:159], v[48:51], a[224:227], v[156:159]\n"
"s_add_u32 s63, s63, 4096\n"
"ds_read_b64_tr_b16 v[44:45], v236 offset:26112\n"
"ds_read_b64_tr_b16 v[46:47], v236 offset:26624\n"
"v_mfma_f32_16x16x32_bf16 v[160:163], v[48:51], a[228:231], v[160:163]\n"
"s_mul_i32 s59, s63, 1152\n"
"s_mul_i32 s44, s44, 1\n"
"v_mfma_f32_16x16x32_bf16 v[164:167], v[48:51], a[232:235], v[164:167]\n"
"s_add_u32 s62, s44, 0\n"
"buffer_load_dword v251, v197, s[16:19], 0 offen offset:0\n"
"v_mfma_f32_16x16x32_bf16 v[168:171], v[48:51], a[236:239], v[168:171]\n"
"s_waitcnt lgkmcnt(4)\n"
"s_and_b32 s62, s62, 3\n"
"v_mfma_f32_16x16x32_bf16 v[140:143], v[52:55], a[224:227], v[140:143]\n"
"s_mul_i32 s63, s62, 73728\n"
"ds_read_b64_tr_b16 v[48:49], v236 offset:30464\n"
"ds_read_b64_tr_b16 v[50:51], v236 offset:30976\n"
"v_mfma_f32_16x16x32_bf16 v[144:147], v[52:55], a[228:231], v[144:147]\n"
"s_add_u32 m0, s53, 159744\n"
"s_add_u32 s12, s58, s63\n"
"s_lshl_b32 s62, s62, 14\n"
"v_mfma_f32_16x16x32_bf16 v[148:151], v[52:55], a[232:235], v[148:151]\n"
"s_add_u32 s64, s57, s62\n"
"buffer_load_dwordx4 v9, s[20:23], s69 offen offset:0 lds\n"
"v_mfma_f32_16x16x32_bf16 v[152:155], v[52:55], a[236:239], v[152:155]\n"
"s_waitcnt lgkmcnt(4)\n"
"s_add_u32 s62, s44, 1\n"
"v_mfma_f32_16x16x32_bf16 v[172:175], v[56:59], a[224:227], v[172:175]\n"
"s_and_b32 s62, s62, 3\n"
"ds_read_b64_tr_b16 v[52:53], v237 offset:26112\n"
"ds_read_b64_tr_b16 v[54:55], v237 offset:26624\n"
"v_mfma_f32_16x16x32_bf16 v[176:179], v[56:59], a[228:231], v[176:179]\n"
"s_add_u32 m0, s53, 135104\n"
"s_mul_i32 s63, s62, 73728\n"
"v_mfma_f32_16x16x32_bf16 v[180:183], v[56:59], a[232:235], v[180:183]\n"
"s_add_u32 s13, s58, s63\n"
"buffer_load_dwordx4 v9, s[20:23], s69 offen offset:64 lds\n"
"v_mfma_f32_16x16x32_bf16 v[184:187], v[56:59], a[236:239], v[184:187]\n"
"s_waitcnt lgkmcnt(4)\n"
"s_lshl_b32 s62, s62, 14\n"
"v_mfma_f32_16x16x32_bf16 v[60:63], v[44:47], a[240:243], v[60:63]\n"
"s_add_u32 s65, s57, s62\n"
"ds_read_b64_tr_b16 v[56:57], v237 offset:30464\n"
"ds_read_b64_tr_b16 v[58:59], v237 offset:30976\n"
"v_mfma_f32_16x16x32_bf16 v[64:67], v[44:47], a[244:247], v[64:67]\n"
"s_add_u32 m0, s53, 143360\n"
"s_add_u32 s62, s44, 2\n"
"v_mfma_f32_16x16x32_bf16 v[68:71], v[44:47], a[248:251], v[68:71]\n"
"s_add_u32 s69, s69, 294912\n"
"buffer_load_dwordx4 v9, s[20:23], s69 offen offset:0 lds\n"
"v_mfma_f32_16x16x32_bf16 v[72:75], v[44:47], a[252:255], v[72:75]\n"
"s_waitcnt lgkmcnt(4)\n"
"s_and_b32 s62, s62, 3\n"
"v_mfma_f32_16x16x32_bf16 v[92:95], v[48:51], a[240:243], v[92:95]\n"
"s_mul_i32 s63, s62, 73728\n"
"ds_read_b64_tr_b16 v[44:45], v236 offset:26176\n"
"ds_read_b64_tr_b16 v[46:47], v236 offset:26688\n"
"v_mfma_f32_16x16x32_bf16 v[96:99], v[48:51], a[244:247], v[96:99]\n"
"s_add_u32 m0, s53, 90048\n"
"s_add_u32 s14, s58, s63\n"
"s_lshl_b32 s62, s62, 14\n"
"v_mfma_f32_16x16x32_bf16 v[100:103], v[48:51], a[248:251], v[100:103]\n"
"s_add_u32 s66, s57, s62\n"
"buffer_load_dwordx4 v9, s[20:23], s69 offen offset:64 lds\n"
"v_mfma_f32_16x16x32_bf16 v[104:107], v[48:51], a[252:255], v[104:107]\n"
"s_waitcnt lgkmcnt(4)\n"
"s_add_u32 s62, s44, 3\n"
"v_mfma_f32_16x16x32_bf16 v[76:79], v[52:55], a[240:243], v[76:79]\n"
"s_and_b32 s62, s62, 3\n"
"ds_read_b64_tr_b16 v[48:49], v236 offset:30528\n"
"ds_read_b64_tr_b16 v[50:51], v236 offset:31040\n"
"v_mfma_f32_16x16x32_bf16 v[80:83], v[52:55], a[244:247], v[80:83]\n"
"s_add_u32 m0, s53, 139264\n"
"s_mul_i32 s63, s62, 73728\n"
"v_mfma_f32_16x16x32_bf16 v[84:87], v[52:55], a[248:251], v[84:87]\n"
"s_add_u32 s69, s69, 294912\n"
"buffer_load_dwordx4 v9, s[20:23], s69 offen offset:0 lds\n"
"v_mfma_f32_16x16x32_bf16 v[88:91], v[52:55], a[252:255], v[88:91]\n"
"s_waitcnt lgkmcnt(4)\n"
"s_add_u32 s15, s58, s63\n"
"s_lshl_b32 s62, s62, 14\n"
"v_mfma_f32_16x16x32_bf16 v[108:111], v[56:59], a[240:243], v[108:111]\n"
"s_add_u32 s67, s57, s62\n"
"ds_read_b64_tr_b16 v[52:53], v237 offset:26176\n"
"ds_read_b64_tr_b16 v[54:55], v237 offset:26688\n"
"v_mfma_f32_16x16x32_bf16 v[112:115], v[56:59], a[244:247], v[112:115]\n"
"s_add_u32 m0, s53, 131008\n"
"s_add_u32 s62, s44, 0\n"
"v_mfma_f32_16x16x32_bf16 v[116:119], v[56:59], a[248:251], v[116:119]\n"
"s_and_b32 s62, s62, 1\n"
"buffer_load_dwordx4 v9, s[20:23], s69 offen offset:64 lds\n"
"v_mfma_f32_16x16x32_bf16 v[120:123], v[56:59], a[252:255], v[120:123]\n"
"s_waitcnt lgkmcnt(4)\n"
"s_mul_i32 s63, s62, 36864\n"
"v_mfma_f32_16x16x32_bf16 v[124:127], v[44:47], a[240:243], v[124:127]\n"
"s_add_u32 s55, s59, s63\n"
"ds_read_b64_tr_b16 v[56:57], v237 offset:30528\n"
"ds_read_b64_tr_b16 v[58:59], v237 offset:31040\n"
"v_mfma_f32_16x16x32_bf16 v[128:131], v[44:47], a[244:247], v[128:131]\n"
"s_lshl_b32 s62, s62, 12\n"
"v_mfma_f32_16x16x32_bf16 v[132:135], v[44:47], a[248:251], v[132:135]\n"
"s_add_u32 s62, s62, 65536\n"
"v_mfma_f32_16x16x32_bf16 v[136:139], v[44:47], a[252:255], v[136:139]\n"
"s_waitcnt vmcnt(10) lgkmcnt(4)\n"
"s_barrier\n"
"s_add_u32 s94, s57, s62\n"
"v_mfma_f32_16x16x32_bf16 v[156:159], v[48:51], a[240:243], v[156:159]\n"
"s_add_u32 s62, s44, 1\n"
"ds_read_b64_tr_b16 v[44:45], v236 offset:34816\n"
"ds_read_b64_tr_b16 v[46:47], v236 offset:35328\n"
"v_mfma_f32_16x16x32_bf16 v[160:163], v[48:51], a[244:247], v[160:163]\n"
"s_and_b32 s62, s62, 1\n"
"v_mfma_f32_16x16x32_bf16 v[164:167], v[48:51], a[248:251], v[164:167]\n"
"s_mul_i32 s63, s62, 36864\n"
"ds_read_b128 v[12:15], v7 offset:17408\n"
"ds_read_b128 v[16:19], v7 offset:19584\n"
"v_mfma_f32_16x16x32_bf16 v[168:171], v[48:51], a[252:255], v[168:171]\n"
"s_waitcnt lgkmcnt(6)\n"
"s_add_u32 s68, s59, s63\n"
"s_lshl_b32 s62, s62, 12\n"
"v_mfma_f32_16x16x32_bf16 v[140:143], v[52:55], a[240:243], v[140:143]\n"
"s_add_u32 s62, s62, 65536\n"
"ds_read_b64_tr_b16 v[48:49], v236 offset:39168\n"
"ds_read_b64_tr_b16 v[50:51], v236 offset:39680\n"
"v_mfma_f32_16x16x32_bf16 v[144:147], v[52:55], a[244:247], v[144:147]\n"
"s_add_u32 s95, s57, s62\n"
"s_lshr_b32 s63, s10, 1\n"
"v_mfma_f32_16x16x32_bf16 v[148:151], v[52:55], a[248:251], v[148:151]\n"
"s_lshl_b32 s62, s10, 2\n"
"ds_read_b128 v[20:23], v7 offset:21760\n"
"ds_read_b128 v[24:27], v7 offset:23936\n"
"v_mfma_f32_16x16x32_bf16 v[152:155], v[52:55], a[252:255], v[152:155]\n"
"s_waitcnt lgkmcnt(8)\n"
"s_add_u32 s62, s62, s8\n"
"s_lshl_b32 s63, s63, 6\n"
"v_mfma_f32_16x16x32_bf16 v[172:175], v[56:59], a[240:243], v[172:175]\n"
"s_lshl_b32 s45, s62, 6\n"
"ds_read_b64_tr_b16 v[52:53], v237 offset:34816\n"
"ds_read_b64_tr_b16 v[54:55], v237 offset:35328\n"
"v_mfma_f32_16x16x32_bf16 v[176:179], v[56:59], a[244:247], v[176:179]\n"
"s_add_u32 s99, s63, 1024\n"
"s_mov_b32 s56, 0\n"
"v_mfma_f32_16x16x32_bf16 v[180:183], v[56:59], a[248:251], v[180:183]\n"
"v_mfma_f32_16x16x32_bf16 v[184:187], v[56:59], a[252:255], v[184:187]\n"
"s_waitcnt lgkmcnt(2)\n"
"v_mfma_f32_16x16x32_bf16 v[60:63], v[44:47], v[12:15], v[60:63]\n"
"ds_read_b64_tr_b16 v[56:57], v237 offset:39168\n"
"ds_read_b64_tr_b16 v[58:59], v237 offset:39680\n"
"v_mfma_f32_16x16x32_bf16 v[64:67], v[44:47], v[16:19], v[64:67]\n"
"v_mfma_f32_16x16x32_bf16 v[68:71], v[44:47], v[20:23], v[68:71]\n"
"ds_read_b128 v[28:31], v7 offset:17472\n"
"ds_read_b128 v[32:35], v7 offset:19648\n"
"v_mfma_f32_16x16x32_bf16 v[72:75], v[44:47], v[24:27], v[72:75]\n"
"v_mfma_f32_16x16x32_bf16 v[92:95], v[48:51], v[12:15], v[92:95]\n"
"ds_read_b64_tr_b16 v[44:45], v236 offset:34880\n"
"ds_read_b64_tr_b16 v[46:47], v236 offset:35392\n"
"v_mfma_f32_16x16x32_bf16 v[96:99], v[48:51], v[16:19], v[96:99]\n"
"v_mfma_f32_16x16x32_bf16 v[100:103], v[48:51], v[20:23], v[100:103]\n"
"ds_read_b128 v[36:39], v7 offset:21824\n"
"ds_read_b128 v[40:43], v7 offset:24000\n"
"v_mfma_f32_16x16x32_bf16 v[104:107], v[48:51], v[24:27], v[104:107]\n"
"s_waitcnt lgkmcnt(8)\n"
"v_mfma_f32_16x16x32_bf16 v[76:79], v[52:55], v[12:15], v[76:79]\n"
"ds_read_b64_tr_b16 v[48:49], v236 offset:39232\n"
"ds_read_b64_tr_b16 v[50:51], v236 offset:39744\n"
"v_mfma_f32_16x16x32_bf16 v[80:83], v[52:55], v[16:19], v[80:83]\n"
"v_mfma_f32_16x16x32_bf16 v[84:87], v[52:55], v[20:23], v[84:87]\n"
"v_mov_b32 v7, v196\n"
"v_mfma_f32_16x16x32_bf16 v[88:91], v[52:55], v[24:27], v[88:91]\n"
"s_waitcnt lgkmcnt(2)\n"
"v_mfma_f32_16x16x32_bf16 v[108:111], v[56:59], v[12:15], v[108:111]\n"
"ds_read_b64_tr_b16 v[52:53], v237 offset:34880\n"
"ds_read_b64_tr_b16 v[54:55], v237 offset:35392\n"
"v_mfma_f32_16x16x32_bf16 v[112:115], v[56:59], v[16:19], v[112:115]\n"
"v_mfma_f32_16x16x32_bf16 v[116:119], v[56:59], v[20:23], v[116:119]\n"
"v_mfma_f32_16x16x32_bf16 v[120:123], v[56:59], v[24:27], v[120:123]\n"
"v_mfma_f32_16x16x32_bf16 v[124:127], v[44:47], v[12:15], v[124:127]\n"
"ds_read_b64_tr_b16 v[56:57], v237 offset:39232\n"
"ds_read_b64_tr_b16 v[58:59], v237 offset:39744\n"
"v_mfma_f32_16x16x32_bf16 v[128:131], v[44:47], v[16:19], v[128:131]\n"
"v_mfma_f32_16x16x32_bf16 v[132:135], v[44:47], v[20:23], v[132:135]\n"
"v_mfma_f32_16x16x32_bf16 v[136:139], v[44:47], v[24:27], v[136:139]\n"
"s_waitcnt vmcnt(7) lgkmcnt(4)\n"
"s_barrier\n"
"v_mfma_f32_16x16x32_bf16 v[156:159], v[48:51], v[12:15], v[156:159]\n"
"s_add_u32 m0, s53, 147456\n"
"ds_read_b64_tr_b16 v[44:45], v236 offset:43520\n"
"ds_read_b64_tr_b16 v[46:47], v236 offset:44032\n"
"v_mfma_f32_16x16x32_bf16 v[160:163], v[48:51], v[16:19], v[160:163]\n"
"s_add_u32 s69, s69, 294912\n"
"buffer_load_dwordx4 v9, s[20:23], s69 offen offset:0 lds\n"
"v_mfma_f32_16x16x32_bf16 v[164:167], v[48:51], v[20:23], v[164:167]\n"
"v_mfma_f32_16x16x32_bf16 v[168:171], v[48:51], v[24:27], v[168:171]\n"
"s_waitcnt lgkmcnt(4)\n"
"v_mfma_f32_16x16x32_bf16 v[140:143], v[52:55], v[12:15], v[140:143]\n"
"s_add_u32 m0, s53, 114624\n"
"ds_read_b64_tr_b16 v[48:49], v236 offset:47872\n"
"ds_read_b64_tr_b16 v[50:51], v236 offset:48384\n"
"v_mfma_f32_16x16x32_bf16 v[144:147], v[52:55], v[16:19], v[144:147]\n"
"buffer_load_dwordx4 v9, s[20:23], s69 offen offset:64 lds\n"
"v_mfma_f32_16x16x32_bf16 v[148:151], v[52:55], v[20:23], v[148:151]\n"
"buffer_store_dwordx2 v[242:243], v244, s[28:31], s82 offen offset:0 sc1\n"
"v_mfma_f32_16x16x32_bf16 v[152:155], v[52:55], v[24:27], v[152:155]\n"
"s_waitcnt lgkmcnt(4)\n"
"v_mfma_f32_16x16x32_bf16 v[172:175], v[56:59], v[12:15], v[172:175]\n"
"ds_read_b64_tr_b16 v[52:53], v237 offset:43520\n"
"ds_read_b64_tr_b16 v[54:55], v237 offset:44032\n"
"v_mfma_f32_16x16x32_bf16 v[176:179], v[56:59], v[16:19], v[176:179]\n"
"s_add_u32 m0, s53, 151552\n"
"v_mfma_f32_16x16x32_bf16 v[180:183], v[56:59], v[20:23], v[180:183]\n"
"s_add_u32 s69, s69, 294912\n"
"buffer_load_dwordx4 v9, s[20:23], s69 offen offset:0 lds\n"
"v_mfma_f32_16x16x32_bf16 v[184:187], v[56:59], v[24:27], v[184:187]\n"
"s_waitcnt lgkmcnt(4)\n"
"v_mfma_f32_16x16x32_bf16 v[60:63], v[44:47], v[28:31], v[60:63]\n"
"ds_read_b64_tr_b16 v[56:57], v237 offset:47872\n"
"ds_read_b64_tr_b16 v[58:59], v237 offset:48384\n"
"v_mfma_f32_16x16x32_bf16 v[64:67], v[44:47], v[32:35], v[64:67]\n"
"s_add_u32 m0, s53, 110528\n"
"v_mfma_f32_16x16x32_bf16 v[68:71], v[44:47], v[36:39], v[68:71]\n"
"buffer_load_dwordx4 v9, s[20:23], s69 offen offset:64 lds\n"
"v_mfma_f32_16x16x32_bf16 v[72:75], v[44:47], v[40:43], v[72:75]\n"
"s_waitcnt lgkmcnt(4)\n"
"v_mfma_f32_16x16x32_bf16 v[92:95], v[48:51], v[28:31], v[92:95]\n"
"ds_read_b64_tr_b16 v[44:45], v236 offset:43584\n"
"ds_read_b64_tr_b16 v[46:47], v236 offset:44096\n"
"v_mfma_f32_16x16x32_bf16 v[96:99], v[48:51], v[32:35], v[96:99]\n"
"s_add_u32 m0, s53, 155648\n"
"v_mfma_f32_16x16x32_bf16 v[100:103], v[48:51], v[36:39], v[100:103]\n"
"s_add_u32 s69, s69, 294912\n"
"buffer_load_dwordx4 v9, s[20:23], s69 offen offset:0 lds\n"
"v_mfma_f32_16x16x32_bf16 v[104:107], v[48:51], v[40:43], v[104:107]\n"
"s_waitcnt lgkmcnt(4)\n"
"v_mfma_f32_16x16x32_bf16 v[76:79], v[52:55], v[28:31], v[76:79]\n"
"ds_read_b64_tr_b16 v[48:49], v236 offset:47936\n"
"ds_read_b64_tr_b16 v[50:51], v236 offset:48448\n"
"v_mfma_f32_16x16x32_bf16 v[80:83], v[52:55], v[32:35], v[80:83]\n"
"s_add_u32 m0, s53, 126912\n"
"v_mfma_f32_16x16x32_bf16 v[84:87], v[52:55], v[36:39], v[84:87]\n"
"buffer_load_dwordx4 v9, s[20:23], s69 offen offset:64 lds\n"
"v_mfma_f32_16x16x32_bf16 v[88:91], v[52:55], v[40:43], v[88:91]\n"
"s_waitcnt lgkmcnt(4)\n"
"v_mfma_f32_16x16x32_bf16 v[108:111], v[56:59], v[28:31], v[108:111]\n"
"ds_read_b64_tr_b16 v[52:53], v237 offset:43584\n"
"ds_read_b64_tr_b16 v[54:55], v237 offset:44096\n"
"v_mfma_f32_16x16x32_bf16 v[112:115], v[56:59], v[32:35], v[112:115]\n"
"v_mfma_f32_16x16x32_bf16 v[116:119], v[56:59], v[36:39], v[116:119]\n"
"v_mfma_f32_16x16x32_bf16 v[120:123], v[56:59], v[40:43], v[120:123]\n"
"s_waitcnt lgkmcnt(4)\n"
"v_mfma_f32_16x16x32_bf16 v[124:127], v[44:47], v[28:31], v[124:127]\n"
"ds_read_b64_tr_b16 v[56:57], v237 offset:47936\n"
"ds_read_b64_tr_b16 v[58:59], v237 offset:48448\n"
"v_mfma_f32_16x16x32_bf16 v[128:131], v[44:47], v[32:35], v[128:131]\n"
"v_mfma_f32_16x16x32_bf16 v[132:135], v[44:47], v[36:39], v[132:135]\n"
"v_mfma_f32_16x16x32_bf16 v[136:139], v[44:47], v[40:43], v[136:139]\n"
"s_waitcnt vmcnt(12) lgkmcnt(4)\n"
"s_barrier\n"
"v_mfma_f32_16x16x32_bf16 v[156:159], v[48:51], v[28:31], v[156:159]\n"
"ds_read_b64_tr_b16 v[44:45], v3 offset:28672\n"
"ds_read_b64_tr_b16 v[46:47], v3 offset:28928\n"
"v_mfma_f32_16x16x32_bf16 v[160:163], v[48:51], v[32:35], v[160:163]\n"
"v_mfma_f32_16x16x32_bf16 v[164:167], v[48:51], v[36:39], v[164:167]\n"
"v_mfma_f32_16x16x32_bf16 v[168:171], v[48:51], v[40:43], v[168:171]\n"
"s_waitcnt lgkmcnt(4)\n"
"v_mfma_f32_16x16x32_bf16 v[140:143], v[52:55], v[28:31], v[140:143]\n"
"ds_read_b64_tr_b16 v[48:49], v3 offset:30720\n"
"ds_read_b64_tr_b16 v[50:51], v3 offset:30976\n"
"v_mfma_f32_16x16x32_bf16 v[144:147], v[52:55], v[32:35], v[144:147]\n"
"v_mfma_f32_16x16x32_bf16 v[148:151], v[52:55], v[36:39], v[148:151]\n"
"v_mfma_f32_16x16x32_bf16 v[152:155], v[52:55], v[40:43], v[152:155]\n"
"s_waitcnt lgkmcnt(4)\n"
"v_mfma_f32_16x16x32_bf16 v[172:175], v[56:59], v[28:31], v[172:175]\n"
"ds_read_b64_tr_b16 v[52:53], v6 offset:28672\n"
"ds_read_b64_tr_b16 v[54:55], v6 offset:28928\n"
"v_mfma_f32_16x16x32_bf16 v[176:179], v[56:59], v[32:35], v[176:179]\n"
"v_mfma_f32_16x16x32_bf16 v[180:183], v[56:59], v[36:39], v[180:183]\n"
"v_mfma_f32_16x16x32_bf16 v[184:187], v[56:59], v[40:43], v[184:187]\n"
"s_waitcnt lgkmcnt(4)\n"
"v_mfma_f32_16x16x32_bf16 v[188:191], v[44:47], a[0:3], 0\n"
"ds_read_b64_tr_b16 v[56:57], v6 offset:30720\n"
"ds_read_b64_tr_b16 v[58:59], v6 offset:30976\n"
"v_mfma_f32_16x16x32_bf16 v[192:195], v[44:47], a[4:7], 0\n"
"v_mfma_f32_16x16x32_bf16 v[196:199], v[44:47], a[8:11], 0\n"
"v_mfma_f32_16x16x32_bf16 v[200:203], v[44:47], a[12:15], 0\n"
"s_waitcnt vmcnt(8) lgkmcnt(4)\n"
"s_barrier\n"
"v_mfma_f32_16x16x32_bf16 v[220:223], v[48:51], a[0:3], 0\n"
"ds_read_b64_tr_b16 v[44:45], v3 offset:12288\n"
"ds_read_b64_tr_b16 v[46:47], v3 offset:12544\n"
"v_mfma_f32_16x16x32_bf16 v[224:227], v[48:51], a[4:7], 0\n"
"v_mul_f32 v60, s80, v60\n"
"v_mul_f32 v61, s80, v61\n"
"v_mfma_f32_16x16x32_bf16 v[228:231], v[48:51], a[8:11], 0\n"
"v_exp_f32 v60, v60\n"
"v_mfma_f32_16x16x32_bf16 v[232:235], v[48:51], a[12:15], 0\n"
"s_waitcnt lgkmcnt(4)\n"
"v_exp_f32 v61, v61\n"
"v_mfma_f32_16x16x32_bf16 v[204:207], v[52:55], a[0:3], 0\n"
"ds_read_b64_tr_b16 v[48:49], v3 offset:14336\n"
"ds_read_b64_tr_b16 v[50:51], v3 offset:14592\n"
"v_mfma_f32_16x16x32_bf16 v[208:211], v[52:55], a[4:7], 0\n"
"v_add_f32 v60, 1.0, v60\n"
"v_add_f32 v61, 1.0, v61\n"
"v_mfma_f32_16x16x32_bf16 v[212:215], v[52:55], a[8:11], 0\n"
"v_rcp_f32 v60, v60\n"
"v_mfma_f32_16x16x32_bf16 v[216:219], v[52:55], a[12:15], 0\n"
"s_waitcnt lgkmcnt(4)\n"
"v_rcp_f32 v61, v61\n"
"v_mfma_f32_16x16x32_bf16 v[236:239], v[56:59], a[0:3], 0\n"
"ds_read_b64_tr_b16 v[52:53], v6 offset:12288\n"
"ds_read_b64_tr_b16 v[54:55], v6 offset:12544\n"
"v_mfma_f32_16x16x32_bf16 v[240:243], v[56:59], a[4:7], 0\n"
"v_mul_f32 v60, v60, v92\n"
"v_mul_f32 v61, v61, v93\n"
"v_mfma_f32_16x16x32_bf16 v[244:247], v[56:59], a[8:11], 0\n"
"v_mul_f32 v62, s80, v62\n"
"v_mul_f32 v63, s80, v63\n"
"v_mfma_f32_16x16x32_bf16 v[248:251], v[56:59], a[12:15], 0\n"
"s_waitcnt lgkmcnt(4)\n"
"v_exp_f32 v62, v62\n"
"v_mfma_f32_16x16x32_bf16 v[188:191], v[44:47], a[16:19], v[188:191]\n"
"ds_read_b64_tr_b16 v[56:57], v6 offset:14336\n"
"ds_read_b64_tr_b16 v[58:59], v6 offset:14592\n"
"v_mfma_f32_16x16x32_bf16 v[192:195], v[44:47], a[20:23], v[192:195]\n"
"v_exp_f32 v63, v63\n"
"v_mfma_f32_16x16x32_bf16 v[196:199], v[44:47], a[24:27], v[196:199]\n"
"v_add_f32 v62, 1.0, v62\n"
"v_add_f32 v63, 1.0, v63\n"
"v_mfma_f32_16x16x32_bf16 v[200:203], v[44:47], a[28:31], v[200:203]\n"
"s_waitcnt lgkmcnt(4)\n"
"v_rcp_f32 v62, v62\n"
"v_mfma_f32_16x16x32_bf16 v[220:223], v[48:51], a[16:19], v[220:223]\n"
"ds_read_b64_tr_b16 v[44:45], v3 offset:8192\n"
"ds_read_b64_tr_b16 v[46:47], v3 offset:8448\n"
"v_mfma_f32_16x16x32_bf16 v[224:227], v[48:51], a[20:23], v[224:227]\n"
"v_rcp_f32 v63, v63\n"
"v_mfma_f32_16x16x32_bf16 v[228:231], v[48:51], a[24:27], v[228:231]\n"
"v_mul_f32 v62, v62, v94\n"
"v_mul_f32 v63, v63, v95\n"
"v_mfma_f32_16x16x32_bf16 v[232:235], v[48:51], a[28:31], v[232:235]\n"
"s_waitcnt lgkmcnt(4)\n"
"v_mul_f32 v76, s80, v76\n"
"v_mul_f32 v77, s80, v77\n"
"v_mfma_f32_16x16x32_bf16 v[204:207], v[52:55], a[16:19], v[204:207]\n"
"ds_read_b64_tr_b16 v[48:49], v3 offset:10240\n"
"ds_read_b64_tr_b16 v[50:51], v3 offset:10496\n"
"v_mfma_f32_16x16x32_bf16 v[208:211], v[52:55], a[20:23], v[208:211]\n"
"v_exp_f32 v76, v76\n"
"v_mfma_f32_16x16x32_bf16 v[212:215], v[52:55], a[24:27], v[212:215]\n"
"v_exp_f32 v77, v77\n"
"v_mfma_f32_16x16x32_bf16 v[216:219], v[52:55], a[28:31], v[216:219]\n"
"s_waitcnt lgkmcnt(4)\n"
"v_add_f32 v76, 1.0, v76\n"
"v_add_f32 v77, 1.0, v77\n"
"v_mfma_f32_16x16x32_bf16 v[236:239], v[56:59], a[16:19], v[236:239]\n"
"ds_read_b64_tr_b16 v[52:53], v6 offset:8192\n"
"ds_read_b64_tr_b16 v[54:55], v6 offset:8448\n"
"v_mfma_f32_16x16x32_bf16 v[240:243], v[56:59], a[20:23], v[240:243]\n"
"v_rcp_f32 v76, v76\n"
"v_mfma_f32_16x16x32_bf16 v[244:247], v[56:59], a[24:27], v[244:247]\n"
"v_rcp_f32 v77, v77\n"
"v_mfma_f32_16x16x32_bf16 v[248:251], v[56:59], a[28:31], v[248:251]\n"
"s_waitcnt lgkmcnt(4)\n"
"v_mul_f32 v76, v76, v108\n"
"v_mul_f32 v77, v77, v109\n"
"v_mfma_f32_16x16x32_bf16 v[188:191], v[44:47], a[32:35], v[188:191]\n"
"ds_read_b64_tr_b16 v[56:57], v6 offset:10240\n"
"ds_read_b64_tr_b16 v[58:59], v6 offset:10496\n"
"v_mfma_f32_16x16x32_bf16 v[192:195], v[44:47], a[36:39], v[192:195]\n"
"v_mul_f32 v78, s80, v78\n"
"v_mul_f32 v79, s80, v79\n"
"v_mfma_f32_16x16x32_bf16 v[196:199], v[44:47], a[40:43], v[196:199]\n"
"v_exp_f32 v78, v78\n"
"v_mfma_f32_16x16x32_bf16 v[200:203], v[44:47], a[44:47], v[200:203]\n"
"s_waitcnt vmcnt(3) lgkmcnt(4)\n"
"s_barrier\n"
"v_add_f32 v78, 1.0, v78\n"
"v_mfma_f32_16x16x32_bf16 v[220:223], v[48:51], a[32:35], v[220:223]\n"
"s_add_u32 m0, s53, 143360\n"
"ds_read_b64_tr_b16 v[44:45], v3 offset:16384\n"
"ds_read_b64_tr_b16 v[46:47], v3 offset:16640\n"
"v_mfma_f32_16x16x32_bf16 v[224:227], v[48:51], a[36:39], v[224:227]\n"
"s_add_u32 s69, s69, s92\n"
"buffer_load_dwordx4 v9, s[20:23], s69 offen offset:0 lds\n"
"v_mfma_f32_16x16x32_bf16 v[228:231], v[48:51], a[40:43], v[228:231]\n"
"v_exp_f32 v79, v79\n"
"v_mfma_f32_16x16x32_bf16 v[232:235], v[48:51], a[44:47], v[232:235]\n"
"s_waitcnt lgkmcnt(4)\n"
"v_rcp_f32 v78, v78\n"
"v_mfma_f32_16x16x32_bf16 v[204:207], v[52:55], a[32:35], v[204:207]\n"
"s_add_u32 m0, s53, 122816\n"
"ds_read_b64_tr_b16 v[48:49], v3 offset:18432\n"
"ds_read_b64_tr_b16 v[50:51], v3 offset:18688\n"
"v_mfma_f32_16x16x32_bf16 v[208:211], v[52:55], a[36:39], v[208:211]\n"
"buffer_load_dwordx4 v9, s[20:23], s69 offen offset:64 lds\n"
"v_mfma_f32_16x16x32_bf16 v[212:215], v[52:55], a[40:43], v[212:215]\n"
"v_add_f32 v79, 1.0, v79\n"
"v_mul_f32 v78, v78, v110\n"
"v_mfma_f32_16x16x32_bf16 v[216:219], v[52:55], a[44:47], v[216:219]\n"
"s_waitcnt lgkmcnt(4)\n"
"v_rcp_f32 v79, v79\n"
"v_mfma_f32_16x16x32_bf16 v[236:239], v[56:59], a[32:35], v[236:239]\n"
"s_add_u32 m0, s91, 32\n"
"ds_read_b64_tr_b16 v[52:53], v6 offset:16384\n"
"ds_read_b64_tr_b16 v[54:55], v6 offset:16640\n"
"v_mfma_f32_16x16x32_bf16 v[240:243], v[56:59], a[36:39], v[240:243]\n"
"s_add_u32 s84, s96, 0\n"
"buffer_load_dwordx4 v10, s[16:19], s84 offen offset:0 lds\n"
"v_mfma_f32_16x16x32_bf16 v[244:247], v[56:59], a[40:43], v[244:247]\n"
"v_mul_f32 v79, v79, v111\n"
"v_cvt_pk_bf16_f32 v92, v60, v61\n"
"v_mfma_f32_16x16x32_bf16 v[248:251], v[56:59], a[44:47], v[248:251]\n"
"s_waitcnt lgkmcnt(4)\n"
"v_cvt_pk_bf16_f32 v93, v62, v63\n"
"v_cvt_pk_bf16_f32 v94, v76, v77\n"
"v_mfma_f32_16x16x32_bf16 v[188:191], v[44:47], a[48:51], v[188:191]\n"
"s_add_u32 m0, s53, 159744\n"
"ds_read_b64_tr_b16 v[56:57], v6 offset:18432\n"
"ds_read_b64_tr_b16 v[58:59], v6 offset:18688\n"
"v_mfma_f32_16x16x32_bf16 v[192:195], v[44:47], a[52:55], v[192:195]\n"
"s_add_u32 s69, s69, 294912\n"
"buffer_load_dwordx4 v9, s[20:23], s69 offen offset:0 lds\n"
"v_mfma_f32_16x16x32_bf16 v[196:199], v[44:47], a[56:59], v[196:199]\n"
"v_cvt_pk_bf16_f32 v95, v78, v79\n"
"v_mul_f32 v64, s80, v64\n"
"v_mfma_f32_16x16x32_bf16 v[200:203], v[44:47], a[60:63], v[200:203]\n"
"s_waitcnt lgkmcnt(4)\n"
"v_exp_f32 v64, v64\n"
"v_mfma_f32_16x16x32_bf16 v[220:223], v[48:51], a[48:51], v[220:223]\n"
"s_add_u32 m0, s53, 118720\n"
"ds_read_b64_tr_b16 v[44:45], v3 offset:20480\n"
"ds_read_b64_tr_b16 v[46:47], v3 offset:20736\n"
"v_mfma_f32_16x16x32_bf16 v[224:227], v[48:51], a[52:55], v[224:227]\n"
"buffer_load_dwordx4 v9, s[20:23], s69 offen offset:64 lds\n"
"v_mfma_f32_16x16x32_bf16 v[228:231], v[48:51], a[56:59], v[228:231]\n"
"v_mul_f32 v65, s80, v65\n"
"v_add_f32 v64, 1.0, v64\n"
"v_mfma_f32_16x16x32_bf16 v[232:235], v[48:51], a[60:63], v[232:235]\n"
"s_waitcnt lgkmcnt(4)\n"
"v_exp_f32 v65, v65\n"
"v_mfma_f32_16x16x32_bf16 v[204:207], v[52:55], a[48:51], v[204:207]\n"
"s_add_u32 m0, s91, 32\n"
"ds_read_b64_tr_b16 v[48:49], v3 offset:22528\n"
"ds_read_b64_tr_b16 v[50:51], v3 offset:22784\n"
"v_mfma_f32_16x16x32_bf16 v[208:211], v[52:55], a[52:55], v[208:211]\n"
"s_add_u32 s84, s96, 0\n"
"buffer_load_dwordx4 v10, s[16:19], s84 offen offset:1024 lds\n"
"v_mfma_f32_16x16x32_bf16 v[212:215], v[52:55], a[56:59], v[212:215]\n"
"v_rcp_f32 v64, v64\n"
"v_mfma_f32_16x16x32_bf16 v[216:219], v[52:55], a[60:63], v[216:219]\n"
"s_waitcnt lgkmcnt(4)\n"
"v_add_f32 v65, 1.0, v65\n"
"v_mul_f32 v64, v64, v96\n"
"v_mfma_f32_16x16x32_bf16 v[236:239], v[56:59], a[48:51], v[236:239]\n"
"ds_read_b64_tr_b16 v[52:53], v6 offset:20480\n"
"ds_read_b64_tr_b16 v[54:55], v6 offset:20736\n"
"v_mfma_f32_16x16x32_bf16 v[240:243], v[56:59], a[52:55], v[240:243]\n"
"v_rcp_f32 v65, v65\n"
"v_mfma_f32_16x16x32_bf16 v[244:247], v[56:59], a[56:59], v[244:247]\n"
"v_mul_f32 v65, v65, v97\n"
"v_mul_f32 v66, s80, v66\n"
"v_mfma_f32_16x16x32_bf16 v[248:251], v[56:59], a[60:63], v[248:251]\n"
"s_waitcnt lgkmcnt(4)\n"
"v_exp_f32 v66, v66\n"
"v_mfma_f32_16x16x32_bf16 v[188:191], v[44:47], a[64:67], v[188:191]\n"
"ds_read_b64_tr_b16 v[56:57], v6 offset:22528\n"
"ds_read_b64_tr_b16 v[58:59], v6 offset:22784\n"
"v_mfma_f32_16x16x32_bf16 v[192:195], v[44:47], a[68:71], v[192:195]\n"
"v_mul_f32 v67, s80, v67\n"
"v_add_f32 v66, 1.0, v66\n"
"v_mfma_f32_16x16x32_bf16 v[196:199], v[44:47], a[72:75], v[196:199]\n"
"v_exp_f32 v67, v67\n"
"v_mfma_f32_16x16x32_bf16 v[200:203], v[44:47], a[76:79], v[200:203]\n"
"s_waitcnt vmcnt(5) lgkmcnt(4)\n"
"s_barrier\n"
"v_add_f32 v67, 1.0, v67\n"
"v_mfma_f32_16x16x32_bf16 v[220:223], v[48:51], a[64:67], v[220:223]\n"
"s_add_u32 m0, s53, 139264\n"
"ds_read_b64_tr_b16 v[44:45], v3 offset:24576\n"
"ds_read_b64_tr_b16 v[46:47], v3 offset:24832\n"
"v_mfma_f32_16x16x32_bf16 v[224:227], v[48:51], a[68:71], v[224:227]\n"
"s_add_u32 s69, s69, 294912\n"
"buffer_load_dwordx4 v9, s[20:23], s69 offen offset:0 lds\n"
"v_mfma_f32_16x16x32_bf16 v[228:231], v[48:51], a[72:75], v[228:231]\n"
"v_rcp_f32 v66, v66\n"
"v_mfma_f32_16x16x32_bf16 v[232:235], v[48:51], a[76:79], v[232:235]\n"
"s_waitcnt lgkmcnt(4)\n"
"v_rcp_f32 v67, v67\n"
"v_mfma_f32_16x16x32_bf16 v[204:207], v[52:55], a[64:67], v[204:207]\n"
"s_add_u32 m0, s53, 106432\n"
"ds_read_b64_tr_b16 v[48:49], v3 offset:26624\n"
"ds_read_b64_tr_b16 v[50:51], v3 offset:26880\n"
"v_mfma_f32_16x16x32_bf16 v[208:211], v[52:55], a[68:71], v[208:211]\n"
"buffer_load_dwordx4 v9, s[20:23], s69 offen offset:64 lds\n"
"v_mfma_f32_16x16x32_bf16 v[212:215], v[52:55], a[72:75], v[212:215]\n"
"v_mul_f32 v66, v66, v98\n"
"v_mul_f32 v67, v67, v99\n"
"v_mfma_f32_16x16x32_bf16 v[216:219], v[52:55], a[76:79], v[216:219]\n"
"s_waitcnt lgkmcnt(4)\n"
"v_mul_f32 v80, s80, v80\n"
"v_mul_f32 v81, s80, v81\n"
"v_mfma_f32_16x16x32_bf16 v[236:239], v[56:59], a[64:67], v[236:239]\n"
"s_add_u32 m0, s91, 32\n"
"ds_read_b64_tr_b16 v[52:53], v6 offset:24576\n"
"ds_read_b64_tr_b16 v[54:55], v6 offset:24832\n"
"v_mfma_f32_16x16x32_bf16 v[240:243], v[56:59], a[68:71], v[240:243]\n"
"s_add_u32 s84, s96, 0\n"
"buffer_load_dwordx4 v10, s[16:19], s84 offen offset:2048 lds\n"
"v_mfma_f32_16x16x32_bf16 v[244:247], v[56:59], a[72:75], v[244:247]\n"
"v_exp_f32 v80, v80\n"
"v_mfma_f32_16x16x32_bf16 v[248:251], v[56:59], a[76:79], v[248:251]\n"
"s_waitcnt lgkmcnt(4)\n"
"v_exp_f32 v81, v81\n"
"v_mfma_f32_16x16x32_bf16 v[188:191], v[44:47], a[80:83], v[188:191]\n"
"s_add_u32 m0, s53, 147456\n"
"ds_read_b64_tr_b16 v[56:57], v6 offset:26624\n"
"ds_read_b64_tr_b16 v[58:59], v6 offset:26880\n"
"v_mfma_f32_16x16x32_bf16 v[192:195], v[44:47], a[84:87], v[192:195]\n"
"s_add_u32 s69, s69, 294912\n"
"buffer_load_dwordx4 v9, s[20:23], s69 offen offset:0 lds\n"
"v_mfma_f32_16x16x32_bf16 v[196:199], v[44:47], a[88:91], v[196:199]\n"
"v_add_f32 v80, 1.0, v80\n"
"v_add_f32 v81, 1.0, v81\n"
"v_mfma_f32_16x16x32_bf16 v[200:203], v[44:47], a[92:95], v[200:203]\n"
"s_waitcnt lgkmcnt(4)\n"
"v_rcp_f32 v80, v80\n"
"v_mfma_f32_16x16x32_bf16 v[220:223], v[48:51], a[80:83], v[220:223]\n"
"s_add_u32 m0, s53, 102336\n"
"ds_read_b64_tr_b16 v[44:45], v3 offset:12288\n"
"ds_read_b64_tr_b16 v[46:47], v3 offset:12544\n"
"v_mfma_f32_16x16x32_bf16 v[224:227], v[48:51], a[84:87], v[224:227]\n"
"buffer_load_dwordx4 v9, s[20:23], s69 offen offset:64 lds\n"
"v_mfma_f32_16x16x32_bf16 v[228:231], v[48:51], a[88:91], v[228:231]\n"
"v_rcp_f32 v81, v81\n"
"v_mfma_f32_16x16x32_bf16 v[232:235], v[48:51], a[92:95], v[232:235]\n"
"s_waitcnt lgkmcnt(4)\n"
"v_mul_f32 v80, v80, v112\n"
"v_mul_f32 v81, v81, v113\n"
"v_mfma_f32_16x16x32_bf16 v[204:207], v[52:55], a[80:83], v[204:207]\n"
"s_add_u32 m0, s91, 32\n"
"ds_read_b64_tr_b16 v[48:49], v3 offset:14336\n"
"ds_read_b64_tr_b16 v[50:51], v3 offset:14592\n"
"v_mfma_f32_16x16x32_bf16 v[208:211], v[52:55], a[84:87], v[208:211]\n"
"s_add_u32 s84, s96, 0\n"
"buffer_load_dwordx4 v10, s[16:19], s84 offen offset:3072 lds\n"
"v_mfma_f32_16x16x32_bf16 v[212:215], v[52:55], a[88:91], v[212:215]\n"
"v_mul_f32 v82, s80, v82\n"
"v_mul_f32 v83, s80, v83\n"
"v_mfma_f32_16x16x32_bf16 v[216:219], v[52:55], a[92:95], v[216:219]\n"
"s_waitcnt lgkmcnt(4)\n"
"v_exp_f32 v82, v82\n"
"v_mfma_f32_16x16x32_bf16 v[236:239], v[56:59], a[80:83], v[236:239]\n"
"ds_read_b64_tr_b16 v[52:53], v6 offset:12288\n"
"ds_read_b64_tr_b16 v[54:55], v6 offset:12544\n"
"v_mfma_f32_16x16x32_bf16 v[240:243], v[56:59], a[84:87], v[240:243]\n"
"v_exp_f32 v83, v83\n"
"v_mfma_f32_16x16x32_bf16 v[244:247], v[56:59], a[88:91], v[244:247]\n"
"v_add_f32 v82, 1.0, v82\n"
"v_add_f32 v83, 1.0, v83\n"
"v_mfma_f32_16x16x32_bf16 v[248:251], v[56:59], a[92:95], v[248:251]\n"
"s_waitcnt lgkmcnt(4)\n"
"v_rcp_f32 v82, v82\n"
"v_mfma_f32_16x16x32_bf16 v[188:191], v[44:47], a[96:99], v[188:191]\n"
"ds_read_b64_tr_b16 v[56:57], v6 offset:14336\n"
"ds_read_b64_tr_b16 v[58:59], v6 offset:14592\n"
"v_mfma_f32_16x16x32_bf16 v[192:195], v[44:47], a[100:103], v[192:195]\n"
"v_rcp_f32 v83, v83\n"
"v_mfma_f32_16x16x32_bf16 v[196:199], v[44:47], a[104:107], v[196:199]\n"
"v_mul_f32 v82, v82, v114\n"
"v_mul_f32 v83, v83, v115\n"
"v_mfma_f32_16x16x32_bf16 v[200:203], v[44:47], a[108:111], v[200:203]\n"
"s_waitcnt vmcnt(5) lgkmcnt(4)\n"
"s_barrier\n"
"v_cvt_pk_bf16_f32 v96, v64, v65\n"
"v_mfma_f32_16x16x32_bf16 v[220:223], v[48:51], a[96:99], v[220:223]\n"
"s_add_u32 m0, s53, 151552\n"
"ds_read_b64_tr_b16 v[44:45], v3 offset:28672\n"
"ds_read_b64_tr_b16 v[46:47], v3 offset:28928\n"
"v_mfma_f32_16x16x32_bf16 v[224:227], v[48:51], a[100:103], v[224:227]\n"
"s_add_u32 s69, s69, 294912\n"
"buffer_load_dwordx4 v9, s[20:23], s69 offen offset:0 lds\n"
"v_mfma_f32_16x16x32_bf16 v[228:231], v[48:51], a[104:107], v[228:231]\n"
"v_cvt_pk_bf16_f32 v97, v66, v67\n"
"v_cvt_pk_bf16_f32 v98, v80, v81\n"
"v_mfma_f32_16x16x32_bf16 v[232:235], v[48:51], a[108:111], v[232:235]\n"
"s_waitcnt lgkmcnt(4)\n"
"v_cvt_pk_bf16_f32 v99, v82, v83\n"
"v_mul_f32 v68, s80, v68\n"
"v_mfma_f32_16x16x32_bf16 v[204:207], v[52:55], a[96:99], v[204:207]\n"
"s_add_u32 m0, s53, 98240\n"
"ds_read_b64_tr_b16 v[48:49], v3 offset:30720\n"
"ds_read_b64_tr_b16 v[50:51], v3 offset:30976\n"
"v_mfma_f32_16x16x32_bf16 v[208:211], v[52:55], a[100:103], v[208:211]\n"
"buffer_load_dwordx4 v9, s[20:23], s69 offen offset:64 lds\n"
"v_mfma_f32_16x16x32_bf16 v[212:215], v[52:55], a[104:107], v[212:215]\n"
"v_exp_f32 v68, v68\n"
"v_mfma_f32_16x16x32_bf16 v[216:219], v[52:55], a[108:111], v[216:219]\n"
"s_waitcnt lgkmcnt(4)\n"
"v_mul_f32 v69, s80, v69\n"
"v_add_f32 v68, 1.0, v68\n"
"v_mfma_f32_16x16x32_bf16 v[236:239], v[56:59], a[96:99], v[236:239]\n"
"s_add_u32 m0, s91, 21248\n"
"ds_read_b64_tr_b16 v[52:53], v6 offset:28672\n"
"ds_read_b64_tr_b16 v[54:55], v6 offset:28928\n"
"v_mfma_f32_16x16x32_bf16 v[240:243], v[56:59], a[100:103], v[240:243]\n"
"s_add_u32 s84, s96, 4096\n"
"buffer_load_dwordx4 v10, s[16:19], s84 offen offset:0 lds\n"
"v_mfma_f32_16x16x32_bf16 v[244:247], v[56:59], a[104:107], v[244:247]\n"
"v_exp_f32 v69, v69\n"
"v_mfma_f32_16x16x32_bf16 v[248:251], v[56:59], a[108:111], v[248:251]\n"
"s_waitcnt lgkmcnt(4)\n"
"v_rcp_f32 v68, v68\n"
"v_mfma_f32_16x16x32_bf16 v[188:191], v[44:47], a[112:115], v[188:191]\n"
"s_add_u32 m0, s53, 155648\n"
"ds_read_b64_tr_b16 v[56:57], v6 offset:30720\n"
"ds_read_b64_tr_b16 v[58:59], v6 offset:30976\n"
"v_mfma_f32_16x16x32_bf16 v[192:195], v[44:47], a[116:119], v[192:195]\n"
"s_add_u32 s69, s69, 294912\n"
"buffer_load_dwordx4 v9, s[20:23], s69 offen offset:0 lds\n"
"v_mfma_f32_16x16x32_bf16 v[196:199], v[44:47], a[120:123], v[196:199]\n"
"v_add_f32 v69, 1.0, v69\n"
"v_mul_f32 v68, v68, v100\n"
"v_mfma_f32_16x16x32_bf16 v[200:203], v[44:47], a[124:127], v[200:203]\n"
"s_waitcnt lgkmcnt(4)\n"
"v_rcp_f32 v69, v69\n"
"v_mfma_f32_16x16x32_bf16 v[220:223], v[48:51], a[112:115], v[220:223]\n"
"s_add_u32 m0, s53, 94144\n"
"ds_read_b64_tr_b16 v[44:45], v3 offset:8192\n"
"ds_read_b64_tr_b16 v[46:47], v3 offset:8448\n"
"v_mfma_f32_16x16x32_bf16 v[224:227], v[48:51], a[116:119], v[224:227]\n"
"buffer_load_dwordx4 v9, s[20:23], s69 offen offset:64 lds\n"
"v_mfma_f32_16x16x32_bf16 v[228:231], v[48:51], a[120:123], v[228:231]\n"
"v_mul_f32 v69, v69, v101\n"
"v_mul_f32 v70, s80, v70\n"
"v_mfma_f32_16x16x32_bf16 v[232:235], v[48:51], a[124:127], v[232:235]\n"
"s_waitcnt lgkmcnt(4)\n"
"v_exp_f32 v70, v70\n"
"v_mfma_f32_16x16x32_bf16 v[204:207], v[52:55], a[112:115], v[204:207]\n"
"s_add_u32 m0, s91, 21248\n"
"ds_read_b64_tr_b16 v[48:49], v3 offset:10240\n"
"ds_read_b64_tr_b16 v[50:51], v3 offset:10496\n"
"v_mfma_f32_16x16x32_bf16 v[208:211], v[52:55], a[116:119], v[208:211]\n"
"s_add_u32 s84, s96, 4096\n"
"buffer_load_dwordx4 v10, s[16:19], s84 offen offset:1024 lds\n"
"v_mfma_f32_16x16x32_bf16 v[212:215], v[52:55], a[120:123], v[212:215]\n"
"v_mul_f32 v71, s80, v71\n"
"v_add_f32 v70, 1.0, v70\n"
"v_mfma_f32_16x16x32_bf16 v[216:219], v[52:55], a[124:127], v[216:219]\n"
"s_waitcnt lgkmcnt(4)\n"
"v_exp_f32 v71, v71\n"
"v_mfma_f32_16x16x32_bf16 v[236:239], v[56:59], a[112:115], v[236:239]\n"
"ds_read_b64_tr_b16 v[52:53], v6 offset:8192\n"
"ds_read_b64_tr_b16 v[54:55], v6 offset:8448\n"
"v_mfma_f32_16x16x32_bf16 v[240:243], v[56:59], a[116:119], v[240:243]\n"
"v_rcp_f32 v70, v70\n"
"v_mfma_f32_16x16x32_bf16 v[244:247], v[56:59], a[120:123], v[244:247]\n"
"v_add_f32 v71, 1.0, v71\n"
"v_mul_f32 v70, v70, v102\n"
"v_mfma_f32_16x16x32_bf16 v[248:251], v[56:59], a[124:127], v[248:251]\n"
"s_waitcnt lgkmcnt(4)\n"
"v_rcp_f32 v71, v71\n"
"v_mfma_f32_16x16x32_bf16 v[188:191], v[44:47], a[128:131], v[188:191]\n"
"ds_read_b64_tr_b16 v[56:57], v6 offset:10240\n"
"ds_read_b64_tr_b16 v[58:59], v6 offset:10496\n"
"v_mfma_f32_16x16x32_bf16 v[192:195], v[44:47], a[132:135], v[192:195]\n"
"v_mul_f32 v71, v71, v103\n"
"v_mul_f32 v84, s80, v84\n"
"v_mfma_f32_16x16x32_bf16 v[196:199], v[44:47], a[136:139], v[196:199]\n"
"v_exp_f32 v84, v84\n"
"v_mfma_f32_16x16x32_bf16 v[200:203], v[44:47], a[140:143], v[200:203]\n"
"s_waitcnt vmcnt(5) lgkmcnt(4)\n"
"s_barrier\n"
"v_mul_f32 v85, s80, v85\n"
"v_mfma_f32_16x16x32_bf16 v[220:223], v[48:51], a[128:131], v[220:223]\n"
"s_add_u32 m0, s53, 143360\n"
"ds_read_b64_tr_b16 v[44:45], v3 offset:16384\n"
"ds_read_b64_tr_b16 v[46:47], v3 offset:16640\n"
"v_mfma_f32_16x16x32_bf16 v[224:227], v[48:51], a[132:135], v[224:227]\n"
"s_add_u32 s69, s69, s93\n"
"buffer_load_dwordx4 v9, s[20:23], s69 offen offset:0 lds\n"
"v_mfma_f32_16x16x32_bf16 v[228:231], v[48:51], a[136:139], v[228:231]\n"
"v_exp_f32 v85, v85\n"
"v_mfma_f32_16x16x32_bf16 v[232:235], v[48:51], a[140:143], v[232:235]\n"
"s_waitcnt lgkmcnt(4)\n"
"v_add_f32 v84, 1.0, v84\n"
"v_add_f32 v85, 1.0, v85\n"
"v_mfma_f32_16x16x32_bf16 v[204:207], v[52:55], a[128:131], v[204:207]\n"
"s_add_u32 m0, s53, 65472\n"
"ds_read_b64_tr_b16 v[48:49], v3 offset:18432\n"
"ds_read_b64_tr_b16 v[50:51], v3 offset:18688\n"
"v_mfma_f32_16x16x32_bf16 v[208:211], v[52:55], a[132:135], v[208:211]\n"
"buffer_load_dwordx4 v9, s[20:23], s69 offen offset:64 lds\n"
"v_mfma_f32_16x16x32_bf16 v[212:215], v[52:55], a[136:139], v[212:215]\n"
"v_rcp_f32 v84, v84\n"
"v_mfma_f32_16x16x32_bf16 v[216:219], v[52:55], a[140:143], v[216:219]\n"
"s_waitcnt lgkmcnt(4)\n"
"v_rcp_f32 v85, v85\n"
"v_mfma_f32_16x16x32_bf16 v[236:239], v[56:59], a[128:131], v[236:239]\n"
"s_add_u32 m0, s91, 21248\n"
"ds_read_b64_tr_b16 v[52:53], v6 offset:16384\n"
"ds_read_b64_tr_b16 v[54:55], v6 offset:16640\n"
"v_mfma_f32_16x16x32_bf16 v[240:243], v[56:59], a[132:135], v[240:243]\n"
"s_add_u32 s84, s96, 4096\n"
"buffer_load_dwordx4 v10, s[16:19], s84 offen offset:2048 lds\n"
"v_mfma_f32_16x16x32_bf16 v[244:247], v[56:59], a[136:139], v[244:247]\n"
"v_mul_f32 v84, v84, v116\n"
"v_mul_f32 v85, v85, v117\n"
"v_mfma_f32_16x16x32_bf16 v[248:251], v[56:59], a[140:143], v[248:251]\n"
"s_waitcnt lgkmcnt(4)\n"
"v_mul_f32 v86, s80, v86\n"
"v_mul_f32 v87, s80, v87\n"
"v_mfma_f32_16x16x32_bf16 v[188:191], v[44:47], a[144:147], v[188:191]\n"
"s_add_u32 m0, s53, 159744\n"
"ds_read_b64_tr_b16 v[56:57], v6 offset:18432\n"
"ds_read_b64_tr_b16 v[58:59], v6 offset:18688\n"
"v_mfma_f32_16x16x32_bf16 v[192:195], v[44:47], a[148:151], v[192:195]\n"
"s_add_u32 s69, s69, 294912\n"
"buffer_load_dwordx4 v9, s[20:23], s69 offen offset:0 lds\n"
"v_mfma_f32_16x16x32_bf16 v[196:199], v[44:47], a[152:155], v[196:199]\n"
"v_exp_f32 v86, v86\n"
"v_mfma_f32_16x16x32_bf16 v[200:203], v[44:47], a[156:159], v[200:203]\n"
"s_waitcnt lgkmcnt(4)\n"
"v_exp_f32 v87, v87\n"
"v_mfma_f32_16x16x32_bf16 v[220:223], v[48:51], a[144:147], v[220:223]\n"
"s_add_u32 m0, s53, 69568\n"
"ds_read_b64_tr_b16 v[44:45], v3 offset:20480\n"
"ds_read_b64_tr_b16 v[46:47], v3 offset:20736\n"
"v_mfma_f32_16x16x32_bf16 v[224:227], v[48:51], a[148:151], v[224:227]\n"
"buffer_load_dwordx4 v9, s[20:23], s69 offen offset:64 lds\n"
"v_mfma_f32_16x16x32_bf16 v[228:231], v[48:51], a[152:155], v[228:231]\n"
"v_add_f32 v86, 1.0, v86\n"
"v_add_f32 v87, 1.0, v87\n"
"v_mfma_f32_16x16x32_bf16 v[232:235], v[48:51], a[156:159], v[232:235]\n"
"s_waitcnt lgkmcnt(4)\n"
"v_rcp_f32 v86, v86\n"
"v_mfma_f32_16x16x32_bf16 v[204:207], v[52:55], a[144:147], v[204:207]\n"
"s_add_u32 m0, s91, 21248\n"
"ds_read_b64_tr_b16 v[48:49], v3 offset:22528\n"
"ds_read_b64_tr_b16 v[50:51], v3 offset:22784\n"
"v_mfma_f32_16x16x32_bf16 v[208:211], v[52:55], a[148:151], v[208:211]\n"
"s_add_u32 s84, s96, 4096\n"
"buffer_load_dwordx4 v10, s[16:19], s84 offen offset:3072 lds\n"
"v_mfma_f32_16x16x32_bf16 v[212:215], v[52:55], a[152:155], v[212:215]\n"
"v_rcp_f32 v87, v87\n"
"v_mfma_f32_16x16x32_bf16 v[216:219], v[52:55], a[156:159], v[216:219]\n"
"s_waitcnt lgkmcnt(4)\n"
"v_mul_f32 v86, v86, v118\n"
"v_mul_f32 v87, v87, v119\n"
"v_mfma_f32_16x16x32_bf16 v[236:239], v[56:59], a[144:147], v[236:239]\n"
"ds_read_b64_tr_b16 v[52:53], v6 offset:20480\n"
"ds_read_b64_tr_b16 v[54:55], v6 offset:20736\n"
"v_mfma_f32_16x16x32_bf16 v[240:243], v[56:59], a[148:151], v[240:243]\n"
"v_cvt_pk_bf16_f32 v100, v68, v69\n"
"v_cvt_pk_bf16_f32 v101, v70, v71\n"
"v_mfma_f32_16x16x32_bf16 v[244:247], v[56:59], a[152:155], v[244:247]\n"
"v_cvt_pk_bf16_f32 v102, v84, v85\n"
"v_cvt_pk_bf16_f32 v103, v86, v87\n"
"v_mfma_f32_16x16x32_bf16 v[248:251], v[56:59], a[156:159], v[248:251]\n"
"s_waitcnt lgkmcnt(4)\n"
"v_mul_f32 v72, s80, v72\n"
"v_mul_f32 v73, s80, v73\n"
"v_mfma_f32_16x16x32_bf16 v[188:191], v[44:47], a[160:163], v[188:191]\n"
"ds_read_b64_tr_b16 v[56:57], v6 offset:22528\n"
"ds_read_b64_tr_b16 v[58:59], v6 offset:22784\n"
"v_mfma_f32_16x16x32_bf16 v[192:195], v[44:47], a[164:167], v[192:195]\n"
"v_exp_f32 v72, v72\n"
"v_mfma_f32_16x16x32_bf16 v[196:199], v[44:47], a[168:171], v[196:199]\n"
"v_exp_f32 v73, v73\n"
"v_mfma_f32_16x16x32_bf16 v[200:203], v[44:47], a[172:175], v[200:203]\n"
"s_waitcnt vmcnt(5) lgkmcnt(4)\n"
"s_barrier\n"
"v_add_f32 v72, 1.0, v72\n"
"v_mfma_f32_16x16x32_bf16 v[220:223], v[48:51], a[160:163], v[220:223]\n"
"s_add_u32 m0, s53, 139264\n"
"ds_read_b64_tr_b16 v[44:45], v3 offset:24576\n"
"ds_read_b64_tr_b16 v[46:47], v3 offset:24832\n"
"v_mfma_f32_16x16x32_bf16 v[224:227], v[48:51], a[164:167], v[224:227]\n"
"s_add_u32 s69, s69, 294912\n"
"buffer_load_dwordx4 v9, s[20:23], s69 offen offset:0 lds\n"
"v_mfma_f32_16x16x32_bf16 v[228:231], v[48:51], a[168:171], v[228:231]\n"
"v_rcp_f32 v72, v72\n"
"v_mfma_f32_16x16x32_bf16 v[232:235], v[48:51], a[172:175], v[232:235]\n"
"s_waitcnt lgkmcnt(4)\n"
"v_add_f32 v73, 1.0, v73\n"
"v_mul_f32 v72, v72, v104\n"
"v_mfma_f32_16x16x32_bf16 v[204:207], v[52:55], a[160:163], v[204:207]\n"
"s_add_u32 m0, s53, 73664\n"
"ds_read_b64_tr_b16 v[48:49], v3 offset:26624\n"
"ds_read_b64_tr_b16 v[50:51], v3 offset:26880\n"
"v_mfma_f32_16x16x32_bf16 v[208:211], v[52:55], a[164:167], v[208:211]\n"
"buffer_load_dwordx4 v9, s[20:23], s69 offen offset:64 lds\n"
"v_mfma_f32_16x16x32_bf16 v[212:215], v[52:55], a[168:171], v[212:215]\n"
"v_rcp_f32 v73, v73\n"
"v_mfma_f32_16x16x32_bf16 v[216:219], v[52:55], a[172:175], v[216:219]\n"
"s_waitcnt lgkmcnt(4)\n"
"v_mul_f32 v73, v73, v105\n"
"v_mul_f32 v74, s80, v74\n"
"v_mfma_f32_16x16x32_bf16 v[236:239], v[56:59], a[160:163], v[236:239]\n"
"s_add_u32 m0, s91, 42464\n"
"ds_read_b64_tr_b16 v[52:53], v6 offset:24576\n"
"ds_read_b64_tr_b16 v[54:55], v6 offset:24832\n"
"v_mfma_f32_16x16x32_bf16 v[240:243], v[56:59], a[164:167], v[240:243]\n"
"s_add_u32 s84, s97, 0\n"
"buffer_load_dwordx4 v10, s[16:19], s84 offen offset:0 lds\n"
"v_mfma_f32_16x16x32_bf16 v[244:247], v[56:59], a[168:171], v[244:247]\n"
"v_exp_f32 v74, v74\n"
"v_mfma_f32_16x16x32_bf16 v[248:251], v[56:59], a[172:175], v[248:251]\n"
"s_waitcnt lgkmcnt(4)\n"
"v_mul_f32 v75, s80, v75\n"
"v_add_f32 v74, 1.0, v74\n"
"v_mfma_f32_16x16x32_bf16 v[188:191], v[44:47], a[176:179], v[188:191]\n"
"s_add_u32 m0, s53, 147456\n"
"ds_read_b64_tr_b16 v[56:57], v6 offset:26624\n"
"ds_read_b64_tr_b16 v[58:59], v6 offset:26880\n"
"v_mfma_f32_16x16x32_bf16 v[192:195], v[44:47], a[180:183], v[192:195]\n"
"s_add_u32 s69, s69, 294912\n"
"buffer_load_dwordx4 v9, s[20:23], s69 offen offset:0 lds\n"
"v_mfma_f32_16x16x32_bf16 v[196:199], v[44:47], a[184:187], v[196:199]\n"
"v_exp_f32 v75, v75\n"
"v_mfma_f32_16x16x32_bf16 v[200:203], v[44:47], a[188:191], v[200:203]\n"
"s_waitcnt lgkmcnt(4)\n"
"v_rcp_f32 v74, v74\n"
"v_mfma_f32_16x16x32_bf16 v[220:223], v[48:51], a[176:179], v[220:223]\n"
"s_add_u32 m0, s53, 77760\n"
"ds_read_b64_tr_b16 v[44:45], v3 offset:12288\n"
"ds_read_b64_tr_b16 v[46:47], v3 offset:12544\n"
"v_mfma_f32_16x16x32_bf16 v[224:227], v[48:51], a[180:183], v[224:227]\n"
"buffer_load_dwordx4 v9, s[20:23], s69 offen offset:64 lds\n"
"v_mfma_f32_16x16x32_bf16 v[228:231], v[48:51], a[184:187], v[228:231]\n"
"v_add_f32 v75, 1.0, v75\n"
"v_mul_f32 v74, v74, v106\n"
"v_mfma_f32_16x16x32_bf16 v[232:235], v[48:51], a[188:191], v[232:235]\n"
"s_waitcnt lgkmcnt(4)\n"
"v_rcp_f32 v75, v75\n"
"v_mfma_f32_16x16x32_bf16 v[204:207], v[52:55], a[176:179], v[204:207]\n"
"s_add_u32 m0, s91, 42464\n"
"ds_read_b64_tr_b16 v[48:49], v3 offset:14336\n"
"ds_read_b64_tr_b16 v[50:51], v3 offset:14592\n"
"v_mfma_f32_16x16x32_bf16 v[208:211], v[52:55], a[180:183], v[208:211]\n"
"s_add_u32 s84, s97, 0\n"
"buffer_load_dwordx4 v10, s[16:19], s84 offen offset:1024 lds\n"
"v_mfma_f32_16x16x32_bf16 v[212:215], v[52:55], a[184:187], v[212:215]\n"
"v_mul_f32 v75, v75, v107\n"
"v_mul_f32 v88, s80, v88\n"
"v_mfma_f32_16x16x32_bf16 v[216:219], v[52:55], a[188:191], v[216:219]\n"
"s_waitcnt lgkmcnt(4)\n"
"v_exp_f32 v88, v88\n"
"v_mfma_f32_16x16x32_bf16 v[236:239], v[56:59], a[176:179], v[236:239]\n"
"ds_read_b64_tr_b16 v[52:53], v6 offset:12288\n"
"ds_read_b64_tr_b16 v[54:55], v6 offset:12544\n"
"v_mfma_f32_16x16x32_bf16 v[240:243], v[56:59], a[180:183], v[240:243]\n"
"v_mul_f32 v89, s80, v89\n"
"v_add_f32 v88, 1.0, v88\n"
"v_mfma_f32_16x16x32_bf16 v[244:247], v[56:59], a[184:187], v[244:247]\n"
"v_exp_f32 v89, v89\n"
"v_mfma_f32_16x16x32_bf16 v[248:251], v[56:59], a[188:191], v[248:251]\n"
"s_waitcnt lgkmcnt(4)\n"
"v_rcp_f32 v88, v88\n"
"v_mfma_f32_16x16x32_bf16 v[188:191], v[44:47], a[192:195], v[188:191]\n"
"ds_read_b64_tr_b16 v[56:57], v6 offset:14336\n"
"ds_read_b64_tr_b16 v[58:59], v6 offset:14592\n"
"v_mfma_f32_16x16x32_bf16 v[192:195], v[44:47], a[196:199], v[192:195]\n"
"v_add_f32 v89, 1.0, v89\n"
"v_mul_f32 v88, v88, v120\n"
"v_mfma_f32_16x16x32_bf16 v[196:199], v[44:47], a[200:203], v[196:199]\n"
"s_andn2_b32 s61, s33, 31\n"
"v_rcp_f32 v89, v89\n"
"v_mfma_f32_16x16x32_bf16 v[200:203], v[44:47], a[204:207], v[200:203]\n"
"s_waitcnt vmcnt(5) lgkmcnt(4)\n"
"s_barrier\n"
"v_mul_f32 v89, v89, v121\n"
"v_mfma_f32_16x16x32_bf16 v[220:223], v[48:51], a[192:195], v[220:223]\n"
"s_add_u32 m0, s53, 151552\n"
"ds_read_b64_tr_b16 v[44:45], v3 offset:28672\n"
"ds_read_b64_tr_b16 v[46:47], v3 offset:28928\n"
"v_mfma_f32_16x16x32_bf16 v[224:227], v[48:51], a[196:199], v[224:227]\n"
"s_add_u32 s69, s69, 294912\n"
"buffer_load_dwordx4 v9, s[20:23], s69 offen offset:0 lds\n"
"v_mfma_f32_16x16x32_bf16 v[228:231], v[48:51], a[200:203], v[228:231]\n"
"s_cmp_eq_u32 s33, 0\ns_cselect_b32 s62, 0, 3\n"
"v_mul_f32 v90, s80, v90\n"
"v_mul_f32 v91, s80, v91\n"
"v_mfma_f32_16x16x32_bf16 v[232:235], v[48:51], a[204:207], v[232:235]\n"
"s_waitcnt lgkmcnt(4)\n"
"v_exp_f32 v90, v90\n"
"v_mfma_f32_16x16x32_bf16 v[204:207], v[52:55], a[192:195], v[204:207]\n"
"s_add_u32 m0, s53, 81856\n"
"ds_read_b64_tr_b16 v[48:49], v3 offset:30720\n"
"ds_read_b64_tr_b16 v[50:51], v3 offset:30976\n"
"v_mfma_f32_16x16x32_bf16 v[208:211], v[52:55], a[196:199], v[208:211]\n"
"buffer_load_dwordx4 v9, s[20:23], s69 offen offset:64 lds\n"
"v_mfma_f32_16x16x32_bf16 v[212:215], v[52:55], a[200:203], v[212:215]\n"
"v_exp_f32 v91, v91\n"
"v_mfma_f32_16x16x32_bf16 v[216:219], v[52:55], a[204:207], v[216:219]\n"
"s_waitcnt lgkmcnt(4)\n"
"v_add_f32 v90, 1.0, v90\n"
"v_add_f32 v91, 1.0, v91\n"
"v_mfma_f32_16x16x32_bf16 v[236:239], v[56:59], a[192:195], v[236:239]\n"
"s_add_u32 m0, s91, 42464\n"
"ds_read_b64_tr_b16 v[52:53], v6 offset:28672\n"
"ds_read_b64_tr_b16 v[54:55], v6 offset:28928\n"
"v_mfma_f32_16x16x32_bf16 v[240:243], v[56:59], a[196:199], v[240:243]\n"
"s_add_u32 s84, s97, 0\n"
"buffer_load_dwordx4 v10, s[16:19], s84 offen offset:2048 lds\n"
"v_mfma_f32_16x16x32_bf16 v[244:247], v[56:59], a[200:203], v[244:247]\n"
"v_rcp_f32 v90, v90\n"
"v_mfma_f32_16x16x32_bf16 v[248:251], v[56:59], a[204:207], v[248:251]\n"
"s_waitcnt lgkmcnt(4)\n"
"v_rcp_f32 v91, v91\n"
"v_mfma_f32_16x16x32_bf16 v[188:191], v[44:47], a[208:211], v[188:191]\n"
"s_add_u32 m0, s53, 155648\n"
"ds_read_b64_tr_b16 v[56:57], v6 offset:30720\n"
"ds_read_b64_tr_b16 v[58:59], v6 offset:30976\n"
"v_mfma_f32_16x16x32_bf16 v[192:195], v[44:47], a[212:215], v[192:195]\n"
"s_add_u32 s69, s69, 294912\n"
"buffer_load_dwordx4 v9, s[20:23], s69 offen offset:0 lds\n"
"v_mfma_f32_16x16x32_bf16 v[196:199], v[44:47], a[216:219], v[196:199]\n"
"v_mul_f32 v90, v90, v122\n"
"v_mul_f32 v91, v91, v123\n"
"v_mfma_f32_16x16x32_bf16 v[200:203], v[44:47], a[220:223], v[200:203]\n"
"s_waitcnt lgkmcnt(4)\n"
"v_cvt_pk_bf16_f32 v104, v72, v73\n"
"v_cvt_pk_bf16_f32 v105, v74, v75\n"
"v_mfma_f32_16x16x32_bf16 v[220:223], v[48:51], a[208:211], v[220:223]\n"
"s_add_u32 m0, s53, 85952\n"
"ds_read_b64_tr_b16 v[44:45], v3 offset:8192\n"
"ds_read_b64_tr_b16 v[46:47], v3 offset:8448\n"
"v_mfma_f32_16x16x32_bf16 v[224:227], v[48:51], a[212:215], v[224:227]\n"
"buffer_load_dwordx4 v9, s[20:23], s69 offen offset:64 lds\n"
"v_mfma_f32_16x16x32_bf16 v[228:231], v[48:51], a[216:219], v[228:231]\n"
"v_cvt_pk_bf16_f32 v106, v88, v89\n"
"v_cvt_pk_bf16_f32 v107, v90, v91\n"
"v_mfma_f32_16x16x32_bf16 v[232:235], v[48:51], a[220:223], v[232:235]\n"
"s_waitcnt lgkmcnt(4)\n"
"v_lshrrev_b32 v122, s62, v0\n"
"v_mov_b32 v252, s34\n"
"v_mfma_f32_16x16x32_bf16 v[204:207], v[52:55], a[208:211], v[204:207]\n"
"s_add_u32 m0, s91, 42464\n"
"ds_read_b64_tr_b16 v[48:49], v3 offset:10240\n"
"ds_read_b64_tr_b16 v[50:51], v3 offset:10496\n"
"v_mfma_f32_16x16x32_bf16 v[208:211], v[52:55], a[212:215], v[208:211]\n"
"s_add_u32 s84, s97, 0\n"
"buffer_load_dwordx4 v10, s[16:19], s84 offen offset:3072 lds\n"
"v_mfma_f32_16x16x32_bf16 v[212:215], v[52:55], a[216:219], v[212:215]\n"
"v_add_lshl_u32 v122, v122, s61, 10\n"
"v_mov_b32 v253, s35\n"
"v_mfma_f32_16x16x32_bf16 v[216:219], v[52:55], a[220:223], v[216:219]\n"
"s_waitcnt lgkmcnt(4)\n"
"buffer_load_dword v7, v7, s[36:39], 0 offen offset:0\n"
"v_mfma_f32_16x16x32_bf16 v[236:239], v[56:59], a[208:211], v[236:239]\n"
"ds_read_b64_tr_b16 v[52:53], v6 offset:8192\n"
"ds_read_b64_tr_b16 v[54:55], v6 offset:8448\n"
"v_mfma_f32_16x16x32_bf16 v[240:243], v[56:59], a[212:215], v[240:243]\n"
"v_lshrrev_b32 v254, 4, v0\n"
"v_mul_f32 v124, s80, v124\n"
"v_mfma_f32_16x16x32_bf16 v[244:247], v[56:59], a[216:219], v[244:247]\n"
"v_min_u32 v254, 1, v254\n"
"v_mul_f32 v125, s80, v125\n"
"v_mfma_f32_16x16x32_bf16 v[248:251], v[56:59], a[220:223], v[248:251]\n"
"s_waitcnt lgkmcnt(4)\n"
"v_lshlrev_b32 v254, 27, v254\n"
"v_mul_f32 v126, s80, v126\n"
"v_mfma_f32_16x16x32_bf16 v[188:191], v[44:47], a[224:227], v[188:191]\n"
"ds_read_b64_tr_b16 v[56:57], v6 offset:10240\n"
"ds_read_b64_tr_b16 v[58:59], v6 offset:10496\n"
"v_mfma_f32_16x16x32_bf16 v[192:195], v[44:47], a[228:231], v[192:195]\n"
"v_add_lshl_u32 v254, v254, v0, 3\n"
"v_mul_f32 v127, s80, v127\n"
"v_mfma_f32_16x16x32_bf16 v[196:199], v[44:47], a[232:235], v[196:199]\n"
"s_min_u32 s60, s52, 2\n"
"s_lshl_b32 s61, s33, 13\n"
"v_bfe_u32 v60, v0, 4, 2\n"
"v_and_b32 v61, 15, v0\n"
"v_mfma_f32_16x16x32_bf16 v[200:203], v[44:47], a[236:239], v[200:203]\n"
"s_waitcnt vmcnt(3) lgkmcnt(4)\n"
"s_barrier\n"
"s_lshl_b32 s62, s60, 12\n"
"s_mul_i32 s49, s60, 21216\n"
"v_mfma_f32_16x16x32_bf16 v[220:223], v[48:51], a[224:227], v[220:223]\n"
"s_add_u32 s61, s61, s62\n"
"ds_read_b64_tr_b16 v[44:45], v3 offset:16384\n"
"ds_read_b64_tr_b16 v[46:47], v3 offset:16640\n"
"v_add_u32 v62, v60, v61\n"
"v_mfma_f32_16x16x32_bf16 v[224:227], v[48:51], a[228:231], v[224:227]\n"
"s_lshr_b32 s62, s33, 2\n"
"v_subrev_u32 v62, 15, v62\n"
"v_mov_b32 v63, 1073741824\n"
"v_mul_u32_u24 v60, 4160, v60\n"
"v_mfma_f32_16x16x32_bf16 v[228:231], v[48:51], a[232:235], v[228:231]\n"
"s_add_u32 s62, s62, 512\n"
"v_cmp_gt_i32 s[50:51], 0, v62\n"
"v_lshl_add_u32 v60, v61, 1, v60\n"
"v_lshlrev_b32 v68, 4, v0\n"
"v_mfma_f32_16x16x32_bf16 v[232:235], v[48:51], a[236:239], v[232:235]\n"
"s_waitcnt lgkmcnt(4)\n"
"s_lshl_b32 s62, s62, 12\n"
"v_lshlrev_b32 v62, 1, v62\n"
"v_add_u32 v60, s49, v60\n"
"v_mfma_f32_16x16x32_bf16 v[204:207], v[52:55], a[224:227], v[204:207]\n"
"s_cmp_eq_u32 s60, 2\ns_cselect_b32 s48, s62, s61\n"
"ds_read_b64_tr_b16 v[48:49], v3 offset:18432\n"
"ds_read_b64_tr_b16 v[50:51], v3 offset:18688\n"
"v_cndmask_b32 v62, v62, v63, s[50:51]\n"
"v_mfma_f32_16x16x32_bf16 v[208:211], v[52:55], a[228:231], v[208:211]\n"
"s_movk_i32 s61, 125\n"
"buffer_load_ushort v63, v62, s[16:19], s48 offen offset:0\n"
"v_add_u32 v69, 63168, v68\n"
"v_mfma_f32_16x16x32_bf16 v[212:215], v[52:55], a[232:235], v[212:215]\n"
"s_movk_i32 s62, 155\n"
"s_movk_i32 s63, 185\n"
"v_add_u32 v70, 18720, v68\n"
"v_cmp_lt_u32 s[50:51], s61, v0\n"
"v_mfma_f32_16x16x32_bf16 v[216:219], v[52:55], a[236:239], v[216:219]\n"
"s_waitcnt lgkmcnt(4)\n"
"v_cndmask_b32 v69, v69, v70, s[50:51]\n"
"v_mov_b32 v64, 0\n"
"v_mov_b32 v65, 0\n"
"v_mfma_f32_16x16x32_bf16 v[236:239], v[56:59], a[224:227], v[236:239]\n"
"s_lshl_b32 s61, s33, 13\n"
"ds_read_b64_tr_b16 v[52:53], v6 offset:16384\n"
"ds_read_b64_tr_b16 v[54:55], v6 offset:16640\n"
"v_add_u32 v70, 39456, v68\n"
"v_mfma_f32_16x16x32_bf16 v[240:243], v[56:59], a[228:231], v[240:243]\n"
"s_add_u32 s61, s61, s53\n"
"v_cmp_lt_u32 s[50:51], s62, v0\n"
"v_mov_b32 v66, 0\n"
"v_mov_b32 v67, 0\n"
"v_mfma_f32_16x16x32_bf16 v[244:247], v[56:59], a[232:235], v[244:247]\n"
"s_add_u32 s46, s61, 31068160\n"
"v_cndmask_b32 v69, v69, v70, s[50:51]\n"
"v_mul_f32 v140, s80, v140\n"
"v_mfma_f32_16x16x32_bf16 v[248:251], v[56:59], a[236:239], v[248:251]\n"
"s_waitcnt lgkmcnt(4)\n"
"v_mov_b32 v70, 20736\n"
"v_cmp_lt_u32 s[50:51], s63, v0\n"
"s_lshr_b32 s61, s33, 2\n"
"v_mfma_f32_16x16x32_bf16 v[188:191], v[44:47], a[240:243], v[188:191]\n"
"s_lshl_b32 s61, s61, 12\n"
"ds_read_b64_tr_b16 v[56:57], v6 offset:18432\n"
"ds_read_b64_tr_b16 v[58:59], v6 offset:18688\n"
"v_cndmask_b32 v69, v69, v70, s[50:51]\n"
"v_mfma_f32_16x16x32_bf16 v[192:195], v[44:47], a[244:247], v[192:195]\n"
"s_add_u32 s61, s61, s53\n"
"buffer_load_dwordx2 v[120:121], v122, s[28:31], 0 offen offset:0 sc1\n"
"v_mfma_f32_16x16x32_bf16 v[196:199], v[44:47], a[248:251], v[196:199]\n"
"s_add_u32 s47, s61, 33165312\n"
"ds_write_b128 v69, v[64:67] offset:0\n"
"v_mfma_f32_16x16x32_bf16 v[200:203], v[44:47], a[252:255], v[200:203]\n"
"s_waitcnt lgkmcnt(5)\n"
"v_exp_f32 v124, v124\n"
"v_mfma_f32_16x16x32_bf16 v[220:223], v[48:51], a[240:243], v[220:223]\n"
"ds_read_b64_tr_b16 v[44:45], v3 offset:20480\n"
"ds_read_b64_tr_b16 v[46:47], v3 offset:20736\n"
"v_mfma_f32_16x16x32_bf16 v[224:227], v[48:51], a[244:247], v[224:227]\n"
"v_exp_f32 v125, v125\n"
"v_mfma_f32_16x16x32_bf16 v[228:231], v[48:51], a[248:251], v[228:231]\n"
"v_exp_f32 v126, v126\n"
"v_mfma_f32_16x16x32_bf16 v[232:235], v[48:51], a[252:255], v[232:235]\n"
"s_waitcnt lgkmcnt(5)\n"
"buffer_store_dwordx4 v[92:95], v10, s[28:31], s71 offen offset:0 sc1\n"
"v_mfma_f32_16x16x32_bf16 v[204:207], v[52:55], a[240:243], v[204:207]\n"
"ds_read_b64_tr_b16 v[48:49], v3 offset:22528\n"
"ds_read_b64_tr_b16 v[50:51], v3 offset:22784\n"
"v_mfma_f32_16x16x32_bf16 v[208:211], v[52:55], a[244:247], v[208:211]\n"
"v_exp_f32 v127, v127\n"
"v_mfma_f32_16x16x32_bf16 v[212:215], v[52:55], a[248:251], v[212:215]\n"
"v_exp_f32 v140, v140\n"
"v_mfma_f32_16x16x32_bf16 v[216:219], v[52:55], a[252:255], v[216:219]\n"
"s_waitcnt lgkmcnt(5)\n"
"v_add_f32 v124, 1.0, v124\n"
"v_add_f32 v125, 1.0, v125\n"
"v_mfma_f32_16x16x32_bf16 v[236:239], v[56:59], a[240:243], v[236:239]\n"
"ds_read_b64_tr_b16 v[52:53], v6 offset:20480\n"
"ds_read_b64_tr_b16 v[54:55], v6 offset:20736\n"
"v_mfma_f32_16x16x32_bf16 v[240:243], v[56:59], a[244:247], v[240:243]\n"
"buffer_store_dwordx4 v[96:99], v10, s[28:31], s71 offen offset:1024 sc1\n"
"v_mfma_f32_16x16x32_bf16 v[244:247], v[56:59], a[248:251], v[244:247]\n"
"v_rcp_f32 v124, v124\n"
"v_mfma_f32_16x16x32_bf16 v[248:251], v[56:59], a[252:255], v[248:251]\n"
"s_waitcnt lgkmcnt(4)\n"
"v_rcp_f32 v125, v125\n"
"v_mfma_f32_16x16x32_bf16 v[188:191], v[44:47], v[12:15], v[188:191]\n"
"ds_read_b64_tr_b16 v[56:57], v6 offset:22528\n"
"ds_read_b64_tr_b16 v[58:59], v6 offset:22784\n"
"v_mfma_f32_16x16x32_bf16 v[192:195], v[44:47], v[16:19], v[192:195]\n"
"v_mul_f32 v124, v124, v156\n"
"v_mul_f32 v125, v125, v157\n"
"v_mfma_f32_16x16x32_bf16 v[196:199], v[44:47], v[20:23], v[196:199]\n"
"v_add_f32 v126, 1.0, v126\n"
"v_add_f32 v127, 1.0, v127\n"
"v_mfma_f32_16x16x32_bf16 v[200:203], v[44:47], v[24:27], v[200:203]\n"
"s_waitcnt lgkmcnt(4)\n"
"buffer_store_dwordx4 v[100:103], v10, s[28:31], s72 offen offset:0 sc1\n"
"v_mfma_f32_16x16x32_bf16 v[220:223], v[48:51], v[12:15], v[220:223]\n"
"ds_read_b64_tr_b16 v[44:45], v3 offset:24576\n"
"ds_read_b64_tr_b16 v[46:47], v3 offset:24832\n"
"v_mfma_f32_16x16x32_bf16 v[224:227], v[48:51], v[16:19], v[224:227]\n"
"v_rcp_f32 v126, v126\n"
"v_mfma_f32_16x16x32_bf16 v[228:231], v[48:51], v[20:23], v[228:231]\n"
"v_rcp_f32 v127, v127\n"
"v_mfma_f32_16x16x32_bf16 v[232:235], v[48:51], v[24:27], v[232:235]\n"
"s_waitcnt lgkmcnt(4)\n"
"v_mul_f32 v126, v126, v158\n"
"v_mul_f32 v127, v127, v159\n"
"v_mfma_f32_16x16x32_bf16 v[204:207], v[52:55], v[12:15], v[204:207]\n"
"ds_read_b64_tr_b16 v[48:49], v3 offset:26624\n"
"ds_read_b64_tr_b16 v[50:51], v3 offset:26880\n"
"v_mfma_f32_16x16x32_bf16 v[208:211], v[52:55], v[16:19], v[208:211]\n"
"buffer_store_dwordx4 v[104:107], v10, s[28:31], s72 offen offset:1024 sc1\n"
"v_mfma_f32_16x16x32_bf16 v[212:215], v[52:55], v[20:23], v[212:215]\n"
"v_mul_f32 v141, s80, v141\n"
"v_add_f32 v140, 1.0, v140\n"
"v_mfma_f32_16x16x32_bf16 v[216:219], v[52:55], v[24:27], v[216:219]\n"
"s_waitcnt lgkmcnt(4)\n"
"v_exp_f32 v141, v141\n"
"v_mfma_f32_16x16x32_bf16 v[236:239], v[56:59], v[12:15], v[236:239]\n"
"ds_read_b64_tr_b16 v[52:53], v6 offset:24576\n"
"ds_read_b64_tr_b16 v[54:55], v6 offset:24832\n"
"v_mfma_f32_16x16x32_bf16 v[240:243], v[56:59], v[16:19], v[240:243]\n"
"v_rcp_f32 v140, v140\n"
"v_mfma_f32_16x16x32_bf16 v[244:247], v[56:59], v[20:23], v[244:247]\n"
"v_add_f32 v141, 1.0, v141\n"
"v_mul_f32 v140, v140, v172\n"
"v_mfma_f32_16x16x32_bf16 v[248:251], v[56:59], v[24:27], v[248:251]\n"
"s_waitcnt lgkmcnt(4)\n"
"s_waitcnt vmcnt(4)\n"
"v_cmp_ne_u64 vcc, s[34:35], v[120:121]\n"
"s_nop 3\n"
"s_cbranch_vccz pF1_ok_gated_mix_fused\n"
"s_mov_b32 s83, 0\n"
"pF1_sp_gated_mix_fused:\n"
"buffer_load_dwordx2 v[120:121], v122, s[28:31], 0 offen offset:0 sc1\n"
"s_waitcnt vmcnt(0)\n"
"s_add_u32 s83, s83, 1\n"
"s_cmp_gt_u32 s83, 1048576\n"
"s_cbranch_scc1 pF1_ok_gated_mix_fused\n"
"v_cmp_ne_u64 vcc, s[34:35], v[120:121]\n"
"s_nop 3\n"
"s_cbranch_vccnz pF1_sp_gated_mix_fused\n"
"pF1_ok_gated_mix_fused:\n"
"v_mfma_f32_16x16x32_bf16 v[188:191], v[44:47], v[28:31], v[188:191]\n"
"ds_read_b64_tr_b16 v[56:57], v6 offset:26624\n"
"ds_read_b64_tr_b16 v[58:59], v6 offset:26880\n"
"v_mfma_f32_16x16x32_bf16 v[192:195], v[44:47], v[32:35], v[192:195]\n"
"v_rcp_f32 v141, v141\n"
"v_mfma_f32_16x16x32_bf16 v[196:199], v[44:47], v[36:39], v[196:199]\n"
"ds_write_b16 v60, v63 offset:0\n"
"v_mfma_f32_16x16x32_bf16 v[200:203], v[44:47], v[40:43], v[200:203]\n"
"s_waitcnt lgkmcnt(5)\n"
"s_barrier\n"
"v_mul_f32 v141, v141, v173\n"
"v_mfma_f32_16x16x32_bf16 v[220:223], v[48:51], v[28:31], v[220:223]\n"
"ds_read_b64_tr_b16 v[44:45], v3 offset:4096\n"
"ds_read_b64_tr_b16 v[46:47], v3 offset:4352\n"
"v_mfma_f32_16x16x32_bf16 v[224:227], v[48:51], v[32:35], v[224:227]\n"
"v_mul_f32 v142, s80, v142\n"
"v_mul_f32 v143, s80, v143\n"
"v_mfma_f32_16x16x32_bf16 v[228:231], v[48:51], v[36:39], v[228:231]\n"
"v_exp_f32 v142, v142\n"
"v_mfma_f32_16x16x32_bf16 v[232:235], v[48:51], v[40:43], v[232:235]\n"
"s_waitcnt lgkmcnt(5)\n"
"v_exp_f32 v143, v143\n"
"v_mfma_f32_16x16x32_bf16 v[204:207], v[52:55], v[28:31], v[204:207]\n"
"ds_read_b64_tr_b16 v[48:49], v3 offset:6144\n"
"ds_read_b64_tr_b16 v[50:51], v3 offset:6400\n"
"v_mfma_f32_16x16x32_bf16 v[208:211], v[52:55], v[32:35], v[208:211]\n"
"v_add_f32 v142, 1.0, v142\n"
"v_add_f32 v143, 1.0, v143\n"
"v_mfma_f32_16x16x32_bf16 v[212:215], v[52:55], v[36:39], v[212:215]\n"
"v_rcp_f32 v142, v142\n"
"v_mfma_f32_16x16x32_bf16 v[216:219], v[52:55], v[40:43], v[216:219]\n"
"s_waitcnt lgkmcnt(5)\n"
"v_rcp_f32 v143, v143\n"
"v_mfma_f32_16x16x32_bf16 v[236:239], v[56:59], v[28:31], v[236:239]\n"
"ds_read_b64_tr_b16 v[52:53], v6 offset:4096\n"
"ds_read_b64_tr_b16 v[54:55], v6 offset:4352\n"
"v_mfma_f32_16x16x32_bf16 v[240:243], v[56:59], v[32:35], v[240:243]\n"
"v_mul_f32 v142, v142, v174\n"
"v_mul_f32 v143, v143, v175\n"
"v_mfma_f32_16x16x32_bf16 v[244:247], v[56:59], v[36:39], v[244:247]\n"
"v_cvt_pk_bf16_f32 v156, v124, v125\n"
"v_cvt_pk_bf16_f32 v157, v126, v127\n"
"v_mfma_f32_16x16x32_bf16 v[248:251], v[56:59], v[40:43], v[248:251]\n"
"s_waitcnt lgkmcnt(4)\n"
"v_cvt_pk_bf16_f32 v158, v140, v141\n"
"v_cvt_pk_bf16_f32 v159, v142, v143\n"
"v_mfma_f32_16x16x32_bf16 v[60:63], v[44:47], a[0:3], 0\n"
"s_add_u32 m0, s53, 16640\n"
"ds_read_b64_tr_b16 v[56:57], v6 offset:6144\n"
"ds_read_b64_tr_b16 v[58:59], v6 offset:6400\n"
"v_mfma_f32_16x16x32_bf16 v[64:67], v[44:47], a[4:7], 0\n"
"s_add_u32 s84, s46, 0\n"
"buffer_load_dwordx4 v10, s[28:31], s84 offen offset:0 sc1 lds\n"
"v_mfma_f32_16x16x32_bf16 v[68:71], v[44:47], a[8:11], 0\n"
"buffer_store_dwordx4 v[156:159], v10, s[28:31], s73 offen offset:0 sc1\n"
"v_mfma_f32_16x16x32_bf16 v[72:75], v[44:47], a[12:15], 0\n"
"s_waitcnt lgkmcnt(4)\n"
"v_mul_f32 v128, s80, v128\n"
"v_mul_f32 v129, s80, v129\n"
"v_mfma_f32_16x16x32_bf16 v[92:95], v[48:51], a[0:3], 0\n"
"s_add_u32 m0, s53, 37856\n"
"ds_read_b64_tr_b16 v[44:45], v2 offset:24576\n"
"ds_read_b64_tr_b16 v[46:47], v2 offset:24832\n"
"v_mfma_f32_16x16x32_bf16 v[96:99], v[48:51], a[4:7], 0\n"
"s_add_u32 s84, s46, 4096\n"
"buffer_load_dwordx4 v10, s[28:31], s84 offen offset:0 sc1 lds\n"
"v_mfma_f32_16x16x32_bf16 v[100:103], v[48:51], a[8:11], 0\n"
"v_exp_f32 v128, v128\n"
"v_mfma_f32_16x16x32_bf16 v[104:107], v[48:51], a[12:15], 0\n"
"s_waitcnt lgkmcnt(4)\n"
"v_exp_f32 v129, v129\n"
"v_mfma_f32_16x16x32_bf16 v[76:79], v[52:55], a[0:3], 0\n"
"s_add_u32 m0, s53, 59072\n"
"ds_read_b64_tr_b16 v[48:49], v2 offset:26624\n"
"ds_read_b64_tr_b16 v[50:51], v2 offset:26880\n"
"v_mfma_f32_16x16x32_bf16 v[80:83], v[52:55], a[4:7], 0\n"
"s_add_u32 s84, s47, 0\n"
"buffer_load_dwordx4 v10, s[28:31], s84 offen offset:0 sc1 lds\n"
"v_mfma_f32_16x16x32_bf16 v[84:87], v[52:55], a[8:11], 0\n"
"v_add_f32 v128, 1.0, v128\n"
"v_add_f32 v129, 1.0, v129\n"
"v_mfma_f32_16x16x32_bf16 v[88:91], v[52:55], a[12:15], 0\n"
"s_waitcnt lgkmcnt(4)\n"
"v_rcp_f32 v128, v128\n"
"v_mfma_f32_16x16x32_bf16 v[108:111], v[56:59], a[0:3], 0\n"
"ds_read_b64_tr_b16 v[52:53], v5 offset:24576\n"
"ds_read_b64_tr_b16 v[54:55], v5 offset:24832\n"
"v_mfma_f32_16x16x32_bf16 v[112:115], v[56:59], a[4:7], 0\n"
"v_rcp_f32 v129, v129\n"
"v_mfma_f32_16x16x32_bf16 v[116:119], v[56:59], a[8:11], 0\n"
"v_mul_f32 v128, v128, v160\n"
"v_mul_f32 v129, v129, v161\n"
"v_mfma_f32_16x16x32_bf16 v[120:123], v[56:59], a[12:15], 0\n"
"s_waitcnt lgkmcnt(4)\n"
"v_mul_f32 v130, s80, v130\n"
"v_mul_f32 v131, s80, v131\n"
"v_mfma_f32_16x16x32_bf16 v[60:63], v[44:47], a[16:19], v[60:63]\n"
"ds_read_b64_tr_b16 v[56:57], v5 offset:26624\n"
"ds_read_b64_tr_b16 v[58:59], v5 offset:26880\n"
"v_mfma_f32_16x16x32_bf16 v[64:67], v[44:47], a[20:23], v[64:67]\n"
"v_exp_f32 v130, v130\n"
"v_mfma_f32_16x16x32_bf16 v[68:71], v[44:47], a[24:27], v[68:71]\n"
"s_add_u32 m0, s53, 143360\n"
"v_exp_f32 v131, v131\n"
"v_mfma_f32_16x16x32_bf16 v[72:75], v[44:47], a[28:31], v[72:75]\n"
"s_waitcnt lgkmcnt(4)\n"
"buffer_load_dwordx4 v9, s[20:23], s70 offen offset:0 lds\n"
"v_mfma_f32_16x16x32_bf16 v[92:95], v[48:51], a[16:19], v[92:95]\n"
"ds_read_b64_tr_b16 v[44:45], v3 offset:0\n"
"ds_read_b64_tr_b16 v[46:47], v3 offset:256\n"
"v_mfma_f32_16x16x32_bf16 v[96:99], v[48:51], a[20:23], v[96:99]\n"
"buffer_load_dword v7, v7, s[36:39], 0 offen offset:0\n"
"v_mfma_f32_16x16x32_bf16 v[100:103], v[48:51], a[24:27], v[100:103]\n"
"v_add_f32 v130, 1.0, v130\n"
"v_add_f32 v131, 1.0, v131\n"
"v_mfma_f32_16x16x32_bf16 v[104:107], v[48:51], a[28:31], v[104:107]\n"
"s_waitcnt lgkmcnt(4)\n"
"v_rcp_f32 v130, v130\n"
"v_mfma_f32_16x16x32_bf16 v[76:79], v[52:55], a[16:19], v[76:79]\n"
"ds_read_b64_tr_b16 v[48:49], v3 offset:2048\n"
"ds_read_b64_tr_b16 v[50:51], v3 offset:2304\n"
"v_mfma_f32_16x16x32_bf16 v[80:83], v[52:55], a[20:23], v[80:83]\n"
"v_rcp_f32 v131, v131\n"
"v_mfma_f32_16x16x32_bf16 v[84:87], v[52:55], a[24:27], v[84:87]\n"
"v_mul_f32 v130, v130, v162\n"
"v_mul_f32 v131, v131, v163\n"
"v_mfma_f32_16x16x32_bf16 v[88:91], v[52:55], a[28:31], v[88:91]\n"
"s_waitcnt lgkmcnt(4)\n"
"v_mul_f32 v144, s80, v144\n"
"v_mul_f32 v145, s80, v145\n"
"v_mfma_f32_16x16x32_bf16 v[108:111], v[56:59], a[16:19], v[108:111]\n"
"ds_read_b64_tr_b16 v[52:53], v6 offset:0\n"
"ds_read_b64_tr_b16 v[54:55], v6 offset:256\n"
"v_mfma_f32_16x16x32_bf16 v[112:115], v[56:59], a[20:23], v[112:115]\n"
"v_exp_f32 v144, v144\n"
"v_mfma_f32_16x16x32_bf16 v[116:119], v[56:59], a[24:27], v[116:119]\n"
"v_exp_f32 v145, v145\n"
"v_mfma_f32_16x16x32_bf16 v[120:123], v[56:59], a[28:31], v[120:123]\n"
"s_waitcnt lgkmcnt(4)\n"
"v_add_f32 v144, 1.0, v144\n"
"v_add_f32 v145, 1.0, v145\n"
"v_mfma_f32_16x16x32_bf16 v[60:63], v[44:47], a[32:35], v[60:63]\n"
"ds_read_b64_tr_b16 v[56:57], v6 offset:2048\n"
"ds_read_b64_tr_b16 v[58:59], v6 offset:2304\n"
"v_mfma_f32_16x16x32_bf16 v[64:67], v[44:47], a[36:39], v[64:67]\n"
"v_rcp_f32 v144, v144\n"
"v_mfma_f32_16x16x32_bf16 v[68:71], v[44:47], a[40:43], v[68:71]\n"
"v_rcp_f32 v145, v145\n"
"v_mfma_f32_16x16x32_bf16 v[72:75], v[44:47], a[44:47], v[72:75]\n"
"s_waitcnt lgkmcnt(4)\n"
"v_mul_f32 v144, v144, v176\n"
"v_mul_f32 v145, v145, v177\n"
"v_mfma_f32_16x16x32_bf16 v[92:95], v[48:51], a[32:35], v[92:95]\n"
"ds_read_b64_tr_b16 v[44:45], v2 offset:49152\n"
"ds_read_b64_tr_b16 v[46:47], v2 offset:49408\n"
"v_mfma_f32_16x16x32_bf16 v[96:99], v[48:51], a[36:39], v[96:99]\n"
"v_mul_f32 v146, s80, v146\n"
"v_mul_f32 v147, s80, v147\n"
"v_mfma_f32_16x16x32_bf16 v[100:103], v[48:51], a[40:43], v[100:103]\n"
"v_exp_f32 v146, v146\n"
"v_mfma_f32_16x16x32_bf16 v[104:107], v[48:51], a[44:47], v[104:107]\n"
"s_waitcnt lgkmcnt(4)\n"
"v_exp_f32 v147, v147\n"
"v_mfma_f32_16x16x32_bf16 v[76:79], v[52:55], a[32:35], v[76:79]\n"
"ds_read_b64_tr_b16 v[48:49], v2 offset:51200\n"
"ds_read_b64_tr_b16 v[50:51], v2 offset:51456\n"
"v_mfma_f32_16x16x32_bf16 v[80:83], v[52:55], a[36:39], v[80:83]\n"
"v_add_f32 v146, 1.0, v146\n"
"v_add_f32 v147, 1.0, v147\n"
"v_mfma_f32_16x16x32_bf16 v[84:87], v[52:55], a[40:43], v[84:87]\n"
"v_rcp_f32 v146, v146\n"
"v_mfma_f32_16x16x32_bf16 v[88:91], v[52:55], a[44:47], v[88:91]\n"
"s_waitcnt lgkmcnt(4)\n"
"v_rcp_f32 v147, v147\n"
"v_mfma_f32_16x16x32_bf16 v[108:111], v[56:59], a[32:35], v[108:111]\n"
"ds_read_b64_tr_b16 v[52:53], v5 offset:49152\n"
"ds_read_b64_tr_b16 v[54:55], v5 offset:49408\n"
"v_mfma_f32_16x16x32_bf16 v[112:115], v[56:59], a[36:39], v[112:115]\n"
"v_mul_f32 v146, v146, v178\n"
"v_mul_f32 v147, v147, v179\n"
"v_mfma_f32_16x16x32_bf16 v[116:119], v[56:59], a[40:43], v[116:119]\n"
"v_cvt_pk_bf16_f32 v160, v128, v129\n"
"v_cvt_pk_bf16_f32 v161, v130, v131\n"
"v_mfma_f32_16x16x32_bf16 v[120:123], v[56:59], a[44:47], v[120:123]\n"
"s_waitcnt lgkmcnt(4)\n"
"v_cvt_pk_bf16_f32 v162, v144, v145\n"
"v_cvt_pk_bf16_f32 v163, v146, v147\n"
"v_mfma_f32_16x16x32_bf16 v[60:63], v[44:47], a[48:51], v[60:63]\n"
"ds_read_b64_tr_b16 v[56:57], v5 offset:51200\n"
"ds_read_b64_tr_b16 v[58:59], v5 offset:51456\n"
"v_mfma_f32_16x16x32_bf16 v[64:67], v[44:47], a[52:55], v[64:67]\n"
"buffer_store_dwordx4 v[160:163], v10, s[28:31], s73 offen offset:1024 sc1\n"
"v_mfma_f32_16x16x32_bf16 v[68:71], v[44:47], a[56:59], v[68:71]\n"
"v_mul_f32 v132, s80, v132\n"
"v_mul_f32 v133, s80, v133\n"
"v_mfma_f32_16x16x32_bf16 v[72:75], v[44:47], a[60:63], v[72:75]\n"
"s_waitcnt vmcnt(7) lgkmcnt(4)\n"
"s_barrier\n"
"v_mul_f32 v134, s80, v134\n"
"v_mfma_f32_16x16x32_bf16 v[92:95], v[48:51], a[48:51], v[92:95]\n"
"s_add_u32 m0, s53, 159744\n"
"ds_read_b64_tr_b16 v[44:45], v2 offset:45056\n"
"ds_read_b64_tr_b16 v[46:47], v2 offset:45312\n"
"v_mfma_f32_16x16x32_bf16 v[96:99], v[48:51], a[52:55], v[96:99]\n"
"s_add_u32 s70, s70, 294912\n"
"buffer_load_dwordx4 v9, s[20:23], s70 offen offset:0 lds\n"
"v_mfma_f32_16x16x32_bf16 v[100:103], v[48:51], a[56:59], v[100:103]\n"
"buffer_store_dwordx2 v[252:253], v254, s[28:31], s82 offen offset:128 sc1\n"
"v_mfma_f32_16x16x32_bf16 v[104:107], v[48:51], a[60:63], v[104:107]\n"
"s_waitcnt lgkmcnt(4)\n"
"v_exp_f32 v132, v132\n"
"v_mfma_f32_16x16x32_bf16 v[76:79], v[52:55], a[48:51], v[76:79]\n"
"ds_read_b64_tr_b16 v[48:49], v2 offset:47104\n"
"ds_read_b64_tr_b16 v[50:51], v2 offset:47360\n"
"v_mfma_f32_16x16x32_bf16 v[80:83], v[52:55], a[52:55], v[80:83]\n"
"v_exp_f32 v133, v133\n"
"v_mfma_f32_16x16x32_bf16 v[84:87], v[52:55], a[56:59], v[84:87]\n"
"v_exp_f32 v134, v134\n"
"v_mfma_f32_16x16x32_bf16 v[88:91], v[52:55], a[60:63], v[88:91]\n"
"s_waitcnt lgkmcnt(4)\n"
"v_add_f32 v132, 1.0, v132\n"
"v_add_f32 v133, 1.0, v133\n"
"v_mfma_f32_16x16x32_bf16 v[108:111], v[56:59], a[48:51], v[108:111]\n"
"ds_read_b64_tr_b16 v[52:53], v5 offset:45056\n"
"ds_read_b64_tr_b16 v[54:55], v5 offset:45312\n"
"v_mfma_f32_16x16x32_bf16 v[112:115], v[56:59], a[52:55], v[112:115]\n"
"v_rcp_f32 v132, v132\n"
"v_mfma_f32_16x16x32_bf16 v[116:119], v[56:59], a[56:59], v[116:119]\n"
"v_rcp_f32 v133, v133\n"
"v_mfma_f32_16x16x32_bf16 v[120:123], v[56:59], a[60:63], v[120:123]\n"
"s_waitcnt lgkmcnt(4)\n"
"v_mul_f32 v132, v132, v164\n"
"v_mul_f32 v133, v133, v165\n"
"v_mfma_f32_16x16x32_bf16 v[60:63], v[44:47], a[64:67], v[60:63]\n"
"s_add_u32 m0, s53, 139264\n"
"ds_read_b64_tr_b16 v[56:57], v5 offset:47104\n"
"ds_read_b64_tr_b16 v[58:59], v5 offset:47360\n"
"v_mfma_f32_16x16x32_bf16 v[64:67], v[44:47], a[68:71], v[64:67]\n"
"s_add_u32 s70, s70, 294912\n"
"buffer_load_dwordx4 v9, s[20:23], s70 offen offset:0 lds\n"
"v_mfma_f32_16x16x32_bf16 v[68:71], v[44:47], a[72:75], v[68:71]\n"
"v_mul_f32 v135, s80, v135\n"
"v_add_f32 v134, 1.0, v134\n"
"v_mfma_f32_16x16x32_bf16 v[72:75], v[44:47], a[76:79], v[72:75]\n"
"s_waitcnt lgkmcnt(4)\n"
"v_exp_f32 v135, v135\n"
"v_mfma_f32_16x16x32_bf16 v[92:95], v[48:51], a[64:67], v[92:95]\n"
"ds_read_b64_tr_b16 v[44:45], v2 offset:61440\n"
"ds_read_b64_tr_b16 v[46:47], v2 offset:61696\n"
"v_mfma_f32_16x16x32_bf16 v[96:99], v[48:51], a[68:71], v[96:99]\n"
"v_rcp_f32 v134, v134\n"
"v_mfma_f32_16x16x32_bf16 v[100:103], v[48:51], a[72:75], v[100:103]\n"
"v_add_f32 v135, 1.0, v135\n"
"v_mul_f32 v134, v134, v166\n"
"v_mfma_f32_16x16x32_bf16 v[104:107], v[48:51], a[76:79], v[104:107]\n"
"s_waitcnt lgkmcnt(4)\n"
"v_rcp_f32 v135, v135\n"
"v_mfma_f32_16x16x32_bf16 v[76:79], v[52:55], a[64:67], v[76:79]\n"
"ds_read_b64_tr_b16 v[48:49], v2 offset:63488\n"
"ds_read_b64_tr_b16 v[50:51], v2 offset:63744\n"
"v_mfma_f32_16x16x32_bf16 v[80:83], v[52:55], a[68:71], v[80:83]\n"
"s_add_u32 s70, s70, 294912\n"
"v_mul_f32 v135, v135, v167\n"
"v_mul_f32 v148, s80, v148\n"
"v_mfma_f32_16x16x32_bf16 v[84:87], v[52:55], a[72:75], v[84:87]\n"
"s_add_u32 m0, s53, 147456\n"
"v_exp_f32 v148, v148\n"
"v_mfma_f32_16x16x32_bf16 v[88:91], v[52:55], a[76:79], v[88:91]\n"
"s_waitcnt lgkmcnt(4)\n"
"buffer_load_dwordx4 v9, s[20:23], s70 offen offset:0 lds\n"
"v_mfma_f32_16x16x32_bf16 v[108:111], v[56:59], a[64:67], v[108:111]\n"
"ds_read_b64_tr_b16 v[52:53], v5 offset:61440\n"
"ds_read_b64_tr_b16 v[54:55], v5 offset:61696\n"
"v_mfma_f32_16x16x32_bf16 v[112:115], v[56:59], a[68:71], v[112:115]\n"
"v_mul_f32 v149, s80, v149\n"
"v_add_f32 v148, 1.0, v148\n"
"v_mfma_f32_16x16x32_bf16 v[116:119], v[56:59], a[72:75], v[116:119]\n"
"v_exp_f32 v149, v149\n"
"v_mfma_f32_16x16x32_bf16 v[120:123], v[56:59], a[76:79], v[120:123]\n"
"s_waitcnt lgkmcnt(4)\n"
"v_rcp_f32 v148, v148\n"
"v_mfma_f32_16x16x32_bf16 v[60:63], v[44:47], a[80:83], v[60:63]\n"
"ds_read_b64_tr_b16 v[56:57], v5 offset:63488\n"
"ds_read_b64_tr_b16 v[58:59], v5 offset:63744\n"
"v_mfma_f32_16x16x32_bf16 v[64:67], v[44:47], a[84:87], v[64:67]\n"
"v_add_f32 v149, 1.0, v149\n"
"v_mul_f32 v148, v148, v180\n"
"v_mfma_f32_16x16x32_bf16 v[68:71], v[44:47], a[88:91], v[68:71]\n"
"v_rcp_f32 v149, v149\n"
"v_mfma_f32_16x16x32_bf16 v[72:75], v[44:47], a[92:95], v[72:75]\n"
"s_waitcnt lgkmcnt(4)\n"
"v_mul_f32 v149, v149, v181\n"
"v_mul_f32 v150, s80, v150\n"
"v_mfma_f32_16x16x32_bf16 v[92:95], v[48:51], a[80:83], v[92:95]\n"
"s_add_u32 m0, s53, 151552\n"
"ds_read_b64_tr_b16 v[44:45], v2 offset:57344\n"
"ds_read_b64_tr_b16 v[46:47], v2 offset:57600\n"
"v_mfma_f32_16x16x32_bf16 v[96:99], v[48:51], a[84:87], v[96:99]\n"
"s_add_u32 s70, s70, 294912\n"
"buffer_load_dwordx4 v9, s[20:23], s70 offen offset:0 lds\n"
"v_mfma_f32_16x16x32_bf16 v[100:103], v[48:51], a[88:91], v[100:103]\n"
"v_exp_f32 v150, v150\n"
"v_mfma_f32_16x16x32_bf16 v[104:107], v[48:51], a[92:95], v[104:107]\n"
"s_waitcnt lgkmcnt(4)\n"
"v_mul_f32 v151, s80, v151\n"
"v_add_f32 v150, 1.0, v150\n"
"v_mfma_f32_16x16x32_bf16 v[76:79], v[52:55], a[80:83], v[76:79]\n"
"ds_read_b64_tr_b16 v[48:49], v2 offset:59392\n"
"ds_read_b64_tr_b16 v[50:51], v2 offset:59648\n"
"v_mfma_f32_16x16x32_bf16 v[80:83], v[52:55], a[84:87], v[80:83]\n"
"v_exp_f32 v151, v151\n"
"v_mfma_f32_16x16x32_bf16 v[84:87], v[52:55], a[88:91], v[84:87]\n"
"v_rcp_f32 v150, v150\n"
"v_mfma_f32_16x16x32_bf16 v[88:91], v[52:55], a[92:95], v[88:91]\n"
"s_waitcnt lgkmcnt(4)\n"
"v_add_f32 v151, 1.0, v151\n"
"v_mul_f32 v150, v150, v182\n"
"v_mfma_f32_16x16x32_bf16 v[108:111], v[56:59], a[80:83], v[108:111]\n"
"ds_read_b64_tr_b16 v[52:53], v5 offset:57344\n"
"ds_read_b64_tr_b16 v[54:55], v5 offset:57600\n"
"v_mfma_f32_16x16x32_bf16 v[112:115], v[56:59], a[84:87], v[112:115]\n"
"s_add_u32 s70, s70, 294912\n"
"v_rcp_f32 v151, v151\n"
"v_mfma_f32_16x16x32_bf16 v[116:119], v[56:59], a[88:91], v[116:119]\n"
"s_add_u32 m0, s53, 155648\n"
"v_mul_f32 v151, v151, v183\n"
"v_cvt_pk_bf16_f32 v164, v132, v133\n"
"v_mfma_f32_16x16x32_bf16 v[120:123], v[56:59], a[92:95], v[120:123]\n"
"s_waitcnt lgkmcnt(4)\n"
"buffer_load_dwordx4 v9, s[20:23], s70 offen offset:0 lds\n"
"v_mfma_f32_16x16x32_bf16 v[60:63], v[44:47], a[96:99], v[60:63]\n"
"ds_read_b64_tr_b16 v[56:57], v5 offset:59392\n"
"ds_read_b64_tr_b16 v[58:59], v5 offset:59648\n"
"v_mfma_f32_16x16x32_bf16 v[64:67], v[44:47], a[100:103], v[64:67]\n"
"v_cvt_pk_bf16_f32 v165, v134, v135\n"
"v_cvt_pk_bf16_f32 v166, v148, v149\n"
"v_mfma_f32_16x16x32_bf16 v[68:71], v[44:47], a[104:107], v[68:71]\n"
"v_cvt_pk_bf16_f32 v167, v150, v151\n"
"v_mul_f32 v136, s80, v136\n"
"v_mfma_f32_16x16x32_bf16 v[72:75], v[44:47], a[108:111], v[72:75]\n"
"s_waitcnt lgkmcnt(4)\n"
"s_barrier\n"
"v_mul_f32 v137, s80, v137\n"
"v_mfma_f32_16x16x32_bf16 v[92:95], v[48:51], a[96:99], v[92:95]\n"
"ds_read_b64_tr_b16 v[44:45], v2 offset:53248\n"
"ds_read_b64_tr_b16 v[46:47], v2 offset:53504\n"
"v_mfma_f32_16x16x32_bf16 v[96:99], v[48:51], a[100:103], v[96:99]\n"
"buffer_store_dwordx4 v[164:167], v10, s[28:31], s74 offen offset:0 sc1\n"
"v_mfma_f32_16x16x32_bf16 v[100:103], v[48:51], a[104:107], v[100:103]\n"
"v_exp_f32 v136, v136\n"
"v_mfma_f32_16x16x32_bf16 v[104:107], v[48:51], a[108:111], v[104:107]\n"
"s_waitcnt lgkmcnt(4)\n"
"v_exp_f32 v137, v137\n"
"v_mfma_f32_16x16x32_bf16 v[76:79], v[52:55], a[96:99], v[76:79]\n"
"s_add_u32 m0, s53, 90112\n"
"ds_read_b64_tr_b16 v[48:49], v2 offset:55296\n"
"ds_read_b64_tr_b16 v[50:51], v2 offset:55552\n"
"v_mfma_f32_16x16x32_bf16 v[80:83], v[52:55], a[100:103], v[80:83]\n"
"s_add_u32 s70, s70, s92\n"
"buffer_load_dwordx4 v9, s[20:23], s70 offen offset:0 lds\n"
"v_mfma_f32_16x16x32_bf16 v[84:87], v[52:55], a[104:107], v[84:87]\n"
"v_add_f32 v136, 1.0, v136\n"
"v_add_f32 v137, 1.0, v137\n"
"v_mfma_f32_16x16x32_bf16 v[88:91], v[52:55], a[108:111], v[88:91]\n"
"s_waitcnt lgkmcnt(4)\n"
"v_rcp_f32 v136, v136\n"
"v_mfma_f32_16x16x32_bf16 v[108:111], v[56:59], a[96:99], v[108:111]\n"
"ds_read_b64_tr_b16 v[52:53], v5 offset:53248\n"
"ds_read_b64_tr_b16 v[54:55], v5 offset:53504\n"
"v_mfma_f32_16x16x32_bf16 v[112:115], v[56:59], a[100:103], v[112:115]\n"
"v_rcp_f32 v137, v137\n"
"v_mfma_f32_16x16x32_bf16 v[116:119], v[56:59], a[104:107], v[116:119]\n"
"v_mul_f32 v136, v136, v168\n"
"v_mul_f32 v137, v137, v169\n"
"v_mfma_f32_16x16x32_bf16 v[120:123], v[56:59], a[108:111], v[120:123]\n"
"s_waitcnt lgkmcnt(4)\n"
"v_mul_f32 v138, s80, v138\n"
"v_mul_f32 v139, s80, v139\n"
"v_mfma_f32_16x16x32_bf16 v[60:63], v[44:47], a[112:115], v[60:63]\n"
"ds_read_b64_tr_b16 v[56:57], v5 offset:55296\n"
"ds_read_b64_tr_b16 v[58:59], v5 offset:55552\n"
"v_mfma_f32_16x16x32_bf16 v[64:67], v[44:47], a[116:119], v[64:67]\n"
"s_add_u32 s70, s70, 294912\n"
"v_exp_f32 v138, v138\n"
"v_mfma_f32_16x16x32_bf16 v[68:71], v[44:47], a[120:123], v[68:71]\n"
"s_add_u32 m0, s53, 131072\n"
"v_exp_f32 v139, v139\n"
"v_mfma_f32_16x16x32_bf16 v[72:75], v[44:47], a[124:127], v[72:75]\n"
"s_waitcnt lgkmcnt(4)\n"
"buffer_load_dwordx4 v9, s[20:23], s70 offen offset:0 lds\n"
"v_mfma_f32_16x16x32_bf16 v[92:95], v[48:51], a[112:115], v[92:95]\n"
"ds_read_b64_tr_b16 v[44:45], v2 offset:40960\n"
"ds_read_b64_tr_b16 v[46:47], v2 offset:41216\n"
"v_mfma_f32_16x16x32_bf16 v[96:99], v[48:51], a[116:119], v[96:99]\n"
"v_add_f32 v138, 1.0, v138\n"
"v_add_f32 v139, 1.0, v139\n"
"v_mfma_f32_16x16x32_bf16 v[100:103], v[48:51], a[120:123], v[100:103]\n"
"v_rcp_f32 v138, v138\n"
"v_mfma_f32_16x16x32_bf16 v[104:107], v[48:51], a[124:127], v[104:107]\n"
"s_waitcnt lgkmcnt(4)\n"
"v_rcp_f32 v139, v139\n"
"v_mfma_f32_16x16x32_bf16 v[76:79], v[52:55], a[112:115], v[76:79]\n"
"ds_read_b64_tr_b16 v[48:49], v2 offset:43008\n"
"ds_read_b64_tr_b16 v[50:51], v2 offset:43264\n"
"v_mfma_f32_16x16x32_bf16 v[80:83], v[52:55], a[116:119], v[80:83]\n"
"v_mul_f32 v138, v138, v170\n"
"v_mul_f32 v139, v139, v171\n"
"v_mfma_f32_16x16x32_bf16 v[84:87], v[52:55], a[120:123], v[84:87]\n"
"v_mul_f32 v152, s80, v152\n"
"v_mul_f32 v153, s80, v153\n"
"v_mfma_f32_16x16x32_bf16 v[88:91], v[52:55], a[124:127], v[88:91]\n"
"s_waitcnt lgkmcnt(4)\n"
"v_exp_f32 v152, v152\n"
"v_mfma_f32_16x16x32_bf16 v[108:111], v[56:59], a[112:115], v[108:111]\n"
"s_add_u32 m0, s53, 135168\n"
"ds_read_b64_tr_b16 v[52:53], v5 offset:40960\n"
"ds_read_b64_tr_b16 v[54:55], v5 offset:41216\n"
"v_mfma_f32_16x16x32_bf16 v[112:115], v[56:59], a[116:119], v[112:115]\n"
"s_add_u32 s70, s70, 294912\n"
"buffer_load_dwordx4 v9, s[20:23], s70 offen offset:0 lds\n"
"v_mfma_f32_16x16x32_bf16 v[116:119], v[56:59], a[120:123], v[116:119]\n"
"v_exp_f32 v153, v153\n"
"v_mfma_f32_16x16x32_bf16 v[120:123], v[56:59], a[124:127], v[120:123]\n"
"s_waitcnt lgkmcnt(4)\n"
"v_add_f32 v152, 1.0, v152\n"
"v_add_f32 v153, 1.0, v153\n"
"v_mfma_f32_16x16x32_bf16 v[60:63], v[44:47], a[128:131], v[60:63]\n"
"ds_read_b64_tr_b16 v[56:57], v5 offset:43008\n"
"ds_read_b64_tr_b16 v[58:59], v5 offset:43264\n"
"v_mfma_f32_16x16x32_bf16 v[64:67], v[44:47], a[132:135], v[64:67]\n"
"v_rcp_f32 v152, v152\n"
"v_mfma_f32_16x16x32_bf16 v[68:71], v[44:47], a[136:139], v[68:71]\n"
"v_rcp_f32 v153, v153\n"
"v_mfma_f32_16x16x32_bf16 v[72:75], v[44:47], a[140:143], v[72:75]\n"
"s_waitcnt lgkmcnt(4)\n"
"v_mul_f32 v152, v152, v184\n"
"v_mul_f32 v153, v153, v185\n"
"v_mfma_f32_16x16x32_bf16 v[92:95], v[48:51], a[128:131], v[92:95]\n"
"ds_read_b64_tr_b16 v[44:45], v2 offset:36864\n"
"ds_read_b64_tr_b16 v[46:47], v2 offset:37120\n"
"v_mfma_f32_16x16x32_bf16 v[96:99], v[48:51], a[132:135], v[96:99]\n"
"s_add_u32 s70, s70, 294912\n"
"v_mul_f32 v154, s80, v154\n"
"v_mul_f32 v155, s80, v155\n"
"v_mfma_f32_16x16x32_bf16 v[100:103], v[48:51], a[136:139], v[100:103]\n"
"s_add_u32 m0, s53, 110592\n"
"v_exp_f32 v154, v154\n"
"v_mfma_f32_16x16x32_bf16 v[104:107], v[48:51], a[140:143], v[104:107]\n"
"s_waitcnt lgkmcnt(4)\n"
"buffer_load_dwordx4 v9, s[20:23], s70 offen offset:0 lds\n"
"v_mfma_f32_16x16x32_bf16 v[76:79], v[52:55], a[128:131], v[76:79]\n"
"ds_read_b64_tr_b16 v[48:49], v2 offset:38912\n"
"ds_read_b64_tr_b16 v[50:51], v2 offset:39168\n"
"v_mfma_f32_16x16x32_bf16 v[80:83], v[52:55], a[132:135], v[80:83]\n"
"v_exp_f32 v155, v155\n"
"v_mfma_f32_16x16x32_bf16 v[84:87], v[52:55], a[136:139], v[84:87]\n"
"v_add_f32 v154, 1.0, v154\n"
"v_add_f32 v155, 1.0, v155\n"
"v_mfma_f32_16x16x32_bf16 v[88:91], v[52:55], a[140:143], v[88:91]\n"
"s_waitcnt lgkmcnt(4)\n"
"v_rcp_f32 v154, v154\n"
"v_mfma_f32_16x16x32_bf16 v[108:111], v[56:59], a[128:131], v[108:111]\n"
"ds_read_b64_tr_b16 v[52:53], v5 offset:36864\n"
"ds_read_b64_tr_b16 v[54:55], v5 offset:37120\n"
"v_mfma_f32_16x16x32_bf16 v[112:115], v[56:59], a[132:135], v[112:115]\n"
"v_rcp_f32 v155, v155\n"
"v_mfma_f32_16x16x32_bf16 v[116:119], v[56:59], a[136:139], v[116:119]\n"
"v_mul_f32 v154, v154, v186\n"
"v_mul_f32 v155, v155, v187\n"
"v_mfma_f32_16x16x32_bf16 v[120:123], v[56:59], a[140:143], v[120:123]\n"
"s_waitcnt lgkmcnt(4)\n"
"v_cvt_pk_bf16_f32 v168, v136, v137\n"
"v_cvt_pk_bf16_f32 v169, v138, v139\n"
"v_mfma_f32_16x16x32_bf16 v[60:63], v[44:47], a[144:147], v[60:63]\n"
"s_add_u32 m0, s53, 114688\n"
"ds_read_b64_tr_b16 v[56:57], v5 offset:38912\n"
"ds_read_b64_tr_b16 v[58:59], v5 offset:39168\n"
"v_mfma_f32_16x16x32_bf16 v[64:67], v[44:47], a[148:151], v[64:67]\n"
"s_add_u32 s70, s70, 294912\n"
"buffer_load_dwordx4 v9, s[20:23], s70 offen offset:0 lds\n"
"v_mfma_f32_16x16x32_bf16 v[68:71], v[44:47], a[152:155], v[68:71]\n"
"v_cvt_pk_bf16_f32 v170, v152, v153\n"
"v_cvt_pk_bf16_f32 v171, v154, v155\n"
"v_mfma_f32_16x16x32_bf16 v[72:75], v[44:47], a[156:159], v[72:75]\n"
"s_waitcnt lgkmcnt(4)\n"
"s_barrier\n"
"buffer_store_dwordx4 v[168:171], v10, s[28:31], s74 offen offset:1024 sc1\n"
"v_mfma_f32_16x16x32_bf16 v[92:95], v[48:51], a[144:147], v[92:95]\n"
"s_mov_b32 s88, 1073741824\n"
"ds_read_b64_tr_b16 v[44:45], v2 offset:32768\n"
"ds_read_b64_tr_b16 v[46:47], v2 offset:33024\n"
"v_mov_b32 v164, s34\n"
"v_mfma_f32_16x16x32_bf16 v[96:99], v[48:51], a[148:151], v[96:99]\n"
"s_cmp_eq_u32 s33, 0\ns_cselect_b32 s88, 262144, s88\n"
"v_mov_b32 v165, s35\n"
"v_lshrrev_b32 v168, 4, v0\n"
"s_lshl_b32 s61, s33, 13\n"
"v_mfma_f32_16x16x32_bf16 v[100:103], v[48:51], a[152:155], v[100:103]\n"
"s_add_u32 s61, s61, s53\n"
"v_min_u32 v168, 1, v168\n"
"s_and_b32 s56, s33, 7\n"
"v_and_b32 v124, 63, v0\n"
"v_mfma_f32_16x16x32_bf16 v[104:107], v[48:51], a[156:159], v[104:107]\n"
"s_waitcnt lgkmcnt(4)\n"
"v_lshlrev_b32 v168, 27, v168\n"
"s_add_u32 s48, s61, 28708864\n"
"v_lshrrev_b32 v125, 4, v124\n"
"v_mfma_f32_16x16x32_bf16 v[76:79], v[52:55], a[144:147], v[76:79]\n"
"s_add_u32 s89, s48, 4096\n"
"ds_read_b64_tr_b16 v[48:49], v2 offset:34816\n"
"ds_read_b64_tr_b16 v[50:51], v2 offset:35072\n"
"v_add_lshl_u32 v168, v168, v0, 3\n"
"v_mfma_f32_16x16x32_bf16 v[80:83], v[52:55], a[148:151], v[80:83]\n"
"s_add_u32 s70, s70, 294912\n"
"buffer_store_dwordx2 v[164:165], v168, s[28:31], s88 offen offset:0 sc1\n"
"s_lshr_b32 s62, s9, 1\n"
"v_mfma_f32_16x16x32_bf16 v[84:87], v[52:55], a[152:155], v[84:87]\n"
"s_add_u32 m0, s53, 126976\n"
"s_and_b32 s62, s62, 3\n"
"v_and_b32 v169, 7, v124\n"
"s_lshl_b32 s63, s11, 10\n"
"v_mfma_f32_16x16x32_bf16 v[88:91], v[52:55], a[156:159], v[88:91]\n"
"s_waitcnt lgkmcnt(4)\n"
"buffer_load_dwordx4 v9, s[20:23], s70 offen offset:0 lds\n"
"s_lshl_b32 s62, s62, 3\n"
"v_mfma_f32_16x16x32_bf16 v[108:111], v[56:59], a[144:147], v[108:111]\n"
"s_or_b32 s62, s62, s56\n"
"ds_read_b64_tr_b16 v[52:53], v5 offset:32768\n"
"ds_read_b64_tr_b16 v[54:55], v5 offset:33024\n"
"v_lshlrev_b32 v169, 15, v169\n"
"v_mfma_f32_16x16x32_bf16 v[112:115], v[56:59], a[148:151], v[112:115]\n"
"s_lshl_b32 s62, s62, 10\n"
"v_add_u32 v162, s63, v10\n"
"v_lshrrev_b32 v126, 5, v124\n"
"v_bfe_u32 v127, v124, 3, 2\n"
"v_mfma_f32_16x16x32_bf16 v[116:119], v[56:59], a[152:155], v[116:119]\n"
"s_xor_b32 s59, s45, 128\n"
"v_add_u32 v169, s62, v169\n"
"v_xor_b32 v163, 1024, v162\n"
"v_lshl_add_u32 v126, v126, 4, v127\n"
"v_mfma_f32_16x16x32_bf16 v[120:123], v[56:59], a[156:159], v[120:123]\n"
"s_waitcnt lgkmcnt(4)\n"
"v_and_b32 v127, 1, v125\n"
"v_and_b32 v128, 7, v124\n"
"s_andn2_b32 s59, s59, 64\n"
"v_mfma_f32_16x16x32_bf16 v[60:63], v[44:47], a[160:163], v[60:63]\n"
"ds_read_b64_tr_b16 v[56:57], v5 offset:34816\n"
"ds_read_b64_tr_b16 v[58:59], v5 offset:35072\n"
"v_lshlrev_b32 v127, 1, v127\n"
"v_mfma_f32_16x16x32_bf16 v[64:67], v[44:47], a[164:167], v[64:67]\n"
"v_xor_b32 v127, v127, v128\n"
"v_mul_u32_u24 v126, 1152, v126\n"
"v_bfe_u32 v129, v124, 4, 1\n"
"v_mfma_f32_16x16x32_bf16 v[68:71], v[44:47], a[168:171], v[68:71]\n"
"s_xor_b32 s57, s45, 64\n"
"v_lshlrev_b32 v127, 4, v127\n"
"v_lshrrev_b32 v130, 5, v124\n"
"v_bfe_u32 v131, v124, 2, 2\n"
"v_mfma_f32_16x16x32_bf16 v[72:75], v[44:47], a[172:175], v[72:75]\n"
"s_waitcnt lgkmcnt(4)\n"
"v_add3_u32 v158, v126, v127, s59\n"
"v_lshl_add_u32 v129, v129, 4, v131\n"
"v_and_b32 v132, 3, v124\n"
"v_mfma_f32_16x16x32_bf16 v[92:95], v[48:51], a[160:163], v[92:95]\n"
"s_add_u32 m0, s53, 106496\n"
"ds_read_b64_tr_b16 v[44:45], v2 offset:28672\n"
"ds_read_b64_tr_b16 v[46:47], v2 offset:28928\n"
"v_add_u32 v159, 4608, v158\n"
"v_mfma_f32_16x16x32_bf16 v[96:99], v[48:51], a[164:167], v[96:99]\n"
"s_add_u32 s70, s70, s93\n"
"buffer_load_dwordx4 v9, s[20:23], s70 offen offset:0 lds\n"
"v_lshl_add_u32 v129, v130, 2, v129\n"
"v_mfma_f32_16x16x32_bf16 v[100:103], v[48:51], a[168:171], v[100:103]\n"
"s_lshl_b32 s58, s52, 7\n"
"v_lshlrev_b32 v130, 1, v130\n"
"v_mov_b32 v156, v8\n"
"v_xor_b32 v157, 32, v8\n"
"v_mfma_f32_16x16x32_bf16 v[104:107], v[48:51], a[172:175], v[104:107]\n"
"s_waitcnt lgkmcnt(4)\n"
"v_xor_b32 v130, v130, v132\n"
"v_and_b32 v133, 15, v124\n"
"v_lshl_add_u32 v134, v125, 1, v131\n"
"v_mfma_f32_16x16x32_bf16 v[76:79], v[52:55], a[160:163], v[76:79]\n"
"ds_read_b64_tr_b16 v[48:49], v2 offset:30720\n"
"ds_read_b64_tr_b16 v[50:51], v2 offset:30976\n"
"v_lshlrev_b32 v130, 4, v130\n"
"v_mfma_f32_16x16x32_bf16 v[80:83], v[52:55], a[164:167], v[80:83]\n"
"v_lshl_add_u32 v161, v129, 12, v130\n"
"v_lshlrev_b32 v134, 3, v134\n"
"v_mul_u32_u24 v135, 4160, v132\n"
"v_mfma_f32_16x16x32_bf16 v[84:87], v[52:55], a[168:171], v[84:87]\n"
"s_add_u32 s59, s58, 480\n"
"v_mul_u32_u24 v129, 1152, v129\n"
"v_add3_u32 v172, v134, v135, s58\n"
"v_lshlrev_b32 v136, 5, v133\n"
"v_mfma_f32_16x16x32_bf16 v[88:91], v[52:55], a[172:175], v[88:91]\n"
"s_waitcnt lgkmcnt(4)\n"
"v_add3_u32 v181, v129, v130, s45\n"
"v_add3_u32 v160, v129, v130, s57\n"
"v_add3_u32 v171, v129, v130, s99\n"
"v_mfma_f32_16x16x32_bf16 v[108:111], v[56:59], a[160:163], v[108:111]\n"
"s_and_b32 s60, s33, 3\n"
"ds_read_b64_tr_b16 v[52:53], v5 offset:28672\n"
"ds_read_b64_tr_b16 v[54:55], v5 offset:28928\n"
"v_lshlrev_b32 v137, 4, v125\n"
"v_mfma_f32_16x16x32_bf16 v[112:115], v[56:59], a[164:167], v[112:115]\n"
"s_add_u32 s70, s70, 294912\n"
"v_sub_u32 v137, v137, v136\n"
"s_lshl_b32 s60, s60, 9\n"
"v_lshl_add_u32 v179, v125, 3, v136\n"
"v_mfma_f32_16x16x32_bf16 v[116:119], v[56:59], a[168:171], v[116:119]\n"
"s_add_u32 m0, s53, 118784\n"
"v_add_u32 v173, s59, v137\n"
"s_sub_u32 s61, 3584, s60\n"
"s_lshl_b32 s62, s52, 13\n"
"v_mfma_f32_16x16x32_bf16 v[120:123], v[56:59], a[172:175], v[120:123]\n"
"s_waitcnt lgkmcnt(4)\n"
"buffer_load_dwordx4 v9, s[20:23], s70 offen offset:0 lds\n"
"v_add_u32 v174, s60, v173\n"
"v_mfma_f32_16x16x32_bf16 v[60:63], v[44:47], a[176:179], v[60:63]\n"
"s_add_u32 s62, s62, 110592\n"
"ds_read_b64_tr_b16 v[56:57], v5 offset:30720\n"
"ds_read_b64_tr_b16 v[58:59], v5 offset:30976\n"
"v_add_u32 v175, s61, v173\n"
"v_mfma_f32_16x16x32_bf16 v[64:67], v[44:47], a[180:183], v[64:67]\n"
"s_lshl_b32 s63, s52, 11\n"
"v_add_u32 v176, s62, v10\n"
"s_sub_u32 s57, 3576, s53\n"
"s_and_b32 s56, s52, 1\n"
"v_mfma_f32_16x16x32_bf16 v[68:71], v[44:47], a[184:187], v[68:71]\n"
"s_add_u32 s63, s63, 110592\n"
"v_sub_u32 v178, s57, v179\n"
"s_cmp_eq_u32 s56, 1\ns_cselect_b32 s88, s60, s61\n"
"v_mul_f32 v188, s80, v188\n"
"v_mfma_f32_16x16x32_bf16 v[72:75], v[44:47], a[188:191], v[72:75]\n"
"s_waitcnt lgkmcnt(4)\n"
"v_add_u32 v177, s63, v10\n"
"s_lshr_b32 s56, s33, 2\n"
"v_mul_f32 v189, s80, v189\n"
"v_mfma_f32_16x16x32_bf16 v[92:95], v[48:51], a[176:179], v[92:95]\n"
"s_lshl_b32 s56, s56, 12\n"
"ds_read_b64_tr_b16 v[44:45], v2 offset:0\n"
"ds_read_b64_tr_b16 v[46:47], v2 offset:256\n"
"v_mul_f32 v190, s80, v190\n"
"v_mfma_f32_16x16x32_bf16 v[96:99], v[48:51], a[180:183], v[96:99]\n"
"s_add_u32 s56, s56, s88\n"
"v_exp_f32 v188, v188\n"
"v_mul_f32 v191, s80, v191\n"
"v_mfma_f32_16x16x32_bf16 v[100:103], v[48:51], a[184:187], v[100:103]\n"
"s_add_u32 s49, s56, 30806016\n"
"v_exp_f32 v189, v189\n"
"v_add_f32 v188, 1.0, v188\n"
"v_mfma_f32_16x16x32_bf16 v[104:107], v[48:51], a[188:191], v[104:107]\n"
"s_waitcnt lgkmcnt(4)\n"
"v_rcp_f32 v188, v188\n"
"v_add_f32 v189, 1.0, v189\n"
"v_mfma_f32_16x16x32_bf16 v[76:79], v[52:55], a[176:179], v[76:79]\n"
"s_add_u32 m0, s53, 122880\n"
"ds_read_b64_tr_b16 v[48:49], v2 offset:2048\n"
"ds_read_b64_tr_b16 v[50:51], v2 offset:2304\n"
"v_mul_f32 v188, v188, v220\n"
"v_mfma_f32_16x16x32_bf16 v[80:83], v[52:55], a[180:183], v[80:83]\n"
"s_add_u32 s70, s70, 294912\n"
"buffer_load_dwordx4 v9, s[20:23], s70 offen offset:0 lds\n"
"v_mul_f32 v204, s80, v204\n"
"v_mfma_f32_16x16x32_bf16 v[84:87], v[52:55], a[184:187], v[84:87]\n"
"v_rcp_f32 v189, v189\n"
"v_mul_f32 v205, s80, v205\n"
"v_mfma_f32_16x16x32_bf16 v[88:91], v[52:55], a[188:191], v[88:91]\n"
"s_waitcnt lgkmcnt(4)\n"
"v_exp_f32 v190, v190\n"
"v_mul_f32 v189, v189, v221\n"
"v_mfma_f32_16x16x32_bf16 v[108:111], v[56:59], a[176:179], v[108:111]\n"
"ds_read_b64_tr_b16 v[52:53], v5 offset:0\n"
"ds_read_b64_tr_b16 v[54:55], v5 offset:256\n"
"v_add_f32 v190, 1.0, v190\n"
"v_mfma_f32_16x16x32_bf16 v[112:115], v[56:59], a[180:183], v[112:115]\n"
"v_exp_f32 v191, v191\n"
"v_mul_f32 v206, s80, v206\n"
"v_mfma_f32_16x16x32_bf16 v[116:119], v[56:59], a[184:187], v[116:119]\n"
"v_rcp_f32 v190, v190\n"
"v_add_f32 v191, 1.0, v191\n"
"v_mfma_f32_16x16x32_bf16 v[120:123], v[56:59], a[188:191], v[120:123]\n"
"s_waitcnt lgkmcnt(4)\n"
"v_rcp_f32 v191, v191\n"
"v_mul_f32 v190, v190, v222\n"
"v_mfma_f32_16x16x32_bf16 v[60:63], v[44:47], a[192:195], v[60:63]\n"
"ds_read_b64_tr_b16 v[56:57], v5 offset:2048\n"
"ds_read_b64_tr_b16 v[58:59], v5 offset:2304\n"
"v_mul_f32 v191, v191, v223\n"
"v_mfma_f32_16x16x32_bf16 v[64:67], v[44:47], a[196:199], v[64:67]\n"
"v_exp_f32 v204, v204\n"
"v_mul_f32 v207, s80, v207\n"
"v_mfma_f32_16x16x32_bf16 v[68:71], v[44:47], a[200:203], v[68:71]\n"
"v_exp_f32 v205, v205\n"
"v_add_f32 v204, 1.0, v204\n"
"v_mfma_f32_16x16x32_bf16 v[72:75], v[44:47], a[204:207], v[72:75]\n"
"s_waitcnt vmcnt(5) lgkmcnt(4)\n"
"s_barrier\n"
"v_rcp_f32 v204, v204\n"
"v_mfma_f32_16x16x32_bf16 v[92:95], v[48:51], a[192:195], v[92:95]\n"
"ds_read_b64_tr_b16 v[44:45], v2 offset:4096\n"
"ds_read_b64_tr_b16 v[46:47], v2 offset:4352\n"
"v_add_f32 v205, 1.0, v205\n"
"v_mfma_f32_16x16x32_bf16 v[96:99], v[48:51], a[196:199], v[96:99]\n"
"buffer_store_dwordx2 v[164:165], v168, s[28:31], s82 offen offset:256 sc1\n"
"v_mul_f32 v204, v204, v236\n"
"v_mfma_f32_16x16x32_bf16 v[100:103], v[48:51], a[200:203], v[100:103]\n"
"v_rcp_f32 v205, v205\n"
"v_cvt_pk_bf16_f32 v220, v188, v189\n"
"v_mfma_f32_16x16x32_bf16 v[104:107], v[48:51], a[204:207], v[104:107]\n"
"s_waitcnt lgkmcnt(4)\n"
"v_exp_f32 v206, v206\n"
"v_mul_f32 v205, v205, v237\n"
"v_mfma_f32_16x16x32_bf16 v[76:79], v[52:55], a[192:195], v[76:79]\n"
"ds_read_b64_tr_b16 v[48:49], v2 offset:6144\n"
"ds_read_b64_tr_b16 v[50:51], v2 offset:6400\n"
"v_add_f32 v206, 1.0, v206\n"
"v_mfma_f32_16x16x32_bf16 v[80:83], v[52:55], a[196:199], v[80:83]\n"
"v_exp_f32 v207, v207\n"
"v_cvt_pk_bf16_f32 v221, v190, v191\n"
"v_mfma_f32_16x16x32_bf16 v[84:87], v[52:55], a[200:203], v[84:87]\n"
"v_rcp_f32 v206, v206\n"
"v_add_f32 v207, 1.0, v207\n"
"v_mfma_f32_16x16x32_bf16 v[88:91], v[52:55], a[204:207], v[88:91]\n"
"s_waitcnt lgkmcnt(4)\n"
"v_rcp_f32 v207, v207\n"
"v_mul_f32 v206, v206, v238\n"
"v_mfma_f32_16x16x32_bf16 v[108:111], v[56:59], a[192:195], v[108:111]\n"
"ds_read_b64_tr_b16 v[52:53], v5 offset:4096\n"
"ds_read_b64_tr_b16 v[54:55], v5 offset:4352\n"
"v_mul_f32 v207, v207, v239\n"
"v_mfma_f32_16x16x32_bf16 v[112:115], v[56:59], a[196:199], v[112:115]\n"
"s_add_u32 s70, s70, 294912\n"
"v_cvt_pk_bf16_f32 v222, v204, v205\n"
"v_cvt_pk_bf16_f32 v223, v206, v207\n"
"v_mul_f32 v192, s80, v192\n"
"v_mfma_f32_16x16x32_bf16 v[116:119], v[56:59], a[200:203], v[116:119]\n"
"s_add_u32 m0, s53, 94208\n"
"buffer_store_dwordx4 v[220:223], v10, s[28:31], s75 offen offset:0 sc1\n"
"v_mul_f32 v193, s80, v193\n"
"v_mfma_f32_16x16x32_bf16 v[120:123], v[56:59], a[204:207], v[120:123]\n"
"s_waitcnt lgkmcnt(4)\n"
"buffer_load_dwordx4 v9, s[20:23], s70 offen offset:0 lds\n"
"v_mul_f32 v194, s80, v194\n"
"v_mfma_f32_16x16x32_bf16 v[60:63], v[44:47], a[208:211], v[60:63]\n"
"ds_read_b64_tr_b16 v[56:57], v5 offset:6144\n"
"ds_read_b64_tr_b16 v[58:59], v5 offset:6400\n"
"v_mul_f32 v195, s80, v195\n"
"v_mfma_f32_16x16x32_bf16 v[64:67], v[44:47], a[212:215], v[64:67]\n"
"v_exp_f32 v192, v192\n"
"v_mul_f32 v208, s80, v208\n"
"v_mfma_f32_16x16x32_bf16 v[68:71], v[44:47], a[216:219], v[68:71]\n"
"v_exp_f32 v193, v193\n"
"v_add_f32 v192, 1.0, v192\n"
"v_mfma_f32_16x16x32_bf16 v[72:75], v[44:47], a[220:223], v[72:75]\n"
"s_waitcnt lgkmcnt(4)\n"
"v_rcp_f32 v192, v192\n"
"v_add_f32 v193, 1.0, v193\n"
"v_mfma_f32_16x16x32_bf16 v[92:95], v[48:51], a[208:211], v[92:95]\n"
"ds_read_b64_tr_b16 v[44:45], v2 offset:8192\n"
"ds_read_b64_tr_b16 v[46:47], v2 offset:8448\n"
"v_mul_f32 v192, v192, v224\n"
"v_mfma_f32_16x16x32_bf16 v[96:99], v[48:51], a[212:215], v[96:99]\n"
"v_rcp_f32 v193, v193\n"
"v_mul_f32 v209, s80, v209\n"
"v_mfma_f32_16x16x32_bf16 v[100:103], v[48:51], a[216:219], v[100:103]\n"
"v_exp_f32 v194, v194\n"
"v_mul_f32 v193, v193, v225\n"
"v_mfma_f32_16x16x32_bf16 v[104:107], v[48:51], a[220:223], v[104:107]\n"
"s_waitcnt lgkmcnt(4)\n"
"v_exp_f32 v195, v195\n"
"v_add_f32 v194, 1.0, v194\n"
"v_mfma_f32_16x16x32_bf16 v[76:79], v[52:55], a[208:211], v[76:79]\n"
"s_add_u32 m0, s53, 98304\n"
"ds_read_b64_tr_b16 v[48:49], v2 offset:10240\n"
"ds_read_b64_tr_b16 v[50:51], v2 offset:10496\n"
"v_add_f32 v195, 1.0, v195\n"
"v_mfma_f32_16x16x32_bf16 v[80:83], v[52:55], a[212:215], v[80:83]\n"
"s_add_u32 s70, s70, 294912\n"
"buffer_load_dwordx4 v9, s[20:23], s70 offen offset:0 lds\n"
"v_mul_f32 v210, s80, v210\n"
"v_mfma_f32_16x16x32_bf16 v[84:87], v[52:55], a[216:219], v[84:87]\n"
"v_rcp_f32 v194, v194\n"
"v_mul_f32 v211, s80, v211\n"
"v_mfma_f32_16x16x32_bf16 v[88:91], v[52:55], a[220:223], v[88:91]\n"
"s_waitcnt lgkmcnt(4)\n"
"v_rcp_f32 v195, v195\n"
"v_mul_f32 v194, v194, v226\n"
"v_mfma_f32_16x16x32_bf16 v[108:111], v[56:59], a[208:211], v[108:111]\n"
"ds_read_b64_tr_b16 v[52:53], v5 offset:8192\n"
"ds_read_b64_tr_b16 v[54:55], v5 offset:8448\n"
"v_mul_f32 v195, v195, v227\n"
"v_mfma_f32_16x16x32_bf16 v[112:115], v[56:59], a[212:215], v[112:115]\n"
"v_exp_f32 v208, v208\n"
"v_cvt_pk_bf16_f32 v224, v192, v193\n"
"v_mfma_f32_16x16x32_bf16 v[116:119], v[56:59], a[216:219], v[116:119]\n"
"v_exp_f32 v209, v209\n"
"v_add_f32 v208, 1.0, v208\n"
"v_mfma_f32_16x16x32_bf16 v[120:123], v[56:59], a[220:223], v[120:123]\n"
"s_waitcnt lgkmcnt(4)\n"
"v_rcp_f32 v208, v208\n"
"v_add_f32 v209, 1.0, v209\n"
"v_mfma_f32_16x16x32_bf16 v[60:63], v[44:47], a[224:227], v[60:63]\n"
"ds_read_b64_tr_b16 v[56:57], v5 offset:10240\n"
"ds_read_b64_tr_b16 v[58:59], v5 offset:10496\n"
"v_mul_f32 v208, v208, v240\n"
"v_mfma_f32_16x16x32_bf16 v[64:67], v[44:47], a[228:231], v[64:67]\n"
"s_add_u32 s70, s70, 294912\n"
"v_rcp_f32 v209, v209\n"
"v_cvt_pk_bf16_f32 v225, v194, v195\n"
"v_mfma_f32_16x16x32_bf16 v[68:71], v[44:47], a[232:235], v[68:71]\n"
"s_add_u32 m0, s53, 102400\n"
"v_exp_f32 v210, v210\n"
"v_mul_f32 v209, v209, v241\n"
"v_mfma_f32_16x16x32_bf16 v[72:75], v[44:47], a[236:239], v[72:75]\n"
"s_waitcnt lgkmcnt(4)\n"
"buffer_load_dwordx4 v9, s[20:23], s70 offen offset:0 lds\n"
"v_add_f32 v210, 1.0, v210\n"
"v_mfma_f32_16x16x32_bf16 v[92:95], v[48:51], a[224:227], v[92:95]\n"
"s_lshl_b32 s56, s52, 11\n"
"ds_read_b64_tr_b16 v[44:45], v2 offset:12288\n"
"ds_read_b64_tr_b16 v[46:47], v2 offset:12544\n"
"v_cvt_pk_bf16_f32 v226, v208, v209\n"
"v_mfma_f32_16x16x32_bf16 v[96:99], v[48:51], a[228:231], v[96:99]\n"
"s_add_u32 s56, s56, 143360\n"
"v_exp_f32 v211, v211\n"
"v_mul_f32 v196, s80, v196\n"
"v_mfma_f32_16x16x32_bf16 v[100:103], v[48:51], a[232:235], v[100:103]\n"
"s_and_b32 s57, s52, 1\n"
"v_rcp_f32 v210, v210\n"
"v_add_f32 v211, 1.0, v211\n"
"v_mfma_f32_16x16x32_bf16 v[104:107], v[48:51], a[236:239], v[104:107]\n"
"s_waitcnt lgkmcnt(4)\n"
"v_rcp_f32 v211, v211\n"
"v_mul_f32 v210, v210, v242\n"
"v_mfma_f32_16x16x32_bf16 v[76:79], v[52:55], a[224:227], v[76:79]\n"
"s_lshl_b32 s57, s57, 10\n"
"ds_read_b64_tr_b16 v[48:49], v2 offset:14336\n"
"ds_read_b64_tr_b16 v[50:51], v2 offset:14592\n"
"v_mul_f32 v211, v211, v243\n"
"v_mfma_f32_16x16x32_bf16 v[80:83], v[52:55], a[228:231], v[80:83]\n"
"s_add_u32 s57, s57, 143360\n"
"v_exp_f32 v196, v196\n"
"v_cvt_pk_bf16_f32 v227, v210, v211\n"
"v_mfma_f32_16x16x32_bf16 v[84:87], v[52:55], a[232:235], v[84:87]\n"
"s_sub_u32 s58, 4088, s88\n"
"buffer_store_dwordx4 v[224:227], v10, s[28:31], s75 offen offset:1024 sc1\n"
"v_mul_f32 v197, s80, v197\n"
"v_mfma_f32_16x16x32_bf16 v[88:91], v[52:55], a[236:239], v[88:91]\n"
"s_waitcnt lgkmcnt(4)\n"
"v_exp_f32 v197, v197\n"
"v_add_f32 v196, 1.0, v196\n"
"v_mfma_f32_16x16x32_bf16 v[108:111], v[56:59], a[224:227], v[108:111]\n"
"ds_read_b64_tr_b16 v[52:53], v5 offset:12288\n"
"ds_read_b64_tr_b16 v[54:55], v5 offset:12544\n"
"v_add_f32 v197, 1.0, v197\n"
"v_mfma_f32_16x16x32_bf16 v[112:115], v[56:59], a[228:231], v[112:115]\n"
"v_rcp_f32 v196, v196\n"
"v_mul_f32 v198, s80, v198\n"
"v_mfma_f32_16x16x32_bf16 v[116:119], v[56:59], a[232:235], v[116:119]\n"
"v_rcp_f32 v197, v197\n"
"v_mul_f32 v196, v196, v228\n"
"v_mfma_f32_16x16x32_bf16 v[120:123], v[56:59], a[236:239], v[120:123]\n"
"s_waitcnt lgkmcnt(4)\n"
"v_exp_f32 v198, v198\n"
"v_mul_f32 v197, v197, v229\n"
"v_mfma_f32_16x16x32_bf16 v[60:63], v[44:47], a[240:243], v[60:63]\n"
"ds_read_b64_tr_b16 v[56:57], v5 offset:14336\n"
"ds_read_b64_tr_b16 v[58:59], v5 offset:14592\n"
"v_mul_f32 v199, s80, v199\n"
"v_mfma_f32_16x16x32_bf16 v[64:67], v[44:47], a[244:247], v[64:67]\n"
"buffer_load_dwordx2 v[166:167], v169, s[28:31], 0 offen offset:128 sc1\n"
"v_add_f32 v198, 1.0, v198\n"
"v_mfma_f32_16x16x32_bf16 v[68:71], v[44:47], a[248:251], v[68:71]\n"
"v_exp_f32 v199, v199\n"
"v_mul_f32 v212, s80, v212\n"
"v_mfma_f32_16x16x32_bf16 v[72:75], v[44:47], a[252:255], v[72:75]\n"
"s_waitcnt lgkmcnt(4)\n"
"v_rcp_f32 v198, v198\n"
"v_add_f32 v199, 1.0, v199\n"
"v_mfma_f32_16x16x32_bf16 v[92:95], v[48:51], a[240:243], v[92:95]\n"
"ds_read_b64_tr_b16 v[44:45], v2 offset:16384\n"
"ds_read_b64_tr_b16 v[46:47], v2 offset:16640\n"
"v_mul_f32 v198, v198, v230\n"
"v_mfma_f32_16x16x32_bf16 v[96:99], v[48:51], a[244:247], v[96:99]\n"
"v_rcp_f32 v199, v199\n"
"v_mul_f32 v213, s80, v213\n"
"v_mfma_f32_16x16x32_bf16 v[100:103], v[48:51], a[248:251], v[100:103]\n"
"v_exp_f32 v212, v212\n"
"v_mul_f32 v199, v199, v231\n"
"v_mfma_f32_16x16x32_bf16 v[104:107], v[48:51], a[252:255], v[104:107]\n"
"s_waitcnt lgkmcnt(4)\n"
"v_exp_f32 v213, v213\n"
"v_add_f32 v212, 1.0, v212\n"
"v_mfma_f32_16x16x32_bf16 v[76:79], v[52:55], a[240:243], v[76:79]\n"
"ds_read_b64_tr_b16 v[48:49], v2 offset:18432\n"
"ds_read_b64_tr_b16 v[50:51], v2 offset:18688\n"
"v_add_f32 v213, 1.0, v213\n"
"v_mfma_f32_16x16x32_bf16 v[80:83], v[52:55], a[244:247], v[80:83]\n"
"v_rcp_f32 v212, v212\n"
"v_mul_f32 v214, s80, v214\n"
"v_mfma_f32_16x16x32_bf16 v[84:87], v[52:55], a[248:251], v[84:87]\n"
"v_rcp_f32 v213, v213\n"
"v_mul_f32 v212, v212, v244\n"
"v_mfma_f32_16x16x32_bf16 v[88:91], v[52:55], a[252:255], v[88:91]\n"
"s_waitcnt lgkmcnt(4)\n"
"v_exp_f32 v214, v214\n"
"v_mul_f32 v213, v213, v245\n"
"v_mfma_f32_16x16x32_bf16 v[108:111], v[56:59], a[240:243], v[108:111]\n"
"ds_read_b64_tr_b16 v[52:53], v5 offset:16384\n"
"ds_read_b64_tr_b16 v[54:55], v5 offset:16640\n"
"v_mul_f32 v215, s80, v215\n"
"v_mfma_f32_16x16x32_bf16 v[112:115], v[56:59], a[244:247], v[112:115]\n"
"v_exp_f32 v215, v215\n"
"v_add_f32 v214, 1.0, v214\n"
"v_mfma_f32_16x16x32_bf16 v[116:119], v[56:59], a[248:251], v[116:119]\n"
"v_rcp_f32 v214, v214\n"
"v_add_f32 v215, 1.0, v215\n"
"v_mfma_f32_16x16x32_bf16 v[120:123], v[56:59], a[252:255], v[120:123]\n"
"s_waitcnt lgkmcnt(4)\n"
"v_rcp_f32 v215, v215\n"
"v_mul_f32 v214, v214, v246\n"
"v_mfma_f32_16x16x32_bf16 v[60:63], v[44:47], v[12:15], v[60:63]\n"
"ds_read_b64_tr_b16 v[56:57], v5 offset:18432\n"
"ds_read_b64_tr_b16 v[58:59], v5 offset:18688\n"
"v_mul_f32 v215, v215, v247\n"
"v_mfma_f32_16x16x32_bf16 v[64:67], v[44:47], v[16:19], v[64:67]\n"
"v_cvt_pk_bf16_f32 v228, v196, v197\n"
"v_cvt_pk_bf16_f32 v229, v198, v199\n"
"v_cvt_pk_bf16_f32 v230, v212, v213\n"
"v_mfma_f32_16x16x32_bf16 v[68:71], v[44:47], v[20:23], v[68:71]\n"
"v_cvt_pk_bf16_f32 v231, v214, v215\n"
"v_mul_f32 v200, s80, v200\n"
"v_mul_f32 v201, s80, v201\n"
"v_mfma_f32_16x16x32_bf16 v[72:75], v[44:47], v[24:27], v[72:75]\n"
"s_waitcnt lgkmcnt(4)\n"
"buffer_store_dwordx4 v[228:231], v10, s[28:31], s76 offen offset:0 sc1\n"
"v_mul_f32 v202, s80, v202\n"
"v_mfma_f32_16x16x32_bf16 v[92:95], v[48:51], v[12:15], v[92:95]\n"
"ds_read_b64_tr_b16 v[44:45], v2 offset:20480\n"
"ds_read_b64_tr_b16 v[46:47], v2 offset:20736\n"
"v_mul_f32 v203, s80, v203\n"
"v_mfma_f32_16x16x32_bf16 v[96:99], v[48:51], v[16:19], v[96:99]\n"
"v_exp_f32 v200, v200\n"
"v_mul_f32 v216, s80, v216\n"
"v_mfma_f32_16x16x32_bf16 v[100:103], v[48:51], v[20:23], v[100:103]\n"
"v_exp_f32 v201, v201\n"
"v_add_f32 v200, 1.0, v200\n"
"v_mfma_f32_16x16x32_bf16 v[104:107], v[48:51], v[24:27], v[104:107]\n"
"s_waitcnt lgkmcnt(4)\n"
"v_rcp_f32 v200, v200\n"
"v_add_f32 v201, 1.0, v201\n"
"v_mfma_f32_16x16x32_bf16 v[76:79], v[52:55], v[12:15], v[76:79]\n"
"ds_read_b64_tr_b16 v[48:49], v2 offset:22528\n"
"ds_read_b64_tr_b16 v[50:51], v2 offset:22784\n"
"v_mul_f32 v200, v200, v232\n"
"v_mfma_f32_16x16x32_bf16 v[80:83], v[52:55], v[16:19], v[80:83]\n"
"v_rcp_f32 v201, v201\n"
"v_mul_f32 v217, s80, v217\n"
"v_mfma_f32_16x16x32_bf16 v[84:87], v[52:55], v[20:23], v[84:87]\n"
"v_exp_f32 v202, v202\n"
"v_mul_f32 v201, v201, v233\n"
"v_mfma_f32_16x16x32_bf16 v[88:91], v[52:55], v[24:27], v[88:91]\n"
"s_waitcnt lgkmcnt(4)\n"
"v_exp_f32 v203, v203\n"
"v_add_f32 v202, 1.0, v202\n"
"v_mfma_f32_16x16x32_bf16 v[108:111], v[56:59], v[12:15], v[108:111]\n"
"ds_read_b64_tr_b16 v[52:53], v5 offset:20480\n"
"ds_read_b64_tr_b16 v[54:55], v5 offset:20736\n"
"v_add_f32 v203, 1.0, v203\n"
"v_mfma_f32_16x16x32_bf16 v[112:115], v[56:59], v[16:19], v[112:115]\n"
"v_rcp_f32 v202, v202\n"
"v_mul_f32 v218, s80, v218\n"
"v_mfma_f32_16x16x32_bf16 v[116:119], v[56:59], v[20:23], v[116:119]\n"
"v_rcp_f32 v203, v203\n"
"v_mul_f32 v202, v202, v234\n"
"v_mfma_f32_16x16x32_bf16 v[120:123], v[56:59], v[24:27], v[120:123]\n"
"s_waitcnt lgkmcnt(4)\n"
"v_exp_f32 v216, v216\n"
"v_mul_f32 v203, v203, v235\n"
"v_mfma_f32_16x16x32_bf16 v[60:63], v[44:47], v[28:31], v[60:63]\n"
"ds_read_b64_tr_b16 v[56:57], v5 offset:22528\n"
"ds_read_b64_tr_b16 v[58:59], v5 offset:22784\n"
"v_add_f32 v216, 1.0, v216\n"
"v_mfma_f32_16x16x32_bf16 v[64:67], v[44:47], v[32:35], v[64:67]\n"
"v_exp_f32 v217, v217\n"
"v_mul_f32 v219, s80, v219\n"
"v_mfma_f32_16x16x32_bf16 v[68:71], v[44:47], v[36:39], v[68:71]\n"
"v_rcp_f32 v216, v216\n"
"v_add_f32 v217, 1.0, v217\n"
"v_mfma_f32_16x16x32_bf16 v[72:75], v[44:47], v[40:43], v[72:75]\n"
"s_waitcnt vmcnt(3) lgkmcnt(4)\n"
"s_barrier\n"
"v_rcp_f32 v217, v217\n"
"v_mfma_f32_16x16x32_bf16 v[92:95], v[48:51], v[28:31], v[92:95]\n"
"s_add_u32 m0, s54, 65536\n"
"ds_read_b64_tr_b16 v[44:45], v3 offset:12288\n"
"ds_read_b64_tr_b16 v[46:47], v3 offset:12544\n"
"v_mul_f32 v216, v216, v248\n"
"v_mfma_f32_16x16x32_bf16 v[96:99], v[48:51], v[32:35], v[96:99]\n"
"s_add_u32 s84, s12, 0\n"
"buffer_load_dwordx4 v158, s[36:39], s84 offen offset:0 lds\n"
"v_mul_f32 v217, v217, v249\n"
"v_mfma_f32_16x16x32_bf16 v[100:103], v[48:51], v[36:39], v[100:103]\n"
"v_exp_f32 v218, v218\n"
"v_cvt_pk_bf16_f32 v232, v200, v201\n"
"v_mfma_f32_16x16x32_bf16 v[104:107], v[48:51], v[40:43], v[104:107]\n"
"s_waitcnt lgkmcnt(4)\n"
"v_exp_f32 v219, v219\n"
"v_add_f32 v218, 1.0, v218\n"
"v_mfma_f32_16x16x32_bf16 v[76:79], v[52:55], v[28:31], v[76:79]\n"
"s_add_u32 m0, s54, 66624\n"
"ds_read_b64_tr_b16 v[48:49], v3 offset:14336\n"
"ds_read_b64_tr_b16 v[50:51], v3 offset:14592\n"
"v_add_f32 v219, 1.0, v219\n"
"v_mfma_f32_16x16x32_bf16 v[80:83], v[52:55], v[32:35], v[80:83]\n"
"buffer_load_dwordx4 v159, s[36:39], s84 offen offset:0 lds\n"
"v_cvt_pk_bf16_f32 v233, v202, v203\n"
"v_mfma_f32_16x16x32_bf16 v[84:87], v[52:55], v[36:39], v[84:87]\n"
"v_rcp_f32 v218, v218\n"
"v_cvt_pk_bf16_f32 v234, v216, v217\n"
"v_mfma_f32_16x16x32_bf16 v[88:91], v[52:55], v[40:43], v[88:91]\n"
"s_waitcnt lgkmcnt(4)\n"
"v_rcp_f32 v219, v219\n"
"v_mul_f32 v218, v218, v250\n"
"v_mfma_f32_16x16x32_bf16 v[108:111], v[56:59], v[28:31], v[108:111]\n"
"s_add_u32 m0, s54, 74240\n"
"ds_read_b64_tr_b16 v[52:53], v6 offset:12288\n"
"ds_read_b64_tr_b16 v[54:55], v6 offset:12544\n"
"v_mul_f32 v219, v219, v251\n"
"v_mfma_f32_16x16x32_bf16 v[112:115], v[56:59], v[32:35], v[112:115]\n"
"s_add_u32 s84, s13, 0\n"
"buffer_load_dwordx4 v158, s[36:39], s84 offen offset:0 lds\n"
"v_cvt_pk_bf16_f32 v235, v218, v219\n"
"v_mfma_f32_16x16x32_bf16 v[116:119], v[56:59], v[36:39], v[116:119]\n"
"s_waitcnt vmcnt(4)\n"
"v_cmp_ne_u64 vcc, s[34:35], v[166:167]\n"
"s_nop 3\n"
"s_cbranch_vccz pF2A_ok_gated_mix_fused\n"
"s_mov_b32 s83, 0\n"
"pF2A_sp_gated_mix_fused:\n"
"buffer_load_dwordx2 v[166:167], v169, s[28:31], 0 offen offset:128 sc1\n"
"s_waitcnt vmcnt(0)\n"
"s_add_u32 s83, s83, 1\n"
"s_cmp_gt_u32 s83, 1048576\n"
"s_cbranch_scc1 pF2A_ok_gated_mix_fused\n"
"v_cmp_ne_u64 vcc, s[34:35], v[166:167]\n"
"s_nop 3\n"
"s_cbranch_vccnz pF2A_sp_gated_mix_fused\n"
"pF2A_ok_gated_mix_fused:\n"
"v_mfma_f32_16x16x32_bf16 v[120:123], v[56:59], v[40:43], v[120:123]\n"
"s_waitcnt lgkmcnt(4)\n"
"buffer_store_dwordx4 v[232:235], v10, s[28:31], s76 offen offset:1024 sc1\n"
"v_mfma_f32_16x16x32_bf16 v[124:127], v[44:47], a[0:3], 0\n"
"s_add_u32 m0, s54, 75328\n"
"ds_read_b64_tr_b16 v[56:57], v6 offset:14336\n"
"ds_read_b64_tr_b16 v[58:59], v6 offset:14592\n"
"v_mfma_f32_16x16x32_bf16 v[128:131], v[44:47], a[4:7], 0\n"
"s_waitcnt lgkmcnt(4)\n"
"buffer_load_dwordx4 v159, s[36:39], s84 offen offset:0 lds\n"
"v_mfma_f32_16x16x32_bf16 v[140:143], v[48:51], a[0:3], 0\n"
"ds_read_b64_tr_b16 v[44:45], v3 offset:28672\n"
"ds_read_b64_tr_b16 v[46:47], v3 offset:28928\n"
"v_mfma_f32_16x16x32_bf16 v[144:147], v[48:51], a[4:7], 0\n"
"s_waitcnt lgkmcnt(4)\n"
"v_add_u32 v252, s56, v10\n"
"v_add_u32 v253, s57, v10\n"
"v_mfma_f32_16x16x32_bf16 v[132:135], v[52:55], a[0:3], 0\n"
"s_add_u32 s44, s64, 0\n"
"ds_read_b64_tr_b16 v[48:49], v3 offset:30720\n"
"ds_read_b64_tr_b16 v[50:51], v3 offset:30976\n"
"v_mul_f32 v60, s80, v60\n"
"v_mul_f32 v61, s80, v61\n"
"v_mfma_f32_16x16x32_bf16 v[136:139], v[52:55], a[4:7], 0\n"
"s_waitcnt lgkmcnt(4)\n"
"buffer_load_dwordx4 a[8:11], v162, s[28:31], s44 offen offset:0 sc1\n"
"v_exp_f32 v60, v60\n"
"v_mfma_f32_16x16x32_bf16 v[148:151], v[56:59], a[0:3], 0\n"
"ds_read_b64_tr_b16 v[52:53], v6 offset:28672\n"
"ds_read_b64_tr_b16 v[54:55], v6 offset:28928\n"
"v_exp_f32 v61, v61\n"
"v_mfma_f32_16x16x32_bf16 v[152:155], v[56:59], a[4:7], 0\n"
"s_waitcnt lgkmcnt(4)\n"
"v_sub_u32 v254, s58, v179\n"
"v_subrev_u32 v7, 65536, v156\n"
"v_add_f32 v60, 1.0, v60\n"
"v_add_f32 v61, 1.0, v61\n"
"v_mfma_f32_16x16x32_bf16 v[124:127], v[44:47], a[16:19], v[124:127]\n"
"ds_read_b64_tr_b16 v[56:57], v6 offset:30720\n"
"ds_read_b64_tr_b16 v[58:59], v6 offset:30976\n"
"v_rcp_f32 v60, v60\n"
"v_mfma_f32_16x16x32_bf16 v[128:131], v[44:47], a[20:23], v[128:131]\n"
"s_waitcnt lgkmcnt(4)\n"
"buffer_load_dwordx4 a[12:15], v163, s[28:31], s44 offen offset:0 sc1\n"
"v_rcp_f32 v61, v61\n"
"v_mfma_f32_16x16x32_bf16 v[140:143], v[48:51], a[16:19], v[140:143]\n"
"ds_read_b64_tr_b16 v[44:45], v3 offset:8192\n"
"ds_read_b64_tr_b16 v[46:47], v3 offset:8448\n"
"v_mul_f32 v60, v60, v92\n"
"v_mul_f32 v61, v61, v93\n"
"v_mfma_f32_16x16x32_bf16 v[144:147], v[48:51], a[20:23], v[144:147]\n"
"s_waitcnt lgkmcnt(4)\n"
"v_subrev_u32 v9, 65536, v157\n"
"v_mul_f32 v62, s80, v62\n"
"v_mul_f32 v63, s80, v63\n"
"v_mul_f32 v76, s80, v76\n"
"v_mfma_f32_16x16x32_bf16 v[132:135], v[52:55], a[16:19], v[132:135]\n"
"ds_read_b64_tr_b16 v[48:49], v3 offset:10240\n"
"ds_read_b64_tr_b16 v[50:51], v3 offset:10496\n"
"v_exp_f32 v62, v62\n"
"v_mfma_f32_16x16x32_bf16 v[136:139], v[52:55], a[20:23], v[136:139]\n"
"s_waitcnt lgkmcnt(4)\n"
"buffer_load_dwordx2 v[166:167], v169, s[28:31], 0 offen offset:256 sc1\n"
"v_exp_f32 v63, v63\n"
"v_mfma_f32_16x16x32_bf16 v[148:151], v[56:59], a[16:19], v[148:151]\n"
"ds_read_b64_tr_b16 v[52:53], v6 offset:8192\n"
"ds_read_b64_tr_b16 v[54:55], v6 offset:8448\n"
"v_exp_f32 v76, v76\n"
"v_mfma_f32_16x16x32_bf16 v[152:155], v[56:59], a[20:23], v[152:155]\n"
"s_waitcnt lgkmcnt(4)\n"
"v_add_f32 v62, 1.0, v62\n"
"v_add_f32 v63, 1.0, v63\n"
"v_mul_f32 v77, s80, v77\n"
"v_add_f32 v76, 1.0, v76\n"
"v_mfma_f32_16x16x32_bf16 v[124:127], v[44:47], a[32:35], v[124:127]\n"
"s_add_u32 s44, s64, 73728\n"
"ds_read_b64_tr_b16 v[56:57], v6 offset:10240\n"
"ds_read_b64_tr_b16 v[58:59], v6 offset:10496\n"
"v_rcp_f32 v62, v62\n"
"v_mfma_f32_16x16x32_bf16 v[128:131], v[44:47], a[36:39], v[128:131]\n"
"s_waitcnt lgkmcnt(4)\n"
"buffer_load_dwordx4 a[24:27], v162, s[28:31], s44 offen offset:0 sc1\n"
"v_rcp_f32 v63, v63\n"
"v_mfma_f32_16x16x32_bf16 v[140:143], v[48:51], a[32:35], v[140:143]\n"
"ds_read_b64_tr_b16 v[44:45], v3 offset:16384\n"
"ds_read_b64_tr_b16 v[46:47], v3 offset:16640\n"
"v_exp_f32 v77, v77\n"
"v_mfma_f32_16x16x32_bf16 v[144:147], v[48:51], a[36:39], v[144:147]\n"
"s_waitcnt lgkmcnt(4)\n"
"v_rcp_f32 v76, v76\n"
"v_mul_f32 v62, v62, v94\n"
"v_mul_f32 v63, v63, v95\n"
"v_mfma_f32_16x16x32_bf16 v[132:135], v[52:55], a[32:35], v[132:135]\n"
"ds_read_b64_tr_b16 v[48:49], v3 offset:18432\n"
"ds_read_b64_tr_b16 v[50:51], v3 offset:18688\n"
"v_add_f32 v77, 1.0, v77\n"
"v_mul_f32 v76, v76, v108\n"
"v_mfma_f32_16x16x32_bf16 v[136:139], v[52:55], a[36:39], v[136:139]\n"
"s_waitcnt lgkmcnt(4)\n"
"buffer_load_dwordx4 a[28:31], v163, s[28:31], s44 offen offset:0 sc1\n"
"v_rcp_f32 v77, v77\n"
"v_mfma_f32_16x16x32_bf16 v[148:151], v[56:59], a[32:35], v[148:151]\n"
"ds_read_b64_tr_b16 v[52:53], v6 offset:16384\n"
"ds_read_b64_tr_b16 v[54:55], v6 offset:16640\n"
"v_mul_f32 v77, v77, v109\n"
"v_mul_f32 v78, s80, v78\n"
"v_mfma_f32_16x16x32_bf16 v[152:155], v[56:59], a[36:39], v[152:155]\n"
"s_waitcnt lgkmcnt(4)\n"
"v_exp_f32 v78, v78\n"
"v_mul_f32 v79, s80, v79\n"
"v_cvt_pk_bf16_f32 v92, v60, v61\n"
"v_mfma_f32_16x16x32_bf16 v[124:127], v[44:47], a[48:51], v[124:127]\n"
"s_add_u32 s44, s65, 0\n"
"ds_read_b64_tr_b16 v[56:57], v6 offset:18432\n"
"ds_read_b64_tr_b16 v[58:59], v6 offset:18688\n"
"v_exp_f32 v79, v79\n"
"v_mfma_f32_16x16x32_bf16 v[128:131], v[44:47], a[52:55], v[128:131]\n"
"s_waitcnt lgkmcnt(4)\n"
"buffer_load_dwordx4 a[72:75], v162, s[28:31], s44 offen offset:0 sc1\n"
"v_add_f32 v78, 1.0, v78\n"
"v_add_f32 v79, 1.0, v79\n"
"v_mfma_f32_16x16x32_bf16 v[140:143], v[48:51], a[48:51], v[140:143]\n"
"ds_read_b64_tr_b16 v[44:45], v3 offset:20480\n"
"ds_read_b64_tr_b16 v[46:47], v3 offset:20736\n"
"v_rcp_f32 v78, v78\n"
"v_mfma_f32_16x16x32_bf16 v[144:147], v[48:51], a[52:55], v[144:147]\n"
"s_waitcnt lgkmcnt(4)\n"
"v_rcp_f32 v79, v79\n"
"v_mul_f32 v78, v78, v110\n"
"v_cvt_pk_bf16_f32 v93, v62, v63\n"
"v_mfma_f32_16x16x32_bf16 v[132:135], v[52:55], a[48:51], v[132:135]\n"
"ds_read_b64_tr_b16 v[48:49], v3 offset:22528\n"
"ds_read_b64_tr_b16 v[50:51], v3 offset:22784\n"
"v_mul_f32 v79, v79, v111\n"
"v_cvt_pk_bf16_f32 v94, v76, v77\n"
"v_mfma_f32_16x16x32_bf16 v[136:139], v[52:55], a[52:55], v[136:139]\n"
"s_waitcnt lgkmcnt(4)\n"
"buffer_load_dwordx4 a[76:79], v163, s[28:31], s44 offen offset:0 sc1\n"
"v_cvt_pk_bf16_f32 v95, v78, v79\n"
"v_mul_f32 v64, s80, v64\n"
"v_mfma_f32_16x16x32_bf16 v[148:151], v[56:59], a[48:51], v[148:151]\n"
"ds_read_b64_tr_b16 v[52:53], v6 offset:20480\n"
"ds_read_b64_tr_b16 v[54:55], v6 offset:20736\n"
"v_exp_f32 v64, v64\n"
"v_mfma_f32_16x16x32_bf16 v[152:155], v[56:59], a[52:55], v[152:155]\n"
"s_waitcnt lgkmcnt(4)\n"
"buffer_store_dwordx4 v[92:95], v10, s[28:31], s77 offen offset:0 sc1\n"
"v_mul_f32 v65, s80, v65\n"
"v_add_f32 v64, 1.0, v64\n"
"v_mfma_f32_16x16x32_bf16 v[124:127], v[44:47], a[64:67], v[124:127]\n"
"s_add_u32 s44, s65, 73728\n"
"ds_read_b64_tr_b16 v[56:57], v6 offset:22528\n"
"ds_read_b64_tr_b16 v[58:59], v6 offset:22784\n"
"v_exp_f32 v65, v65\n"
"v_mfma_f32_16x16x32_bf16 v[128:131], v[44:47], a[68:71], v[128:131]\n"
"s_waitcnt lgkmcnt(4)\n"
"buffer_load_dwordx4 a[88:91], v162, s[28:31], s44 offen offset:0 sc1\n"
"v_rcp_f32 v64, v64\n"
"v_mfma_f32_16x16x32_bf16 v[140:143], v[48:51], a[64:67], v[140:143]\n"
"ds_read_b64_tr_b16 v[44:45], v3 offset:24576\n"
"ds_read_b64_tr_b16 v[46:47], v3 offset:24832\n"
"v_add_f32 v65, 1.0, v65\n"
"v_mul_f32 v64, v64, v96\n"
"v_mfma_f32_16x16x32_bf16 v[144:147], v[48:51], a[68:71], v[144:147]\n"
"s_waitcnt lgkmcnt(4)\n"
"v_rcp_f32 v65, v65\n"
"v_mul_f32 v66, s80, v66\n"
"v_mul_f32 v67, s80, v67\n"
"v_mfma_f32_16x16x32_bf16 v[132:135], v[52:55], a[64:67], v[132:135]\n"
"ds_read_b64_tr_b16 v[48:49], v3 offset:26624\n"
"ds_read_b64_tr_b16 v[50:51], v3 offset:26880\n"
"v_exp_f32 v66, v66\n"
"v_mfma_f32_16x16x32_bf16 v[136:139], v[52:55], a[68:71], v[136:139]\n"
"s_waitcnt lgkmcnt(4)\n"
"buffer_load_dwordx4 a[92:95], v163, s[28:31], s44 offen offset:0 sc1\n"
"v_exp_f32 v67, v67\n"
"v_mfma_f32_16x16x32_bf16 v[148:151], v[56:59], a[64:67], v[148:151]\n"
"ds_read_b64_tr_b16 v[52:53], v6 offset:24576\n"
"ds_read_b64_tr_b16 v[54:55], v6 offset:24832\n"
"v_mul_f32 v65, v65, v97\n"
"v_add_f32 v66, 1.0, v66\n"
"v_mfma_f32_16x16x32_bf16 v[152:155], v[56:59], a[68:71], v[152:155]\n"
"s_waitcnt lgkmcnt(4)\n"
"v_rcp_f32 v66, v66\n"
"v_add_f32 v67, 1.0, v67\n"
"v_mul_f32 v80, s80, v80\n"
"v_mfma_f32_16x16x32_bf16 v[124:127], v[44:47], a[80:83], v[124:127]\n"
"s_add_u32 s44, s66, 0\n"
"ds_read_b64_tr_b16 v[56:57], v6 offset:26624\n"
"ds_read_b64_tr_b16 v[58:59], v6 offset:26880\n"
"v_rcp_f32 v67, v67\n"
"v_mfma_f32_16x16x32_bf16 v[128:131], v[44:47], a[84:87], v[128:131]\n"
"s_waitcnt lgkmcnt(4)\n"
"buffer_load_dwordx4 a[104:107], v162, s[28:31], s44 offen offset:0 sc1\n"
"v_exp_f32 v80, v80\n"
"v_mfma_f32_16x16x32_bf16 v[140:143], v[48:51], a[80:83], v[140:143]\n"
"ds_read_b64_tr_b16 v[44:45], v2 offset:24576\n"
"ds_read_b64_tr_b16 v[46:47], v2 offset:24832\n"
"v_mul_f32 v66, v66, v98\n"
"v_mul_f32 v67, v67, v99\n"
"v_mfma_f32_16x16x32_bf16 v[144:147], v[48:51], a[84:87], v[144:147]\n"
"s_waitcnt lgkmcnt(4)\n"
"v_mul_f32 v81, s80, v81\n"
"v_add_f32 v80, 1.0, v80\n"
"v_mul_f32 v82, s80, v82\n"
"v_mul_f32 v83, s80, v83\n"
"v_mfma_f32_16x16x32_bf16 v[132:135], v[52:55], a[80:83], v[132:135]\n"
"ds_read_b64_tr_b16 v[48:49], v2 offset:26624\n"
"ds_read_b64_tr_b16 v[50:51], v2 offset:26880\n"
"v_exp_f32 v81, v81\n"
"v_mfma_f32_16x16x32_bf16 v[136:139], v[52:55], a[84:87], v[136:139]\n"
"s_waitcnt lgkmcnt(4)\n"
"buffer_load_dwordx4 a[108:111], v163, s[28:31], s44 offen offset:0 sc1\n"
"v_rcp_f32 v80, v80\n"
"v_mfma_f32_16x16x32_bf16 v[148:151], v[56:59], a[80:83], v[148:151]\n"
"ds_read_b64_tr_b16 v[52:53], v5 offset:24576\n"
"ds_read_b64_tr_b16 v[54:55], v5 offset:24832\n"
"v_exp_f32 v82, v82\n"
"v_mfma_f32_16x16x32_bf16 v[152:155], v[56:59], a[84:87], v[152:155]\n"
"s_waitcnt lgkmcnt(4)\n"
"v_exp_f32 v83, v83\n"
"v_add_f32 v81, 1.0, v81\n"
"v_mul_f32 v80, v80, v112\n"
"v_mfma_f32_16x16x32_bf16 v[124:127], v[44:47], a[96:99], v[124:127]\n"
"s_add_u32 s44, s66, 73728\n"
"ds_read_b64_tr_b16 v[56:57], v5 offset:26624\n"
"ds_read_b64_tr_b16 v[58:59], v5 offset:26880\n"
"v_rcp_f32 v81, v81\n"
"v_mfma_f32_16x16x32_bf16 v[128:131], v[44:47], a[100:103], v[128:131]\n"
"s_waitcnt lgkmcnt(4)\n"
"buffer_load_dwordx4 a[120:123], v162, s[28:31], s44 offen offset:0 sc1\n"
"v_mul_f32 v81, v81, v113\n"
"v_add_f32 v82, 1.0, v82\n"
"v_mfma_f32_16x16x32_bf16 v[140:143], v[48:51], a[96:99], v[140:143]\n"
"ds_read_b64_tr_b16 v[44:45], v3 offset:0\n"
"ds_read_b64_tr_b16 v[46:47], v3 offset:256\n"
"v_rcp_f32 v82, v82\n"
"v_mfma_f32_16x16x32_bf16 v[144:147], v[48:51], a[100:103], v[144:147]\n"
"s_waitcnt lgkmcnt(4)\n"
"v_add_f32 v83, 1.0, v83\n"
"v_mul_f32 v82, v82, v114\n"
"v_cvt_pk_bf16_f32 v96, v64, v65\n"
"v_cvt_pk_bf16_f32 v97, v66, v67\n"
"v_mfma_f32_16x16x32_bf16 v[132:135], v[52:55], a[96:99], v[132:135]\n"
"ds_read_b64_tr_b16 v[48:49], v3 offset:2048\n"
"ds_read_b64_tr_b16 v[50:51], v3 offset:2304\n"
"v_rcp_f32 v83, v83\n"
"v_mfma_f32_16x16x32_bf16 v[136:139], v[52:55], a[100:103], v[136:139]\n"
"s_waitcnt lgkmcnt(4)\n"
"buffer_load_dwordx4 a[124:127], v163, s[28:31], s44 offen offset:0 sc1\n"
"v_mul_f32 v83, v83, v115\n"
"v_cvt_pk_bf16_f32 v98, v80, v81\n"
"v_mfma_f32_16x16x32_bf16 v[148:151], v[56:59], a[96:99], v[148:151]\n"
"ds_read_b64_tr_b16 v[52:53], v6 offset:0\n"
"ds_read_b64_tr_b16 v[54:55], v6 offset:256\n"
"v_cvt_pk_bf16_f32 v99, v82, v83\n"
"v_mul_f32 v68, s80, v68\n"
"v_mfma_f32_16x16x32_bf16 v[152:155], v[56:59], a[100:103], v[152:155]\n"
"s_waitcnt lgkmcnt(4)\n"
"buffer_store_dwordx4 v[96:99], v10, s[28:31], s77 offen offset:1024 sc1\n"
"v_exp_f32 v68, v68\n"
"v_mfma_f32_16x16x32_bf16 v[124:127], v[44:47], a[112:115], v[124:127]\n"
"s_add_u32 s44, s67, 0\n"
"ds_read_b64_tr_b16 v[56:57], v6 offset:2048\n"
"ds_read_b64_tr_b16 v[58:59], v6 offset:2304\n"
"v_mul_f32 v69, s80, v69\n"
"v_add_f32 v68, 1.0, v68\n"
"v_mfma_f32_16x16x32_bf16 v[128:131], v[44:47], a[116:119], v[128:131]\n"
"s_waitcnt lgkmcnt(4)\n"
"buffer_load_dwordx4 a[136:139], v162, s[28:31], s44 offen offset:0 sc1\n"
"v_exp_f32 v69, v69\n"
"v_mfma_f32_16x16x32_bf16 v[140:143], v[48:51], a[112:115], v[140:143]\n"
"ds_read_b64_tr_b16 v[44:45], v3 offset:4096\n"
"ds_read_b64_tr_b16 v[46:47], v3 offset:4352\n"
"v_rcp_f32 v68, v68\n"
"v_mfma_f32_16x16x32_bf16 v[144:147], v[48:51], a[116:119], v[144:147]\n"
"s_waitcnt lgkmcnt(4)\n"
"s_waitcnt vmcnt(13)\n"
"v_cmp_ne_u64 vcc, s[34:35], v[166:167]\n"
"s_nop 3\n"
"s_cbranch_vccz pF2B_ok_gated_mix_fused\n"
"s_mov_b32 s83, 0\n"
"pF2B_sp_gated_mix_fused:\n"
"buffer_load_dwordx2 v[166:167], v169, s[28:31], 0 offen offset:256 sc1\n"
"s_waitcnt vmcnt(0)\n"
"s_add_u32 s83, s83, 1\n"
"s_cmp_gt_u32 s83, 1048576\n"
"s_cbranch_scc1 pF2B_ok_gated_mix_fused\n"
"v_cmp_ne_u64 vcc, s[34:35], v[166:167]\n"
"s_nop 3\n"
"s_cbranch_vccnz pF2B_sp_gated_mix_fused\n"
"pF2B_ok_gated_mix_fused:\n"
"v_mfma_f32_16x16x32_bf16 v[132:135], v[52:55], a[112:115], v[132:135]\n"
"ds_read_b64_tr_b16 v[48:49], v3 offset:6144\n"
"ds_read_b64_tr_b16 v[50:51], v3 offset:6400\n"
"v_add_f32 v69, 1.0, v69\n"
"v_mul_f32 v68, v68, v100\n"
"v_mfma_f32_16x16x32_bf16 v[136:139], v[52:55], a[116:119], v[136:139]\n"
"s_waitcnt lgkmcnt(4)\n"
"buffer_load_dwordx4 a[140:143], v163, s[28:31], s44 offen offset:0 sc1\n"
"v_rcp_f32 v69, v69\n"
"v_mfma_f32_16x16x32_bf16 v[148:151], v[56:59], a[112:115], v[148:151]\n"
"ds_read_b64_tr_b16 v[52:53], v6 offset:4096\n"
"ds_read_b64_tr_b16 v[54:55], v6 offset:4352\n"
"v_mul_f32 v69, v69, v101\n"
"v_mul_f32 v70, s80, v70\n"
"v_mfma_f32_16x16x32_bf16 v[152:155], v[56:59], a[116:119], v[152:155]\n"
"s_waitcnt lgkmcnt(4)\n"
"v_exp_f32 v70, v70\n"
"v_mul_f32 v71, s80, v71\n"
"v_mul_f32 v84, s80, v84\n"
"v_mfma_f32_16x16x32_bf16 v[124:127], v[44:47], a[128:131], v[124:127]\n"
"s_add_u32 s44, s67, 73728\n"
"ds_read_b64_tr_b16 v[56:57], v6 offset:6144\n"
"ds_read_b64_tr_b16 v[58:59], v6 offset:6400\n"
"v_exp_f32 v71, v71\n"
"v_mfma_f32_16x16x32_bf16 v[128:131], v[44:47], a[132:135], v[128:131]\n"
"s_waitcnt lgkmcnt(4)\n"
"buffer_load_dwordx4 a[152:155], v162, s[28:31], s44 offen offset:0 sc1\n"
"v_exp_f32 v84, v84\n"
"v_mfma_f32_16x16x32_bf16 v[140:143], v[48:51], a[128:131], v[140:143]\n"
"ds_read_b64_tr_b16 v[44:45], v2 offset:45056\n"
"ds_read_b64_tr_b16 v[46:47], v2 offset:45312\n"
"v_add_f32 v70, 1.0, v70\n"
"v_add_f32 v71, 1.0, v71\n"
"v_mfma_f32_16x16x32_bf16 v[144:147], v[48:51], a[132:135], v[144:147]\n"
"s_waitcnt lgkmcnt(4)\n"
"v_rcp_f32 v70, v70\n"
"v_rcp_f32 v71, v71\n"
"v_mfma_f32_16x16x32_bf16 v[132:135], v[52:55], a[128:131], v[132:135]\n"
"ds_read_b64_tr_b16 v[48:49], v2 offset:47104\n"
"ds_read_b64_tr_b16 v[50:51], v2 offset:47360\n"
"v_mul_f32 v70, v70, v102\n"
"v_mul_f32 v71, v71, v103\n"
"v_mfma_f32_16x16x32_bf16 v[136:139], v[52:55], a[132:135], v[136:139]\n"
"s_waitcnt lgkmcnt(4)\n"
"buffer_load_dwordx4 a[156:159], v163, s[28:31], s44 offen offset:0 sc1\n"
"v_mul_f32 v85, s80, v85\n"
"v_add_f32 v84, 1.0, v84\n"
"v_mfma_f32_16x16x32_bf16 v[148:151], v[56:59], a[128:131], v[148:151]\n"
"ds_read_b64_tr_b16 v[52:53], v5 offset:45056\n"
"ds_read_b64_tr_b16 v[54:55], v5 offset:45312\n"
"v_exp_f32 v85, v85\n"
"v_mfma_f32_16x16x32_bf16 v[152:155], v[56:59], a[132:135], v[152:155]\n"
"s_waitcnt lgkmcnt(4)\n"
"v_rcp_f32 v84, v84\n"
"v_add_f32 v85, 1.0, v85\n"
"v_mul_f32 v86, s80, v86\n"
"v_mfma_f32_16x16x32_bf16 v[124:127], v[44:47], a[144:147], v[124:127]\n"
"ds_read_b64_tr_b16 v[56:57], v5 offset:47104\n"
"ds_read_b64_tr_b16 v[58:59], v5 offset:47360\n"
"v_rcp_f32 v85, v85\n"
"v_mfma_f32_16x16x32_bf16 v[128:131], v[44:47], a[148:151], v[128:131]\n"
"s_waitcnt lgkmcnt(4)\n"
"s_barrier\n"
"v_exp_f32 v86, v86\n"
"v_mul_f32 v84, v84, v116\n"
"v_mfma_f32_16x16x32_bf16 v[140:143], v[48:51], a[144:147], v[140:143]\n"
"s_add_u32 s44, s64, 4096\n"
"ds_read_b64_tr_b16 v[44:45], v2 offset:49152\n"
"ds_read_b64_tr_b16 v[46:47], v2 offset:49408\n"
"v_mul_f32 v85, v85, v117\n"
"v_mul_f32 v87, s80, v87\n"
"v_mfma_f32_16x16x32_bf16 v[144:147], v[48:51], a[148:151], v[144:147]\n"
"s_waitcnt lgkmcnt(4)\n"
"buffer_load_dwordx4 a[40:43], v162, s[28:31], s44 offen offset:0 sc1\n"
"v_exp_f32 v87, v87\n"
"v_mfma_f32_16x16x32_bf16 v[132:135], v[52:55], a[144:147], v[132:135]\n"
"ds_read_b64_tr_b16 v[48:49], v2 offset:51200\n"
"ds_read_b64_tr_b16 v[50:51], v2 offset:51456\n"
"v_add_f32 v86, 1.0, v86\n"
"v_add_f32 v87, 1.0, v87\n"
"v_mfma_f32_16x16x32_bf16 v[136:139], v[52:55], a[148:151], v[136:139]\n"
"s_waitcnt lgkmcnt(4)\n"
"v_rcp_f32 v86, v86\n"
"v_rcp_f32 v87, v87\n"
"v_mfma_f32_16x16x32_bf16 v[148:151], v[56:59], a[144:147], v[148:151]\n"
"ds_read_b64_tr_b16 v[52:53], v5 offset:49152\n"
"ds_read_b64_tr_b16 v[54:55], v5 offset:49408\n"
"v_mul_f32 v86, v86, v118\n"
"v_mfma_f32_16x16x32_bf16 v[152:155], v[56:59], a[148:151], v[152:155]\n"
"s_waitcnt lgkmcnt(4)\n"
"buffer_load_dwordx4 a[44:47], v163, s[28:31], s44 offen offset:0 sc1\n"
"v_mul_f32 v87, v87, v119\n"
"v_mfma_f32_16x16x32_bf16 v[124:127], v[44:47], a[160:163], v[124:127]\n"
"ds_read_b64_tr_b16 v[56:57], v5 offset:51200\n"
"ds_read_b64_tr_b16 v[58:59], v5 offset:51456\n"
"v_cvt_pk_bf16_f32 v100, v68, v69\n"
"v_mfma_f32_16x16x32_bf16 v[128:131], v[44:47], a[164:167], v[128:131]\n"
"s_waitcnt lgkmcnt(4)\n"
"buffer_store_dwordx2 v[164:165], v168, s[28:31], s82 offen offset:384 sc1\n"
"v_cvt_pk_bf16_f32 v101, v70, v71\n"
"v_mfma_f32_16x16x32_bf16 v[140:143], v[48:51], a[160:163], v[140:143]\n"
"ds_read_b64_tr_b16 v[44:45], v2 offset:61440\n"
"ds_read_b64_tr_b16 v[46:47], v2 offset:61696\n"
"v_cvt_pk_bf16_f32 v102, v84, v85\n"
"v_mfma_f32_16x16x32_bf16 v[144:147], v[48:51], a[164:167], v[144:147]\n"
"s_waitcnt lgkmcnt(4)\n"
"s_add_u32 s84, s12, 0\n"
"v_cvt_pk_bf16_f32 v103, v86, v87\n"
"v_mul_f32 v72, s80, v72\n"
"v_mfma_f32_16x16x32_bf16 v[132:135], v[52:55], a[160:163], v[132:135]\n"
"s_add_u32 m0, s53, 151552\n"
"ds_read_b64_tr_b16 v[48:49], v2 offset:63488\n"
"ds_read_b64_tr_b16 v[50:51], v2 offset:63744\n"
"v_mul_f32 v73, s80, v73\n"
"v_mfma_f32_16x16x32_bf16 v[136:139], v[52:55], a[164:167], v[136:139]\n"
"s_waitcnt lgkmcnt(4)\n"
"buffer_load_dwordx4 v171, s[36:39], s84 offen offset:0 lds\n"
"v_mul_f32 v74, s80, v74\n"
"v_mfma_f32_16x16x32_bf16 v[148:151], v[56:59], a[160:163], v[148:151]\n"
"ds_read_b64_tr_b16 v[52:53], v5 offset:61440\n"
"ds_read_b64_tr_b16 v[54:55], v5 offset:61696\n"
"v_mul_f32 v75, s80, v75\n"
"v_mfma_f32_16x16x32_bf16 v[152:155], v[56:59], a[164:167], v[152:155]\n"
"s_waitcnt lgkmcnt(4)\n"
"s_add_u32 s84, s13, 0\n"
"buffer_store_dwordx4 v[100:103], v10, s[28:31], s78 offen offset:0 sc1\n"
"v_mfma_f32_16x16x32_bf16 v[124:127], v[44:47], a[176:179], v[124:127]\n"
"s_add_u32 m0, s53, 155648\n"
"ds_read_b64_tr_b16 v[56:57], v5 offset:63488\n"
"ds_read_b64_tr_b16 v[58:59], v5 offset:63744\n"
"v_mul_f32 v88, s80, v88\n"
"v_mfma_f32_16x16x32_bf16 v[128:131], v[44:47], a[180:183], v[128:131]\n"
"s_waitcnt lgkmcnt(4)\n"
"buffer_load_dwordx4 v171, s[36:39], s84 offen offset:0 lds\n"
"v_mul_f32 v89, s80, v89\n"
"v_mfma_f32_16x16x32_bf16 v[140:143], v[48:51], a[176:179], v[140:143]\n"
"ds_read_b64_tr_b16 v[44:45], v2 offset:40960\n"
"ds_read_b64_tr_b16 v[46:47], v2 offset:41216\n"
"v_mul_f32 v90, s80, v90\n"
"v_mfma_f32_16x16x32_bf16 v[144:147], v[48:51], a[180:183], v[144:147]\n"
"s_waitcnt lgkmcnt(4)\n"
"s_add_u32 s84, s14, 0\n"
"v_exp_f32 v72, v72\n"
"v_mfma_f32_16x16x32_bf16 v[132:135], v[52:55], a[176:179], v[132:135]\n"
"s_add_u32 m0, s54, 82944\n"
"ds_read_b64_tr_b16 v[48:49], v2 offset:43008\n"
"ds_read_b64_tr_b16 v[50:51], v2 offset:43264\n"
"v_add_f32 v72, 1.0, v72\n"
"v_mfma_f32_16x16x32_bf16 v[136:139], v[52:55], a[180:183], v[136:139]\n"
"s_waitcnt lgkmcnt(4)\n"
"buffer_load_dwordx4 v158, s[36:39], s84 offen offset:0 lds\n"
"v_mul_f32 v91, s80, v91\n"
"v_mfma_f32_16x16x32_bf16 v[148:151], v[56:59], a[176:179], v[148:151]\n"
"ds_read_b64_tr_b16 v[52:53], v5 offset:40960\n"
"ds_read_b64_tr_b16 v[54:55], v5 offset:41216\n"
"v_mfma_f32_16x16x32_bf16 v[152:155], v[56:59], a[180:183], v[152:155]\n"
"s_waitcnt lgkmcnt(4)\n"
"v_exp_f32 v73, v73\n"
"v_mfma_f32_16x16x32_bf16 v[124:127], v[44:47], a[192:195], v[124:127]\n"
"s_add_u32 m0, s54, 84032\n"
"ds_read_b64_tr_b16 v[56:57], v5 offset:43008\n"
"ds_read_b64_tr_b16 v[58:59], v5 offset:43264\n"
"v_add_f32 v73, 1.0, v73\n"
"v_mfma_f32_16x16x32_bf16 v[128:131], v[44:47], a[196:199], v[128:131]\n"
"s_waitcnt lgkmcnt(4)\n"
"buffer_load_dwordx4 v159, s[36:39], s84 offen offset:0 lds\n"
"v_mfma_f32_16x16x32_bf16 v[140:143], v[48:51], a[192:195], v[140:143]\n"
"ds_read_b64_tr_b16 v[44:45], v2 offset:53248\n"
"ds_read_b64_tr_b16 v[46:47], v2 offset:53504\n"
"v_mfma_f32_16x16x32_bf16 v[144:147], v[48:51], a[196:199], v[144:147]\n"
"s_waitcnt lgkmcnt(4)\n"
"s_add_u32 s84, s14, 0\n"
"v_rcp_f32 v72, v72\n"
"v_mfma_f32_16x16x32_bf16 v[132:135], v[52:55], a[192:195], v[132:135]\n"
"s_add_u32 m0, s53, 159744\n"
"ds_read_b64_tr_b16 v[48:49], v2 offset:55296\n"
"ds_read_b64_tr_b16 v[50:51], v2 offset:55552\n"
"v_mul_f32 v72, v72, v104\n"
"v_mfma_f32_16x16x32_bf16 v[136:139], v[52:55], a[196:199], v[136:139]\n"
"s_waitcnt lgkmcnt(4)\n"
"buffer_load_dwordx4 v171, s[36:39], s84 offen offset:0 lds\n"
"v_mfma_f32_16x16x32_bf16 v[148:151], v[56:59], a[192:195], v[148:151]\n"
"ds_read_b64_tr_b16 v[52:53], v5 offset:53248\n"
"ds_read_b64_tr_b16 v[54:55], v5 offset:53504\n"
"v_mfma_f32_16x16x32_bf16 v[152:155], v[56:59], a[196:199], v[152:155]\n"
"s_waitcnt lgkmcnt(4)\n"
"v_rcp_f32 v73, v73\n"
"v_mfma_f32_16x16x32_bf16 v[124:127], v[44:47], a[208:211], v[124:127]\n"
"s_add_u32 s44, s64, 77824\n"
"ds_read_b64_tr_b16 v[56:57], v5 offset:55296\n"
"ds_read_b64_tr_b16 v[58:59], v5 offset:55552\n"
"v_mul_f32 v73, v73, v105\n"
"v_mfma_f32_16x16x32_bf16 v[128:131], v[44:47], a[212:215], v[128:131]\n"
"s_waitcnt lgkmcnt(4)\n"
"buffer_load_dwordx4 a[56:59], v162, s[28:31], s44 offen offset:0 sc1\n"
"v_cvt_pk_bf16_f32 v104, v72, v73\n"
"v_mfma_f32_16x16x32_bf16 v[140:143], v[48:51], a[208:211], v[140:143]\n"
"ds_read_b64_tr_b16 v[44:45], v2 offset:57344\n"
"ds_read_b64_tr_b16 v[46:47], v2 offset:57600\n"
"v_mfma_f32_16x16x32_bf16 v[144:147], v[48:51], a[212:215], v[144:147]\n"
"s_waitcnt lgkmcnt(4)\n"
"v_exp_f32 v74, v74\n"
"v_mfma_f32_16x16x32_bf16 v[132:135], v[52:55], a[208:211], v[132:135]\n"
"ds_read_b64_tr_b16 v[48:49], v2 offset:59392\n"
"ds_read_b64_tr_b16 v[50:51], v2 offset:59648\n"
"v_add_f32 v74, 1.0, v74\n"
"v_mfma_f32_16x16x32_bf16 v[136:139], v[52:55], a[212:215], v[136:139]\n"
"s_waitcnt lgkmcnt(4)\n"
"buffer_load_dwordx4 a[60:63], v163, s[28:31], s44 offen offset:0 sc1\n"
"v_mfma_f32_16x16x32_bf16 v[148:151], v[56:59], a[208:211], v[148:151]\n"
"ds_read_b64_tr_b16 v[52:53], v5 offset:57344\n"
"ds_read_b64_tr_b16 v[54:55], v5 offset:57600\n"
"v_mfma_f32_16x16x32_bf16 v[152:155], v[56:59], a[212:215], v[152:155]\n"
"s_waitcnt lgkmcnt(4)\n"
"v_exp_f32 v75, v75\n"
"v_mfma_f32_16x16x32_bf16 v[124:127], v[44:47], a[224:227], v[124:127]\n"
"s_add_u32 s44, s65, 4096\n"
"ds_read_b64_tr_b16 v[56:57], v5 offset:59392\n"
"ds_read_b64_tr_b16 v[58:59], v5 offset:59648\n"
"v_add_f32 v75, 1.0, v75\n"
"v_mfma_f32_16x16x32_bf16 v[128:131], v[44:47], a[228:231], v[128:131]\n"
"s_waitcnt lgkmcnt(4)\n"
"buffer_load_dwordx4 a[200:203], v162, s[28:31], s44 offen offset:0 sc1\n"
"v_mfma_f32_16x16x32_bf16 v[140:143], v[48:51], a[224:227], v[140:143]\n"
"ds_read_b64_tr_b16 v[44:45], v2 offset:28672\n"
"ds_read_b64_tr_b16 v[46:47], v2 offset:28928\n"
"v_mfma_f32_16x16x32_bf16 v[144:147], v[48:51], a[228:231], v[144:147]\n"
"s_waitcnt lgkmcnt(4)\n"
"v_rcp_f32 v74, v74\n"
"v_mfma_f32_16x16x32_bf16 v[132:135], v[52:55], a[224:227], v[132:135]\n"
"ds_read_b64_tr_b16 v[48:49], v2 offset:30720\n"
"ds_read_b64_tr_b16 v[50:51], v2 offset:30976\n"
"v_mul_f32 v74, v74, v106\n"
"v_mfma_f32_16x16x32_bf16 v[136:139], v[52:55], a[228:231], v[136:139]\n"
"s_waitcnt lgkmcnt(4)\n"
"buffer_load_dwordx4 a[204:207], v163, s[28:31], s44 offen offset:0 sc1\n"
"v_mfma_f32_16x16x32_bf16 v[148:151], v[56:59], a[224:227], v[148:151]\n"
"ds_read_b64_tr_b16 v[52:53], v5 offset:28672\n"
"ds_read_b64_tr_b16 v[54:55], v5 offset:28928\n"
"v_mfma_f32_16x16x32_bf16 v[152:155], v[56:59], a[228:231], v[152:155]\n"
"s_waitcnt lgkmcnt(4)\n"
"v_rcp_f32 v75, v75\n"
"v_mfma_f32_16x16x32_bf16 v[124:127], v[44:47], a[240:243], v[124:127]\n"
"ds_read_b64_tr_b16 v[56:57], v5 offset:30720\n"
"ds_read_b64_tr_b16 v[58:59], v5 offset:30976\n"
"v_mul_f32 v75, v75, v107\n"
"v_mfma_f32_16x16x32_bf16 v[128:131], v[44:47], a[244:247], v[128:131]\n"
"s_waitcnt lgkmcnt(4)\n"
"s_barrier\n"
"v_exp_f32 v88, v88\n"
"v_mfma_f32_16x16x32_bf16 v[140:143], v[48:51], a[240:243], v[140:143]\n"
"ds_read_b64_tr_b16 v[44:45], v2 offset:32768\n"
"ds_read_b64_tr_b16 v[46:47], v2 offset:33024\n"
"v_add_f32 v88, 1.0, v88\n"
"v_mfma_f32_16x16x32_bf16 v[144:147], v[48:51], a[244:247], v[144:147]\n"
"s_waitcnt lgkmcnt(4)\n"
"ds_read_b64 a[96:97], v172 offset:0\n"
"ds_read_b64 a[98:99], v172 offset:8\n"
"v_cvt_pk_bf16_f32 v105, v74, v75\n"
"v_mfma_f32_16x16x32_bf16 v[132:135], v[52:55], a[240:243], v[132:135]\n"
"ds_read_b64_tr_b16 v[48:49], v2 offset:34816\n"
"ds_read_b64_tr_b16 v[50:51], v2 offset:35072\n"
"v_mfma_f32_16x16x32_bf16 v[136:139], v[52:55], a[244:247], v[136:139]\n"
"s_waitcnt lgkmcnt(6)\n"
"ds_read_b128 a[100:103], v173 offset:16640\n"
"ds_read_b128 a[112:115], v173 offset:17152\n"
"v_mfma_f32_16x16x32_bf16 v[148:151], v[56:59], a[240:243], v[148:151]\n"
"ds_read_b64_tr_b16 v[52:53], v5 offset:32768\n"
"ds_read_b64_tr_b16 v[54:55], v5 offset:33024\n"
"v_mfma_f32_16x16x32_bf16 v[152:155], v[56:59], a[244:247], v[152:155]\n"
"s_waitcnt lgkmcnt(8)\n"
"ds_read_b128 a[116:119], v173 offset:17664\n"
"ds_read_b128 a[128:131], v173 offset:18176\n"
"v_mfma_f32_16x16x32_bf16 v[124:127], v[44:47], v[12:15], v[124:127]\n"
"ds_read_b64_tr_b16 v[56:57], v5 offset:34816\n"
"ds_read_b64_tr_b16 v[58:59], v5 offset:35072\n"
"v_mfma_f32_16x16x32_bf16 v[128:131], v[44:47], v[16:19], v[128:131]\n"
"s_waitcnt lgkmcnt(8)\n"
"ds_read_b128 a[132:135], v173 offset:18688\n"
"ds_read_b128 a[144:147], v173 offset:19200\n"
"v_mfma_f32_16x16x32_bf16 v[140:143], v[48:51], v[12:15], v[140:143]\n"
"ds_read_b64_tr_b16 v[44:45], v2 offset:36864\n"
"ds_read_b64_tr_b16 v[46:47], v2 offset:37120\n"
"v_mfma_f32_16x16x32_bf16 v[144:147], v[48:51], v[16:19], v[144:147]\n"
"s_waitcnt lgkmcnt(8)\n"
"ds_read_b128 a[148:151], v173 offset:19712\n"
"ds_read_b128 a[160:163], v173 offset:20224\n"
"v_mfma_f32_16x16x32_bf16 v[132:135], v[52:55], v[12:15], v[132:135]\n"
"ds_read_b64_tr_b16 v[48:49], v2 offset:38912\n"
"ds_read_b64_tr_b16 v[50:51], v2 offset:39168\n"
"v_mfma_f32_16x16x32_bf16 v[136:139], v[52:55], v[16:19], v[136:139]\n"
"s_waitcnt lgkmcnt(8)\n"
"ds_read_b64 a[164:165], v172 offset:512\n"
"ds_read_b64 a[166:167], v172 offset:520\n"
"v_mfma_f32_16x16x32_bf16 v[148:151], v[56:59], v[12:15], v[148:151]\n"
"ds_read_b64_tr_b16 v[52:53], v5 offset:36864\n"
"ds_read_b64_tr_b16 v[54:55], v5 offset:37120\n"
"v_mfma_f32_16x16x32_bf16 v[152:155], v[56:59], v[16:19], v[152:155]\n"
"s_waitcnt lgkmcnt(8)\n"
"ds_read_b64 a[176:177], v172 offset:1024\n"
"ds_read_b64 a[178:179], v172 offset:1032\n"
"v_mfma_f32_16x16x32_bf16 v[124:127], v[44:47], v[28:31], v[124:127]\n"
"s_add_u32 s44, s65, 77824\n"
"ds_read_b64_tr_b16 v[56:57], v5 offset:38912\n"
"ds_read_b64_tr_b16 v[58:59], v5 offset:39168\n"
"v_mfma_f32_16x16x32_bf16 v[128:131], v[44:47], v[32:35], v[128:131]\n"
"s_waitcnt lgkmcnt(8)\n"
"buffer_load_dwordx4 a[216:219], v162, s[28:31], s44 offen offset:0 sc1\n"
"v_mfma_f32_16x16x32_bf16 v[140:143], v[48:51], v[28:31], v[140:143]\n"
"v_exp_f32 v89, v89\n"
"v_mfma_f32_16x16x32_bf16 v[144:147], v[48:51], v[32:35], v[144:147]\n"
"s_waitcnt lgkmcnt(4)\n"
"v_rcp_f32 v88, v88\n"
"v_add_f32 v89, 1.0, v89\n"
"v_mfma_f32_16x16x32_bf16 v[132:135], v[52:55], v[28:31], v[132:135]\n"
"buffer_load_dwordx4 a[220:223], v163, s[28:31], s44 offen offset:0 sc1\n"
"v_mul_f32 v88, v88, v120\n"
"v_mfma_f32_16x16x32_bf16 v[136:139], v[52:55], v[32:35], v[136:139]\n"
"s_waitcnt lgkmcnt(0)\n"
"ds_read_b64 a[180:181], v172 offset:1536\n"
"ds_read_b64 a[182:183], v172 offset:1544\n"
"v_mfma_f32_16x16x32_bf16 v[148:151], v[56:59], v[28:31], v[148:151]\n"
"s_add_u32 s44, s66, 4096\n"
"v_rcp_f32 v89, v89\n"
"v_mfma_f32_16x16x32_bf16 v[152:155], v[56:59], v[32:35], v[152:155]\n"
"buffer_load_dwordx4 a[232:235], v162, s[28:31], s44 offen offset:0 sc1\n"
"v_mul_f32 v89, v89, v121\n"
"v_mfma_f32_16x16x32_bf16 v[216:219], a[96:99], a[100:103], 0\n"
"v_exp_f32 v90, v90\n"
"v_exp_f32 v91, v91\n"
"v_mfma_f32_16x16x32_bf16 v[212:215], a[96:99], a[112:115], 0\n"
"v_add_f32 v90, 1.0, v90\n"
"v_add_f32 v91, 1.0, v91\n"
"v_cvt_pk_bf16_f32 v106, v88, v89\n"
"v_mfma_f32_16x16x32_bf16 v[208:211], a[96:99], a[116:119], 0\n"
"ds_read_b64 a[192:193], v172 offset:2048\n"
"ds_read_b64 a[194:195], v172 offset:2056\n"
"v_rcp_f32 v90, v90\n"
"v_mfma_f32_16x16x32_bf16 v[204:207], a[96:99], a[128:131], 0\n"
"buffer_load_dwordx4 a[236:239], v163, s[28:31], s44 offen offset:0 sc1\n"
"v_rcp_f32 v91, v91\n"
"v_mfma_f32_16x16x32_bf16 v[200:203], a[96:99], a[132:135], 0\n"
"v_mul_f32 v90, v90, v122\n"
"v_mul_f32 v91, v91, v123\n"
"v_mul_f32 v124, s80, v124\n"
"v_mul_f32 v125, s80, v125\n"
"v_mfma_f32_16x16x32_bf16 v[196:199], a[96:99], a[144:147], 0\n"
"s_add_u32 s44, s66, 77824\n"
"v_exp_f32 v124, v124\n"
"v_cvt_pk_bf16_f32 v107, v90, v91\n"
"v_mfma_f32_16x16x32_bf16 v[192:195], a[96:99], a[148:151], 0\n"
"ds_read_b64 a[196:197], v172 offset:2560\n"
"ds_read_b64 a[198:199], v172 offset:2568\n"
"v_exp_f32 v125, v125\n"
"v_mfma_f32_16x16x32_bf16 v[188:191], a[96:99], a[160:163], 0\n"
"buffer_load_dwordx4 a[248:251], v162, s[28:31], s44 offen offset:0 sc1\n"
"v_add_f32 v124, 1.0, v124\n"
"v_add_f32 v125, 1.0, v125\n"
"v_mfma_f32_16x16x32_bf16 v[216:219], a[164:167], a[112:115], v[216:219]\n"
"buffer_store_dwordx4 v[104:107], v10, s[28:31], s78 offen offset:1024 sc1\n"
"v_rcp_f32 v124, v124\n"
"v_mfma_f32_16x16x32_bf16 v[212:215], a[164:167], a[116:119], v[212:215]\n"
"ds_read_b64 a[208:209], v172 offset:3072\n"
"ds_read_b64 a[210:211], v172 offset:3080\n"
"v_rcp_f32 v125, v125\n"
"v_mfma_f32_16x16x32_bf16 v[208:211], a[164:167], a[128:131], v[208:211]\n"
"buffer_load_dwordx4 a[252:255], v163, s[28:31], s44 offen offset:0 sc1\n"
"v_mul_f32 v124, v124, v140\n"
"v_mul_f32 v125, v125, v141\n"
"v_mfma_f32_16x16x32_bf16 v[204:207], a[164:167], a[132:135], v[204:207]\n"
"ds_read_b64 a[212:213], v172 offset:3584\n"
"ds_read_b64 a[214:215], v172 offset:3592\n"
"v_mul_f32 v126, s80, v126\n"
"v_mul_f32 v127, s80, v127\n"
"v_mfma_f32_16x16x32_bf16 v[200:203], a[164:167], a[144:147], v[200:203]\n"
"ds_read_b64 a[224:225], v172 offset:64\n"
"ds_read_b64 a[226:227], v172 offset:72\n"
"v_exp_f32 v126, v126\n"
"v_mfma_f32_16x16x32_bf16 v[196:199], a[164:167], a[148:151], v[196:199]\n"
"ds_read_b128 a[228:231], v173 offset:16704\n"
"ds_read_b128 a[240:243], v173 offset:17216\n"
"v_exp_f32 v127, v127\n"
"v_mfma_f32_16x16x32_bf16 v[192:195], a[164:167], a[160:163], v[192:195]\n"
"ds_read_b128 a[244:247], v173 offset:17728\n"
"v_add_f32 v126, 1.0, v126\n"
"v_add_f32 v127, 1.0, v127\n"
"v_mul_f32 v132, s80, v132\n"
"v_mfma_f32_16x16x32_bf16 v[216:219], a[176:179], a[116:119], v[216:219]\n"
"ds_read_b128 v[12:15], v173 offset:18240\n"
"v_rcp_f32 v126, v126\n"
"v_mul_f32 v133, s80, v133\n"
"v_mfma_f32_16x16x32_bf16 v[212:215], a[176:179], a[128:131], v[212:215]\n"
"ds_read_b128 v[16:19], v173 offset:18752\n"
"v_rcp_f32 v127, v127\n"
"v_mul_f32 v126, v126, v142\n"
"v_mfma_f32_16x16x32_bf16 v[208:211], a[176:179], a[132:135], v[208:211]\n"
"ds_read_b128 a[100:103], v173 offset:19264\n"
"v_exp_f32 v132, v132\n"
"v_mul_f32 v127, v127, v143\n"
"v_mfma_f32_16x16x32_bf16 v[204:207], a[176:179], a[144:147], v[204:207]\n"
"ds_read_b128 a[96:99], v173 offset:19776\n"
"v_exp_f32 v133, v133\n"
"v_add_f32 v132, 1.0, v132\n"
"v_mfma_f32_16x16x32_bf16 v[200:203], a[176:179], a[148:151], v[200:203]\n"
"ds_read_b128 a[112:115], v173 offset:20288\n"
"v_rcp_f32 v132, v132\n"
"v_add_f32 v133, 1.0, v133\n"
"v_mfma_f32_16x16x32_bf16 v[196:199], a[176:179], a[160:163], v[196:199]\n"
"s_waitcnt lgkmcnt(15)\n"
"ds_read_b64 a[164:165], v172 offset:576\n"
"ds_read_b64 a[166:167], v172 offset:584\n"
"v_rcp_f32 v133, v133\n"
"v_mfma_f32_16x16x32_bf16 v[216:219], a[180:183], a[128:131], v[216:219]\n"
"buffer_load_dwordx2 v[166:167], v169, s[28:31], 0 offen offset:384 sc1\n"
"v_mul_f32 v132, v132, v148\n"
"v_mul_f32 v133, v133, v149\n"
"v_mfma_f32_16x16x32_bf16 v[212:215], a[180:183], a[132:135], v[212:215]\n"
"v_mul_f32 v134, s80, v134\n"
"v_mul_f32 v135, s80, v135\n"
"v_cvt_pk_bf16_f32 v140, v124, v125\n"
"v_cvt_pk_bf16_f32 v141, v126, v127\n"
"v_mfma_f32_16x16x32_bf16 v[208:211], a[180:183], a[144:147], v[208:211]\n"
"v_exp_f32 v134, v134\n"
"v_exp_f32 v135, v135\n"
"v_mfma_f32_16x16x32_bf16 v[204:207], a[180:183], a[148:151], v[204:207]\n"
"s_add_u32 s44, s67, 4096\n"
"buffer_load_dwordx4 v[20:23], v162, s[28:31], s44 offen offset:0 sc1\n"
"v_add_f32 v134, 1.0, v134\n"
"v_add_f32 v135, 1.0, v135\n"
"v_mfma_f32_16x16x32_bf16 v[200:203], a[180:183], a[160:163], v[200:203]\n"
"s_waitcnt lgkmcnt(15)\n"
"v_rcp_f32 v134, v134\n"
"v_rcp_f32 v135, v135\n"
"v_mfma_f32_16x16x32_bf16 v[216:219], a[192:195], a[132:135], v[216:219]\n"
"v_mul_f32 v134, v134, v150\n"
"v_mul_f32 v135, v135, v151\n"
"v_cvt_pk_bf16_f32 v142, v132, v133\n"
"v_mul_f32 v128, s80, v128\n"
"v_mfma_f32_16x16x32_bf16 v[212:215], a[192:195], a[144:147], v[212:215]\n"
"ds_read_b64 a[116:117], v172 offset:1088\n"
"ds_read_b64 a[118:119], v172 offset:1096\n"
"v_exp_f32 v128, v128\n"
"v_mfma_f32_16x16x32_bf16 v[208:211], a[192:195], a[148:151], v[208:211]\n"
"buffer_load_dwordx4 v[24:27], v163, s[28:31], s44 offen offset:0 sc1\n"
"v_cvt_pk_bf16_f32 v143, v134, v135\n"
"v_mul_f32 v129, s80, v129\n"
"v_mfma_f32_16x16x32_bf16 v[204:207], a[192:195], a[160:163], v[204:207]\n"
"s_waitcnt lgkmcnt(15)\n"
"buffer_store_dwordx4 v[140:143], v10, s[28:31], s79 offen offset:0 sc1\n"
"v_exp_f32 v129, v129\n"
"v_mfma_f32_16x16x32_bf16 v[216:219], a[196:199], a[144:147], v[216:219]\n"
"v_add_f32 v128, 1.0, v128\n"
"v_add_f32 v129, 1.0, v129\n"
"v_mul_f32 v130, s80, v130\n"
"v_mul_f32 v131, s80, v131\n"
"v_mfma_f32_16x16x32_bf16 v[212:215], a[196:199], a[148:151], v[212:215]\n"
"s_add_u32 s44, s67, 77824\n"
"buffer_load_dwordx4 v[36:39], v162, s[28:31], s44 offen offset:0 sc1\n"
"v_rcp_f32 v128, v128\n"
"v_mfma_f32_16x16x32_bf16 v[208:211], a[196:199], a[160:163], v[208:211]\n"
"s_waitcnt lgkmcnt(15)\n"
"v_rcp_f32 v129, v129\n"
"v_exp_f32 v130, v130\n"
"v_mfma_f32_16x16x32_bf16 v[216:219], a[208:211], a[148:151], v[216:219]\n"
"ds_read_b64 a[176:177], v172 offset:1600\n"
"ds_read_b64 a[178:179], v172 offset:1608\n"
"v_exp_f32 v131, v131\n"
"v_mfma_f32_16x16x32_bf16 v[212:215], a[208:211], a[160:163], v[212:215]\n"
"s_waitcnt lgkmcnt(15)\n"
"buffer_load_dwordx4 v[40:43], v163, s[28:31], s44 offen offset:0 sc1\n"
"v_mul_f32 v128, v128, v144\n"
"v_mul_f32 v129, v129, v145\n"
"v_mfma_f32_16x16x32_bf16 v[216:219], a[212:215], a[160:163], v[216:219]\n"
"s_waitcnt lgkmcnt(13)\n"
"v_add_f32 v130, 1.0, v130\n"
"v_add_f32 v131, 1.0, v131\n"
"v_mul_f32 v136, s80, v136\n"
"v_mul_f32 v137, s80, v137\n"
"v_mfma_f32_16x16x32_bf16 v[216:219], a[224:227], a[228:231], v[216:219]\n"
"s_waitcnt lgkmcnt(12)\n"
"v_rcp_f32 v130, v130\n"
"v_rcp_f32 v131, v131\n"
"v_mfma_f32_16x16x32_bf16 v[212:215], a[224:227], a[240:243], v[212:215]\n"
"s_waitcnt lgkmcnt(11)\n"
"v_exp_f32 v136, v136\n"
"v_exp_f32 v137, v137\n"
"v_mfma_f32_16x16x32_bf16 v[208:211], a[224:227], a[244:247], v[208:211]\n"
"s_waitcnt lgkmcnt(10)\n"
"ds_read_b64 a[128:129], v172 offset:2112\n"
"ds_read_b64 a[130:131], v172 offset:2120\n"
"v_mul_f32 v130, v130, v146\n"
"v_mul_f32 v131, v131, v147\n"
"v_mfma_f32_16x16x32_bf16 v[204:207], a[224:227], v[12:15], v[204:207]\n"
"s_waitcnt lgkmcnt(11)\n"
"v_add_f32 v136, 1.0, v136\n"
"v_add_f32 v137, 1.0, v137\n"
"v_mul_f32 v138, s80, v138\n"
"v_mul_f32 v139, s80, v139\n"
"v_mfma_f32_16x16x32_bf16 v[200:203], a[224:227], v[16:19], v[200:203]\n"
"s_waitcnt lgkmcnt(10)\n"
"v_rcp_f32 v136, v136\n"
"v_rcp_f32 v137, v137\n"
"v_mfma_f32_16x16x32_bf16 v[196:199], a[224:227], a[100:103], v[196:199]\n"
"s_waitcnt lgkmcnt(9)\n"
"v_exp_f32 v138, v138\n"
"v_exp_f32 v139, v139\n"
"v_mfma_f32_16x16x32_bf16 v[192:195], a[224:227], a[96:99], v[192:195]\n"
"s_waitcnt lgkmcnt(8)\n"
"ds_read_b64 a[180:181], v172 offset:2624\n"
"ds_read_b64 a[182:183], v172 offset:2632\n"
"v_mul_f32 v136, v136, v152\n"
"v_mul_f32 v137, v137, v153\n"
"v_mfma_f32_16x16x32_bf16 v[188:191], a[224:227], a[112:115], v[188:191]\n"
"s_waitcnt lgkmcnt(8)\n"
"v_add_f32 v138, 1.0, v138\n"
"v_add_f32 v139, 1.0, v139\n"
"v_cvt_pk_bf16_f32 v144, v128, v129\n"
"v_cvt_pk_bf16_f32 v145, v130, v131\n"
"v_mfma_f32_16x16x32_bf16 v[216:219], a[164:167], a[240:243], v[216:219]\n"
"v_rcp_f32 v138, v138\n"
"v_rcp_f32 v139, v139\n"
"v_mfma_f32_16x16x32_bf16 v[212:215], a[164:167], a[244:247], v[212:215]\n"
"ds_read_b64 a[132:133], v172 offset:3136\n"
"ds_read_b64 a[134:135], v172 offset:3144\n"
"v_mul_f32 v138, v138, v154\n"
"v_mul_f32 v139, v139, v155\n"
"v_mfma_f32_16x16x32_bf16 v[208:211], a[164:167], v[12:15], v[208:211]\n"
"v_cvt_pk_bf16_f32 v146, v136, v137\n"
"v_cvt_pk_bf16_f32 v147, v138, v139\n"
"v_mfma_f32_16x16x32_bf16 v[204:207], a[164:167], v[16:19], v[204:207]\n"
"ds_read_b64 a[192:193], v172 offset:3648\n"
"ds_read_b64 a[194:195], v172 offset:3656\n"
"buffer_store_dwordx4 v[144:147], v10, s[28:31], s79 offen offset:1024 sc1\n"
"v_mfma_f32_16x16x32_bf16 v[200:203], a[164:167], a[100:103], v[200:203]\n"
"ds_read_b64 a[144:145], v172 offset:21216\n"
"ds_read_b64 a[146:147], v172 offset:21224\n"
"v_mfma_f32_16x16x32_bf16 v[196:199], a[164:167], a[96:99], v[196:199]\n"
"ds_read_b128 a[196:199], v173 offset:37856\n"
"ds_read_b128 a[148:151], v173 offset:38368\n"
"v_mfma_f32_16x16x32_bf16 v[192:195], a[164:167], a[112:115], v[192:195]\n"
"s_waitcnt lgkmcnt(14)\n"
"ds_read_b128 a[208:211], v173 offset:38880\n"
"v_mfma_f32_16x16x32_bf16 v[216:219], a[116:119], a[244:247], v[216:219]\n"
"ds_read_b128 a[212:215], v173 offset:39392\n"
"v_mfma_f32_16x16x32_bf16 v[212:215], a[116:119], v[12:15], v[212:215]\n"
"ds_read_b128 a[160:163], v173 offset:39904\n"
"v_mfma_f32_16x16x32_bf16 v[208:211], a[116:119], v[16:19], v[208:211]\n"
"ds_read_b128 a[228:231], v173 offset:40416\n"
"v_mfma_f32_16x16x32_bf16 v[204:207], a[116:119], a[100:103], v[204:207]\n"
"ds_read_b128 a[224:227], v173 offset:40928\n"
"s_waitcnt vmcnt(6)\n"
"v_cmp_ne_u64 vcc, s[34:35], v[166:167]\n"
"s_nop 3\n"
"s_cbranch_vccz pF2C_ok_gated_mix_fused\n"
"s_mov_b32 s83, 0\n"
"pF2C_sp_gated_mix_fused:\n"
"buffer_load_dwordx2 v[166:167], v169, s[28:31], 0 offen offset:384 sc1\n"
"s_waitcnt vmcnt(0)\n"
"s_add_u32 s83, s83, 1\n"
"s_cmp_gt_u32 s83, 1048576\n"
"s_cbranch_scc1 pF2C_ok_gated_mix_fused\n"
"v_cmp_ne_u64 vcc, s[34:35], v[166:167]\n"
"s_nop 3\n"
"s_cbranch_vccnz pF2C_sp_gated_mix_fused\n"
"pF2C_ok_gated_mix_fused:\n"
"v_mfma_f32_16x16x32_bf16 v[200:203], a[116:119], a[96:99], v[200:203]\n"
"ds_read_b128 a[240:243], v173 offset:41440\n"
"v_mfma_f32_16x16x32_bf16 v[196:199], a[116:119], a[112:115], v[196:199]\n"
"s_waitcnt lgkmcnt(15)\n"
"ds_read_b64 a[164:165], v172 offset:21728\n"
"ds_read_b64 a[166:167], v172 offset:21736\n"
"v_mfma_f32_16x16x32_bf16 v[216:219], a[176:179], v[12:15], v[216:219]\n"
"s_add_u32 s44, s64, 8192\n"
"buffer_load_dwordx4 a[168:171], v162, s[28:31], s44 offen offset:0 sc1\n"
"v_mfma_f32_16x16x32_bf16 v[212:215], a[176:179], v[16:19], v[212:215]\n"
"v_mfma_f32_16x16x32_bf16 v[208:211], a[176:179], a[100:103], v[208:211]\n"
"v_mfma_f32_16x16x32_bf16 v[204:207], a[176:179], a[96:99], v[204:207]\n"
"buffer_load_dwordx4 a[172:175], v163, s[28:31], s44 offen offset:0 sc1\n"
"v_mfma_f32_16x16x32_bf16 v[200:203], a[176:179], a[112:115], v[200:203]\n"
"s_waitcnt lgkmcnt(15)\n"
"v_mfma_f32_16x16x32_bf16 v[216:219], a[128:131], v[16:19], v[216:219]\n"
"v_mfma_f32_16x16x32_bf16 v[212:215], a[128:131], a[100:103], v[212:215]\n"
"ds_read_b64 a[244:245], v172 offset:22240\n"
"ds_read_b64 a[246:247], v172 offset:22248\n"
"v_mfma_f32_16x16x32_bf16 v[208:211], a[128:131], a[96:99], v[208:211]\n"
"s_add_u32 s44, s64, 81920\n"
"buffer_load_dwordx4 a[184:187], v162, s[28:31], s44 offen offset:0 sc1\n"
"v_mfma_f32_16x16x32_bf16 v[204:207], a[128:131], a[112:115], v[204:207]\n"
"s_waitcnt lgkmcnt(15)\n"
"v_mfma_f32_16x16x32_bf16 v[216:219], a[180:183], a[100:103], v[216:219]\n"
"v_mfma_f32_16x16x32_bf16 v[212:215], a[180:183], a[96:99], v[212:215]\n"
"buffer_load_dwordx4 a[188:191], v163, s[28:31], s44 offen offset:0 sc1\n"
"v_mfma_f32_16x16x32_bf16 v[208:211], a[180:183], a[112:115], v[208:211]\n"
"s_waitcnt lgkmcnt(15)\n"
"v_mfma_f32_16x16x32_bf16 v[216:219], a[132:135], a[96:99], v[216:219]\n"
"s_add_u32 s44, s65, 8192\n"
"ds_read_b64 a[116:117], v172 offset:22752\n"
"ds_read_b64 a[118:119], v172 offset:22760\n"
"v_mfma_f32_16x16x32_bf16 v[212:215], a[132:135], a[112:115], v[212:215]\n"
"s_waitcnt lgkmcnt(15)\n"
"buffer_load_dwordx4 a[0:3], v162, s[28:31], s44 offen offset:0 sc1\n"
"v_mfma_f32_16x16x32_bf16 v[216:219], a[192:195], a[112:115], v[216:219]\n"
"s_waitcnt lgkmcnt(13)\n"
"v_mfma_f32_16x16x32_bf16 v[248:251], a[144:147], a[196:199], 0\n"
"s_waitcnt lgkmcnt(12)\n"
"v_mfma_f32_16x16x32_bf16 v[244:247], a[144:147], a[148:151], 0\n"
"s_waitcnt lgkmcnt(11)\n"
"buffer_load_dwordx4 a[4:7], v163, s[28:31], s44 offen offset:0 sc1\n"
"v_mfma_f32_16x16x32_bf16 v[240:243], a[144:147], a[208:211], 0\n"
"s_waitcnt lgkmcnt(10)\n"
"ds_read_b64 v[12:13], v172 offset:23264\n"
"ds_read_b64 v[14:15], v172 offset:23272\n"
"ds_write_b128 v176, v[188:191] offset:0\n"
"v_mfma_f32_16x16x32_bf16 v[236:239], a[144:147], a[212:215], 0\n"
"s_waitcnt lgkmcnt(12)\n"
"s_add_u32 s44, s65, 81920\n"
"v_mfma_f32_16x16x32_bf16 v[232:235], a[144:147], a[160:163], 0\n"
"s_waitcnt lgkmcnt(11)\n"
"buffer_load_dwordx4 a[16:19], v162, s[28:31], s44 offen offset:0 sc1\n"
"v_mfma_f32_16x16x32_bf16 v[228:231], a[144:147], a[228:231], 0\n"
"s_waitcnt lgkmcnt(10)\n"
"ds_write_b128 v176, v[192:195] offset:1024\n"
"v_mfma_f32_16x16x32_bf16 v[224:227], a[144:147], a[224:227], 0\n"
"s_waitcnt lgkmcnt(10)\n"
"ds_read_b64 a[176:177], v172 offset:23776\n"
"ds_read_b64 a[178:179], v172 offset:23784\n"
"v_mfma_f32_16x16x32_bf16 v[220:223], a[144:147], a[240:243], 0\n"
"s_waitcnt lgkmcnt(10)\n"
"buffer_load_dwordx4 a[20:23], v163, s[28:31], s44 offen offset:0 sc1\n"
"v_mfma_f32_16x16x32_bf16 v[248:251], a[164:167], a[148:151], v[248:251]\n"
"ds_write_b128 v176, v[196:199] offset:2048\n"
"v_mfma_f32_16x16x32_bf16 v[244:247], a[164:167], a[208:211], v[244:247]\n"
"ds_read_b64 v[16:17], v172 offset:24288\n"
"ds_read_b64 v[18:19], v172 offset:24296\n"
"v_mfma_f32_16x16x32_bf16 v[240:243], a[164:167], a[212:215], v[240:243]\n"
"s_add_u32 s44, s66, 8192\n"
"buffer_load_dwordx4 a[32:35], v162, s[28:31], s44 offen offset:0 sc1\n"
"v_mfma_f32_16x16x32_bf16 v[236:239], a[164:167], a[160:163], v[236:239]\n"
"ds_read_b64 a[128:129], v172 offset:24800\n"
"ds_read_b64 a[130:131], v172 offset:24808\n"
"ds_write_b128 v176, v[200:203] offset:3072\n"
"v_mfma_f32_16x16x32_bf16 v[232:235], a[164:167], a[228:231], v[232:235]\n"
"ds_read_b64 a[100:101], v172 offset:21280\n"
"ds_read_b64 a[102:103], v172 offset:21288\n"
"v_mfma_f32_16x16x32_bf16 v[228:231], a[164:167], a[224:227], v[228:231]\n"
"ds_read_b128 a[180:183], v173 offset:37920\n"
"ds_read_b128 a[96:99], v173 offset:38432\n"
"ds_write_b128 v176, v[204:207] offset:4096\n"
"v_mfma_f32_16x16x32_bf16 v[224:227], a[164:167], a[240:243], v[224:227]\n"
"s_waitcnt lgkmcnt(15)\n"
"ds_read_b128 a[132:135], v173 offset:38944\n"
"v_mfma_f32_16x16x32_bf16 v[248:251], a[244:247], a[208:211], v[248:251]\n"
"ds_read_b128 a[192:195], v173 offset:39456\n"
"ds_write_b128 v176, v[208:211] offset:5120\n"
"v_mfma_f32_16x16x32_bf16 v[244:247], a[244:247], a[212:215], v[244:247]\n"
"ds_read_b128 a[112:115], v173 offset:39968\n"
"v_mfma_f32_16x16x32_bf16 v[240:243], a[244:247], a[160:163], v[240:243]\n"
"ds_read_b128 a[196:199], v173 offset:40480\n"
"ds_write_b128 v176, v[212:215] offset:6144\n"
"v_mfma_f32_16x16x32_bf16 v[236:239], a[244:247], a[228:231], v[236:239]\n"
"ds_read_b128 a[144:147], v173 offset:40992\n"
"v_mfma_f32_16x16x32_bf16 v[232:235], a[244:247], a[224:227], v[232:235]\n"
"ds_read_b128 a[148:151], v173 offset:41504\n"
"ds_write_b128 v176, v[216:219] offset:7168\n"
"v_mfma_f32_16x16x32_bf16 v[228:231], a[244:247], a[240:243], v[228:231]\n"
"s_waitcnt lgkmcnt(15)\n"
"ds_read_b64 a[164:165], v172 offset:21792\n"
"ds_read_b64 a[166:167], v172 offset:21800\n"
"v_mfma_f32_16x16x32_bf16 v[248:251], a[116:119], a[212:215], v[248:251]\n"
"buffer_load_dwordx4 a[36:39], v163, s[28:31], s44 offen offset:0 sc1\n"
"v_mfma_f32_16x16x32_bf16 v[244:247], a[116:119], a[160:163], v[244:247]\n"
"v_mfma_f32_16x16x32_bf16 v[240:243], a[116:119], a[228:231], v[240:243]\n"
"v_mfma_f32_16x16x32_bf16 v[236:239], a[116:119], a[224:227], v[236:239]\n"
"s_add_u32 s44, s66, 81920\n"
"buffer_load_dwordx4 a[48:51], v162, s[28:31], s44 offen offset:0 sc1\n"
"v_mfma_f32_16x16x32_bf16 v[232:235], a[116:119], a[240:243], v[232:235]\n"
"s_waitcnt lgkmcnt(15)\n"
"v_mfma_f32_16x16x32_bf16 v[248:251], v[12:15], a[160:163], v[248:251]\n"
"s_waitcnt vmcnt(11) lgkmcnt(2)\n"
"s_barrier\n"
"v_mfma_f32_16x16x32_bf16 v[244:247], v[12:15], a[228:231], v[244:247]\n"
"ds_read_b64 a[208:209], v172 offset:22304\n"
"ds_read_b64 a[210:211], v172 offset:22312\n"
"v_mfma_f32_16x16x32_bf16 v[240:243], v[12:15], a[224:227], v[240:243]\n"
"buffer_load_dwordx4 a[52:55], v163, s[28:31], s44 offen offset:0 sc1\n"
"v_mfma_f32_16x16x32_bf16 v[236:239], v[12:15], a[240:243], v[236:239]\n"
"buffer_store_dwordx2 v[164:165], v168, s[28:31], s82 offen offset:512 sc1\n"
"v_mfma_f32_16x16x32_bf16 v[248:251], a[176:179], a[228:231], v[248:251]\n"
"s_add_u32 s84, s15, 0\n"
"ds_read_b128 v[188:191], v177 offset:0\n"
"ds_read_b128 v[192:195], v177 offset:8192\n"
"v_mfma_f32_16x16x32_bf16 v[244:247], a[176:179], a[224:227], v[244:247]\n"
"s_add_u32 m0, s54, 91648\n"
"ds_read_b128 v[196:199], v177 offset:16384\n"
"ds_read_b128 v[200:203], v177 offset:24576\n"
"v_mfma_f32_16x16x32_bf16 v[240:243], a[176:179], a[240:243], v[240:243]\n"
"buffer_load_dwordx4 v158, s[36:39], s84 offen offset:0 lds\n"
"v_mfma_f32_16x16x32_bf16 v[248:251], v[16:19], a[224:227], v[248:251]\n"
"ds_read_b64 a[244:245], v172 offset:22816\n"
"ds_read_b64 a[246:247], v172 offset:22824\n"
"v_mfma_f32_16x16x32_bf16 v[244:247], v[16:19], a[240:243], v[244:247]\n"
"s_add_u32 m0, s54, 92736\n"
"ds_read_b128 v[204:207], v177 offset:1024\n"
"ds_read_b128 v[208:211], v177 offset:9216\n"
"v_mfma_f32_16x16x32_bf16 v[248:251], a[128:131], a[240:243], v[248:251]\n"
"buffer_load_dwordx4 v159, s[36:39], s84 offen offset:0 lds\n"
"v_mfma_f32_16x16x32_bf16 v[248:251], a[100:103], a[180:183], v[248:251]\n"
"s_add_u32 s84, s12, 36864\n"
"ds_read_b128 v[212:215], v177 offset:17408\n"
"ds_read_b128 v[216:219], v177 offset:25600\n"
"v_mfma_f32_16x16x32_bf16 v[244:247], a[100:103], a[96:99], v[244:247]\n"
"s_add_u32 m0, s54, 100352\n"
"ds_read_b64 v[60:61], v178 offset:17152\n"
"ds_read_b64 v[62:63], v178 offset:16640\n"
"v_mfma_f32_16x16x32_bf16 v[240:243], a[100:103], a[132:135], v[240:243]\n"
"ds_read_b64 a[212:213], v172 offset:23328\n"
"ds_read_b64 a[214:215], v172 offset:23336\n"
"v_mfma_f32_16x16x32_bf16 v[236:239], a[100:103], a[192:195], v[236:239]\n"
"buffer_load_dwordx4 v158, s[36:39], s84 offen offset:0 lds\n"
"v_mfma_f32_16x16x32_bf16 v[232:235], a[100:103], a[112:115], v[232:235]\n"
"v_mfma_f32_16x16x32_bf16 v[228:231], a[100:103], a[196:199], v[228:231]\n"
"s_add_u32 m0, s54, 101440\n"
"v_mfma_f32_16x16x32_bf16 v[224:227], a[100:103], a[144:147], v[224:227]\n"
"ds_read_b64 a[116:117], v172 offset:23840\n"
"ds_read_b64 a[118:119], v172 offset:23848\n"
"s_waitcnt lgkmcnt(4)\n"
"v_mfma_f32_16x16x32_bf16 v[220:223], a[100:103], a[148:151], v[220:223]\n"
"buffer_load_dwordx4 v159, s[36:39], s84 offen offset:0 lds\n"
"v_add_f32 v188, v188, v192\n"
"v_mfma_f32_16x16x32_bf16 v[248:251], a[164:167], a[96:99], v[248:251]\n"
"v_add_f32 v196, v196, v200\n"
"v_add_f32 v189, v189, v193\n"
"v_add_f32 v197, v197, v201\n"
"v_mfma_f32_16x16x32_bf16 v[244:247], a[164:167], a[132:135], v[244:247]\n"
"s_add_u32 m0, s54, 0\n"
"ds_read_b64 a[160:161], v172 offset:24352\n"
"ds_read_b64 a[162:163], v172 offset:24360\n"
"v_add_f32 v190, v190, v194\n"
"v_mfma_f32_16x16x32_bf16 v[240:243], a[164:167], a[192:195], v[240:243]\n"
"s_add_u32 s84, s13, 36864\n"
"buffer_load_dwordx4 v158, s[36:39], s84 offen offset:0 lds\n"
"v_add_f32 v198, v198, v202\n"
"v_mfma_f32_16x16x32_bf16 v[236:239], a[164:167], a[112:115], v[236:239]\n"
"ds_read_b64 v[12:13], v172 offset:24864\n"
"ds_read_b64 v[14:15], v172 offset:24872\n"
"v_add_f32 v191, v191, v195\n"
"v_mfma_f32_16x16x32_bf16 v[232:235], a[164:167], a[196:199], v[232:235]\n"
"ds_read_b64 a[228:229], v172 offset:42432\n"
"ds_read_b64 a[230:231], v172 offset:42440\n"
"v_add_f32 v199, v199, v203\n"
"v_mfma_f32_16x16x32_bf16 v[228:231], a[164:167], a[144:147], v[228:231]\n"
"ds_read_b128 a[176:179], v174 offset:59072\n"
"ds_read_b128 a[224:227], v175 offset:59072\n"
"v_lshlrev_b32 v195, 16, v60\n"
"v_mfma_f32_16x16x32_bf16 v[224:227], a[164:167], a[148:151], v[224:227]\n"
"ds_read_b64 v[16:17], v172 offset:42944\n"
"ds_read_b64 v[18:19], v172 offset:42952\n"
"v_and_b32 v194, s81, v60\n"
"v_mfma_f32_16x16x32_bf16 v[248:251], a[208:211], a[132:135], v[248:251]\n"
"ds_read_b128 a[128:131], v174 offset:59584\n"
"ds_read_b128 a[240:243], v175 offset:59584\n"
"v_lshlrev_b32 v193, 16, v61\n"
"v_mfma_f32_16x16x32_bf16 v[244:247], a[208:211], a[192:195], v[244:247]\n"
"ds_read_b64 a[180:181], v172 offset:43456\n"
"ds_read_b64 a[182:183], v172 offset:43464\n"
"v_and_b32 v192, s81, v61\n"
"v_mfma_f32_16x16x32_bf16 v[240:243], a[208:211], a[112:115], v[240:243]\n"
"ds_read_b128 a[100:103], v174 offset:60096\n"
"ds_read_b128 a[96:99], v175 offset:60096\n"
"v_add_f32 v188, v188, v196\n"
"v_mfma_f32_16x16x32_bf16 v[236:239], a[208:211], a[196:199], v[236:239]\n"
"ds_read_b64 a[164:165], v172 offset:43968\n"
"ds_read_b64 a[166:167], v172 offset:43976\n"
"v_add_f32 v189, v189, v197\n"
"v_mfma_f32_16x16x32_bf16 v[232:235], a[208:211], a[144:147], v[232:235]\n"
"s_add_u32 m0, s54, 1088\n"
"ds_read_b128 a[132:135], v174 offset:60608\n"
"v_add_f32 v190, v190, v198\n"
"v_add_f32 v191, v191, v199\n"
"v_mfma_f32_16x16x32_bf16 v[228:231], a[208:211], a[148:151], v[228:231]\n"
"buffer_load_dwordx4 v159, s[36:39], s84 offen offset:0 lds\n"
"v_fmac_f32 v188, s85, v192\n"
"v_mfma_f32_16x16x32_bf16 v[248:251], a[244:247], a[192:195], v[248:251]\n"
"ds_read_b128 a[208:211], v175 offset:60608\n"
"v_fmac_f32 v189, s85, v193\n"
"v_fmac_f32 v190, s85, v194\n"
"v_mfma_f32_16x16x32_bf16 v[244:247], a[244:247], a[112:115], v[244:247]\n"
"ds_read_b64 a[192:193], v172 offset:44480\n"
"ds_read_b64 a[194:195], v172 offset:44488\n"
"v_fmac_f32 v191, s85, v195\n"
"v_mfma_f32_16x16x32_bf16 v[240:243], a[244:247], a[196:199], v[240:243]\n"
"s_add_u32 s44, s67, 8192\n"
"buffer_load_dwordx4 a[64:67], v162, s[28:31], s44 offen offset:0 sc1\n"
"v_cvt_pk_bf16_f32 v196, v188, v189\n"
"v_mfma_f32_16x16x32_bf16 v[236:239], a[244:247], a[144:147], v[236:239]\n"
"v_cvt_pk_bf16_f32 v197, v190, v191\n"
"v_add_f32 v204, v204, v208\n"
"v_add_f32 v212, v212, v216\n"
"v_mfma_f32_16x16x32_bf16 v[232:235], a[244:247], a[148:151], v[232:235]\n"
"s_waitcnt lgkmcnt(15)\n"
"buffer_store_dwordx2 v[196:197], v179, s[28:31], s48 offen offset:0 sc1\n"
"v_add_f32 v205, v205, v209\n"
"v_mfma_f32_16x16x32_bf16 v[248:251], a[212:215], a[112:115], v[248:251]\n"
"ds_read_b128 a[244:247], v174 offset:61120\n"
"v_add_f32 v213, v213, v217\n"
"v_add_f32 v206, v206, v210\n"
"v_mfma_f32_16x16x32_bf16 v[244:247], a[212:215], a[196:199], v[244:247]\n"
"ds_read_b64 a[112:113], v172 offset:44992\n"
"ds_read_b64 a[114:115], v172 offset:45000\n"
"v_add_f32 v214, v214, v218\n"
"v_mfma_f32_16x16x32_bf16 v[240:243], a[212:215], a[144:147], v[240:243]\n"
"buffer_load_dwordx4 a[68:71], v163, s[28:31], s44 offen offset:0 sc1\n"
"v_add_f32 v207, v207, v211\n"
"v_mfma_f32_16x16x32_bf16 v[236:239], a[212:215], a[148:151], v[236:239]\n"
"s_waitcnt lgkmcnt(15)\n"
"v_add_f32 v215, v215, v219\n"
"v_lshlrev_b32 v211, 16, v62\n"
"v_and_b32 v210, s81, v62\n"
"v_mfma_f32_16x16x32_bf16 v[248:251], a[116:119], a[196:199], v[248:251]\n"
"ds_read_b128 a[212:215], v174 offset:61632\n"
"v_lshlrev_b32 v209, 16, v63\n"
"v_and_b32 v208, s81, v63\n"
"v_mfma_f32_16x16x32_bf16 v[244:247], a[116:119], a[144:147], v[244:247]\n"
"s_add_u32 s44, s67, 81920\n"
"ds_read_b64 a[196:197], v172 offset:45504\n"
"ds_read_b64 a[198:199], v172 offset:45512\n"
"v_add_f32 v204, v204, v212\n"
"v_mfma_f32_16x16x32_bf16 v[240:243], a[116:119], a[148:151], v[240:243]\n"
"s_waitcnt lgkmcnt(15)\n"
"buffer_load_dwordx4 a[80:83], v162, s[28:31], s44 offen offset:0 sc1\n"
"v_add_f32 v205, v205, v213\n"
"v_mfma_f32_16x16x32_bf16 v[248:251], a[160:163], a[144:147], v[248:251]\n"
"ds_read_b128 a[116:119], v174 offset:62144\n"
"v_add_f32 v206, v206, v214\n"
"v_add_f32 v207, v207, v215\n"
"v_mfma_f32_16x16x32_bf16 v[244:247], a[160:163], a[148:151], v[244:247]\n"
"s_waitcnt lgkmcnt(15)\n"
"ds_read_b64 a[144:145], v172 offset:46016\n"
"ds_read_b64 a[146:147], v172 offset:46024\n"
"v_fmac_f32 v204, s85, v208\n"
"v_mfma_f32_16x16x32_bf16 v[248:251], v[12:15], a[148:151], v[248:251]\n"
"s_waitcnt lgkmcnt(15)\n"
"ds_read_b128 a[160:163], v174 offset:62656\n"
"v_fmac_f32 v205, s85, v209\n"
"v_fmac_f32 v206, s85, v210\n"
"v_mfma_f32_16x16x32_bf16 v[28:31], a[228:231], a[176:179], 0\n"
"s_waitcnt lgkmcnt(15)\n"
"ds_read_b64 v[12:13], v172 offset:42496\n"
"ds_read_b64 v[14:15], v172 offset:42504\n"
"v_fmac_f32 v207, s85, v211\n"
"v_mfma_f32_16x16x32_bf16 v[32:35], a[228:231], a[224:227], 0\n"
"s_waitcnt lgkmcnt(15)\n"
"s_barrier\n"
"ds_read_b128 a[148:151], v174 offset:59136\n"
"ds_read_b128 a[176:179], v175 offset:59136\n"
"v_mfma_f32_16x16x32_bf16 v[28:31], v[16:19], a[128:131], v[28:31]\n"
"s_waitcnt lgkmcnt(15)\n"
"ds_read_b64 a[224:225], v172 offset:43008\n"
"ds_read_b64 a[226:227], v172 offset:43016\n"
"ds_write_b128 v176, v[220:223] offset:0\n"
"v_mfma_f32_16x16x32_bf16 v[32:35], v[16:19], a[240:243], v[32:35]\n"
"s_waitcnt lgkmcnt(15)\n"
"ds_read_b128 a[228:231], v174 offset:59648\n"
"ds_read_b128 a[128:131], v175 offset:59648\n"
"v_cvt_pk_bf16_f32 v212, v204, v205\n"
"v_mfma_f32_16x16x32_bf16 v[28:31], a[180:183], a[100:103], v[28:31]\n"
"s_waitcnt lgkmcnt(15)\n"
"ds_read_b64 a[240:241], v172 offset:43520\n"
"ds_read_b64 a[242:243], v172 offset:43528\n"
"ds_write_b128 v176, v[224:227] offset:1024\n"
"v_mfma_f32_16x16x32_bf16 v[32:35], a[180:183], a[96:99], v[32:35]\n"
"s_waitcnt lgkmcnt(15)\n"
"ds_read_b128 v[16:19], v174 offset:60160\n"
"ds_read_b128 a[100:103], v175 offset:60160\n"
"v_cvt_pk_bf16_f32 v213, v206, v207\n"
"v_mfma_f32_16x16x32_bf16 v[28:31], a[164:167], a[132:135], v[28:31]\n"
"s_waitcnt lgkmcnt(15)\n"
"ds_read_b64 a[96:97], v172 offset:44032\n"
"ds_read_b64 a[98:99], v172 offset:44040\n"
"ds_write_b128 v176, v[228:231] offset:2048\n"
"buffer_store_dwordx2 v[212:213], v179, s[28:31], s48 offen offset:512 sc1\n"
"v_mfma_f32_16x16x32_bf16 v[32:35], a[164:167], a[208:211], v[32:35]\n"
"s_waitcnt lgkmcnt(15)\n"
"ds_read_b128 a[180:183], v174 offset:60672\n"
"ds_read_b128 a[132:135], v175 offset:60672\n"
"v_mfma_f32_16x16x32_bf16 v[28:31], a[192:195], a[244:247], v[28:31]\n"
"s_waitcnt lgkmcnt(15)\n"
"ds_read_b64 a[208:209], v172 offset:44544\n"
"ds_read_b64 a[210:211], v172 offset:44552\n"
"ds_write_b128 v176, v[232:235] offset:3072\n"
"v_mfma_f32_16x16x32_bf16 v[28:31], a[112:115], a[212:215], v[28:31]\n"
"s_waitcnt lgkmcnt(15)\n"
"ds_read_b128 a[164:167], v174 offset:61184\n"
"ds_read_b64 a[244:245], v172 offset:45056\n"
"v_mfma_f32_16x16x32_bf16 v[28:31], a[196:199], a[116:119], v[28:31]\n"
"s_waitcnt lgkmcnt(15)\n"
"ds_read_b64 a[246:247], v172 offset:45064\n"
"ds_read_b128 a[192:195], v174 offset:61696\n"
"ds_write_b128 v176, v[236:239] offset:4096\n"
"v_mfma_f32_16x16x32_bf16 v[28:31], a[144:147], a[160:163], v[28:31]\n"
"s_waitcnt lgkmcnt(15)\n"
"ds_read_b64 a[212:213], v172 offset:45568\n"
"ds_read_b64 a[214:215], v172 offset:45576\n"
"v_mfma_f32_16x16x32_bf16 v[28:31], v[12:15], a[148:151], v[28:31]\n"
"s_waitcnt lgkmcnt(15)\n"
"s_barrier\n"
"ds_read_b128 a[112:115], v174 offset:62208\n"
"ds_read_b64 a[116:117], v172 offset:46080\n"
"ds_write_b128 v176, v[240:243] offset:5120\n"
"v_mfma_f32_16x16x32_bf16 v[32:35], v[12:15], a[176:179], v[32:35]\n"
"s_waitcnt lgkmcnt(15)\n"
"ds_read_b64 a[118:119], v172 offset:46088\n"
"ds_read_b128 a[196:199], v174 offset:62720\n"
"v_mfma_f32_16x16x32_bf16 v[28:31], a[224:227], a[228:231], v[28:31]\n"
"s_waitcnt lgkmcnt(15)\n"
"ds_read_b64_tr_b16 v[44:45], v156 offset:0\n"
"ds_read_b64_tr_b16 v[46:47], v156 offset:512\n"
"ds_write_b128 v176, v[244:247] offset:6144\n"
"v_mfma_f32_16x16x32_bf16 v[32:35], a[224:227], a[128:131], v[32:35]\n"
"s_waitcnt lgkmcnt(15)\n"
"ds_read_b64_tr_b16 v[48:49], v157 offset:0\n"
"ds_read_b64_tr_b16 v[50:51], v157 offset:512\n"
"v_mfma_f32_16x16x32_bf16 v[28:31], a[240:243], v[16:19], v[28:31]\n"
"s_waitcnt lgkmcnt(15)\n"
"ds_read_b64_tr_b16 v[52:53], v3 offset:20480\n"
"ds_read_b64_tr_b16 v[54:55], v3 offset:20736\n"
"ds_write_b128 v176, v[248:251] offset:7168\n"
"v_mfma_f32_16x16x32_bf16 v[32:35], a[240:243], a[100:103], v[32:35]\n"
"s_waitcnt lgkmcnt(15)\n"
"ds_read_b64_tr_b16 v[56:57], v156 offset:64\n"
"ds_read_b64_tr_b16 v[58:59], v156 offset:576\n"
"v_mfma_f32_16x16x32_bf16 v[28:31], a[96:99], a[180:183], v[28:31]\n"
"s_waitcnt lgkmcnt(15)\n"
"ds_read_b64_tr_b16 v[124:125], v157 offset:64\n"
"ds_read_b64_tr_b16 v[126:127], v157 offset:576\n"
"v_mfma_f32_16x16x32_bf16 v[32:35], a[96:99], a[132:135], v[32:35]\n"
"s_waitcnt lgkmcnt(15)\n"
"ds_read_b64_tr_b16 v[128:129], v6 offset:20480\n"
"ds_read_b64_tr_b16 v[130:131], v6 offset:20736\n"
"v_mfma_f32_16x16x32_bf16 v[28:31], a[208:211], a[164:167], v[28:31]\n"
"s_waitcnt lgkmcnt(15)\n"
"ds_read_b64_tr_b16 v[132:133], v156 offset:4352\n"
"ds_read_b64_tr_b16 v[134:135], v156 offset:4864\n"
"v_mfma_f32_16x16x32_bf16 v[28:31], a[244:247], a[192:195], v[28:31]\n"
"s_waitcnt lgkmcnt(15)\n"
"buffer_load_dwordx4 a[84:87], v163, s[28:31], s44 offen offset:0 sc1\n"
"v_mfma_f32_16x16x32_bf16 v[28:31], a[212:215], a[112:115], v[28:31]\n"
"s_waitcnt lgkmcnt(15)\n"
"v_mfma_f32_16x16x32_bf16 v[28:31], a[116:119], a[196:199], v[28:31]\n"
"s_waitcnt lgkmcnt(9)\n"
"v_mfma_f32_16x16x32_bf16 v[188:191], v[44:47], a[8:11], 0\n"
"ds_read_b64_tr_b16 v[136:137], v157 offset:4352\n"
"ds_read_b64_tr_b16 v[138:139], v157 offset:4864\n"
"v_mfma_f32_16x16x32_bf16 v[192:195], v[44:47], a[12:15], 0\n"
"buffer_load_dwordx2 v[166:167], v169, s[28:31], 0 offen offset:512 sc1\n"
"v_mfma_f32_16x16x32_bf16 v[196:199], v[48:51], a[8:11], 0\n"
"v_mfma_f32_16x16x32_bf16 v[200:203], v[48:51], a[12:15], 0\n"
"s_add_u32 m0, s53, 20480\n"
"ds_read_b64_tr_b16 v[44:45], v3 offset:22528\n"
"ds_read_b64_tr_b16 v[46:47], v3 offset:22784\n"
"v_mfma_f32_16x16x32_bf16 v[220:223], v[52:55], a[8:11], 0\n"
"s_waitcnt lgkmcnt(6)\n"
"ds_read_b64_tr_b16 v[48:49], v156 offset:4416\n"
"ds_read_b64_tr_b16 v[50:51], v156 offset:4928\n"
"ds_write_b128 v252, v[28:31] offset:0\n"
"v_mfma_f32_16x16x32_bf16 v[204:207], v[56:59], a[8:11], 0\n"
"s_add_u32 s84, s15, 0\n"
"buffer_load_dwordx4 v171, s[36:39], s84 offen offset:0 lds\n"
"v_mfma_f32_16x16x32_bf16 v[208:211], v[56:59], a[12:15], 0\n"
"ds_read_b64_tr_b16 v[52:53], v157 offset:4416\n"
"ds_read_b64_tr_b16 v[54:55], v157 offset:4928\n"
"ds_write_b128 v252, v[32:35] offset:1024\n"
"v_mfma_f32_16x16x32_bf16 v[212:215], v[124:127], a[8:11], 0\n"
"s_add_u32 m0, s53, 24576\n"
"ds_read_b64_tr_b16 v[56:57], v6 offset:22528\n"
"ds_read_b64_tr_b16 v[58:59], v6 offset:22784\n"
"v_mfma_f32_16x16x32_bf16 v[216:219], v[124:127], a[12:15], 0\n"
"s_add_u32 s84, s12, 36864\n"
"buffer_load_dwordx4 v171, s[36:39], s84 offen offset:0 lds\n"
"v_mfma_f32_16x16x32_bf16 v[224:227], v[128:131], a[8:11], 0\n"
"s_waitcnt lgkmcnt(8)\n"
"v_mfma_f32_16x16x32_bf16 v[188:191], v[132:135], a[24:27], v[188:191]\n"
"ds_read_b64_tr_b16 v[124:125], v156 offset:8704\n"
"ds_read_b64_tr_b16 v[126:127], v156 offset:9216\n"
"v_mfma_f32_16x16x32_bf16 v[192:195], v[132:135], a[28:31], v[192:195]\n"
"s_add_u32 m0, s53, 28672\n"
"ds_read_b64_tr_b16 v[128:129], v157 offset:8704\n"
"ds_read_b64_tr_b16 v[130:131], v157 offset:9216\n"
"v_mfma_f32_16x16x32_bf16 v[196:199], v[136:139], a[24:27], v[196:199]\n"
"s_add_u32 s84, s13, 36864\n"
"buffer_load_dwordx4 v171, s[36:39], s84 offen offset:0 lds\n"
"v_mfma_f32_16x16x32_bf16 v[200:203], v[136:139], a[28:31], v[200:203]\n"
"s_waitcnt lgkmcnt(6)\n"
"s_barrier\n"
"ds_read_b64_tr_b16 v[132:133], v3 offset:24576\n"
"ds_read_b64_tr_b16 v[134:135], v3 offset:24832\n"
"v_mfma_f32_16x16x32_bf16 v[220:223], v[44:47], a[24:27], v[220:223]\n"
"s_waitcnt lgkmcnt(6)\n"
"ds_read_b64_tr_b16 v[136:137], v156 offset:8768\n"
"ds_read_b64_tr_b16 v[138:139], v156 offset:9280\n"
"ds_read_b128 v[60:63], v177 offset:0\n"
"v_mfma_f32_16x16x32_bf16 v[204:207], v[48:51], a[24:27], v[204:207]\n"
"s_add_u32 m0, s54, 8704\n"
"ds_read_b64_tr_b16 v[44:45], v157 offset:8768\n"
"ds_read_b64_tr_b16 v[46:47], v157 offset:9280\n"
"ds_read_b128 v[64:67], v177 offset:8192\n"
"v_mfma_f32_16x16x32_bf16 v[208:211], v[48:51], a[28:31], v[208:211]\n"
"s_add_u32 s84, s14, 36864\n"
"buffer_load_dwordx4 v158, s[36:39], s84 offen offset:0 lds\n"
"v_mfma_f32_16x16x32_bf16 v[212:215], v[52:55], a[24:27], v[212:215]\n"
"ds_read_b64_tr_b16 v[48:49], v6 offset:24576\n"
"ds_read_b64_tr_b16 v[50:51], v6 offset:24832\n"
"ds_read_b128 v[68:71], v177 offset:16384\n"
"v_mfma_f32_16x16x32_bf16 v[216:219], v[52:55], a[28:31], v[216:219]\n"
"s_add_u32 m0, s54, 9792\n"
"ds_read_b128 v[72:75], v177 offset:24576\n"
"ds_read_b128 v[76:79], v177 offset:1024\n"
"ds_read_b128 v[80:83], v177 offset:9216\n"
"v_mfma_f32_16x16x32_bf16 v[224:227], v[56:59], a[24:27], v[224:227]\n"
"s_waitcnt lgkmcnt(12)\n"
"buffer_load_dwordx4 v159, s[36:39], s84 offen offset:0 lds\n"
"v_mfma_f32_16x16x32_bf16 v[188:191], v[124:127], a[72:75], v[188:191]\n"
"ds_read_b64_tr_b16 v[52:53], v156 offset:13056\n"
"ds_read_b64_tr_b16 v[54:55], v156 offset:13568\n"
"ds_read_b128 v[84:87], v177 offset:17408\n"
"v_mfma_f32_16x16x32_bf16 v[192:195], v[124:127], a[76:79], v[192:195]\n"
"s_add_u32 m0, s53, 32768\n"
"ds_read_b64_tr_b16 v[56:57], v157 offset:13056\n"
"ds_read_b64_tr_b16 v[58:59], v157 offset:13568\n"
"ds_read_b128 v[88:91], v177 offset:25600\n"
"v_mfma_f32_16x16x32_bf16 v[196:199], v[128:131], a[72:75], v[196:199]\n"
"s_add_u32 s84, s14, 36864\n"
"buffer_load_dwordx4 v171, s[36:39], s84 offen offset:0 lds\n"
"v_mfma_f32_16x16x32_bf16 v[200:203], v[128:131], a[76:79], v[200:203]\n"
"ds_read_b64_tr_b16 v[124:125], v3 offset:26624\n"
"ds_read_b64_tr_b16 v[126:127], v3 offset:26880\n"
"ds_read_b64 v[92:93], v178 offset:38368\n"
"v_mfma_f32_16x16x32_bf16 v[220:223], v[132:135], a[72:75], v[220:223]\n"
"s_waitcnt lgkmcnt(13)\n"
"ds_read_b64_tr_b16 v[128:129], v156 offset:13120\n"
"ds_read_b64_tr_b16 v[130:131], v156 offset:13632\n"
"ds_read_b64 v[94:95], v178 offset:37856\n"
"v_mfma_f32_16x16x32_bf16 v[204:207], v[136:139], a[72:75], v[204:207]\n"
"ds_read_b128 v[100:103], v253 offset:0\n"
"ds_read_b128 v[104:107], v253 offset:2048\n"
"ds_read_b128 v[108:111], v253 offset:4096\n"
"v_mfma_f32_16x16x32_bf16 v[208:211], v[136:139], a[76:79], v[208:211]\n"
"ds_read_b64_tr_b16 v[132:133], v157 offset:13120\n"
"ds_read_b64_tr_b16 v[134:135], v157 offset:13632\n"
"ds_read_b128 v[112:115], v253 offset:6144\n"
"v_mfma_f32_16x16x32_bf16 v[212:215], v[44:47], a[72:75], v[212:215]\n"
"ds_read_b64_tr_b16 v[136:137], v6 offset:26624\n"
"ds_read_b64_tr_b16 v[138:139], v6 offset:26880\n"
"ds_read_b64 v[116:117], v254 offset:59072\n"
"v_mfma_f32_16x16x32_bf16 v[216:219], v[44:47], a[76:79], v[216:219]\n"
"v_mfma_f32_16x16x32_bf16 v[224:227], v[48:51], a[72:75], v[224:227]\n"
"s_waitcnt vmcnt(5) lgkmcnt(13)\n"
"s_barrier\n"
"v_mfma_f32_16x16x32_bf16 v[188:191], v[52:55], a[88:91], v[188:191]\n"
"ds_read_b64_tr_b16 v[44:45], v156 offset:17408\n"
"ds_read_b64_tr_b16 v[46:47], v156 offset:17920\n"
"v_mfma_f32_16x16x32_bf16 v[192:195], v[52:55], a[92:95], v[192:195]\n"
"s_add_u32 m0, s54, 65536\n"
"ds_read_b64_tr_b16 v[48:49], v157 offset:17408\n"
"ds_read_b64_tr_b16 v[50:51], v157 offset:17920\n"
"v_mfma_f32_16x16x32_bf16 v[196:199], v[56:59], a[88:91], v[196:199]\n"
"s_add_u32 s84, s15, 36864\n"
"buffer_load_dwordx4 v158, s[36:39], s84 offen offset:0 lds\n"
"s_waitcnt lgkmcnt(13)\n"
"v_mfma_f32_16x16x32_bf16 v[200:203], v[56:59], a[92:95], v[200:203]\n"
"ds_read_b64_tr_b16 v[52:53], v3 offset:28672\n"
"ds_read_b64_tr_b16 v[54:55], v3 offset:28928\n"
"v_add_f32 v60, v60, v64\n"
"v_mfma_f32_16x16x32_bf16 v[220:223], v[124:127], a[88:91], v[220:223]\n"
"s_waitcnt lgkmcnt(7)\n"
"ds_read_b64_tr_b16 v[56:57], v156 offset:17472\n"
"ds_read_b64_tr_b16 v[58:59], v156 offset:17984\n"
"v_add_f32 v68, v68, v72\n"
"v_mfma_f32_16x16x32_bf16 v[204:207], v[128:131], a[88:91], v[204:207]\n"
"s_add_u32 m0, s54, 66624\n"
"ds_read_b64_tr_b16 v[124:125], v157 offset:17472\n"
"ds_read_b64_tr_b16 v[126:127], v157 offset:17984\n"
"v_add_f32 v61, v61, v65\n"
"v_mfma_f32_16x16x32_bf16 v[208:211], v[128:131], a[92:95], v[208:211]\n"
"buffer_load_dwordx4 v159, s[36:39], s84 offen offset:0 lds\n"
"v_add_f32 v69, v69, v73\n"
"v_mfma_f32_16x16x32_bf16 v[212:215], v[132:135], a[88:91], v[212:215]\n"
"s_add_u32 s84, s15, 36864\n"
"ds_read_b64_tr_b16 v[128:129], v6 offset:28672\n"
"ds_read_b64_tr_b16 v[130:131], v6 offset:28928\n"
"v_add_f32 v62, v62, v66\n"
"v_mfma_f32_16x16x32_bf16 v[216:219], v[132:135], a[92:95], v[216:219]\n"
"s_add_u32 m0, s53, 151552\n"
"v_add_f32 v70, v70, v74\n"
"v_add_f32 v63, v63, v67\n"
"v_add_f32 v71, v71, v75\n"
"v_mfma_f32_16x16x32_bf16 v[224:227], v[136:139], a[88:91], v[224:227]\n"
"s_waitcnt lgkmcnt(6)\n"
"buffer_load_dwordx4 v171, s[36:39], s84 offen offset:0 lds\n"
"v_lshlrev_b32 v67, 16, v92\n"
"v_mfma_f32_16x16x32_bf16 v[188:191], v[44:47], a[104:107], v[188:191]\n"
"ds_read_b64_tr_b16 v[132:133], v156 offset:21760\n"
"ds_read_b64_tr_b16 v[134:135], v156 offset:22272\n"
"v_and_b32 v66, s81, v92\n"
"v_mfma_f32_16x16x32_bf16 v[192:195], v[44:47], a[108:111], v[192:195]\n"
"ds_read_b64_tr_b16 v[136:137], v157 offset:21760\n"
"ds_read_b64_tr_b16 v[138:139], v157 offset:22272\n"
"v_lshlrev_b32 v65, 16, v93\n"
"v_mfma_f32_16x16x32_bf16 v[196:199], v[48:51], a[104:107], v[196:199]\n"
"v_and_b32 v64, s81, v93\n"
"v_add_f32 v60, v60, v68\n"
"v_add_f32 v61, v61, v69\n"
"v_mfma_f32_16x16x32_bf16 v[200:203], v[48:51], a[108:111], v[200:203]\n"
"ds_read_b64_tr_b16 v[44:45], v3 offset:30720\n"
"ds_read_b64_tr_b16 v[46:47], v3 offset:30976\n"
"v_add_f32 v62, v62, v70\n"
"v_mfma_f32_16x16x32_bf16 v[220:223], v[52:55], a[104:107], v[220:223]\n"
"s_waitcnt lgkmcnt(6)\n"
"ds_read_b64_tr_b16 v[48:49], v156 offset:21824\n"
"ds_read_b64_tr_b16 v[50:51], v156 offset:22336\n"
"v_add_f32 v63, v63, v71\n"
"v_mfma_f32_16x16x32_bf16 v[204:207], v[56:59], a[104:107], v[204:207]\n"
"v_cmp_ne_u64 vcc, s[34:35], v[166:167]\n"
"s_nop 3\n"
"s_cbranch_vccz pF2D_ok_gated_mix_fused\n"
"s_mov_b32 s83, 0\n"
"pF2D_sp_gated_mix_fused:\n"
"buffer_load_dwordx2 v[166:167], v169, s[28:31], 0 offen offset:512 sc1\n"
"s_waitcnt vmcnt(0)\n"
"s_add_u32 s83, s83, 1\n"
"s_cmp_gt_u32 s83, 1048576\n"
"s_cbranch_scc1 pF2D_ok_gated_mix_fused\n"
"v_cmp_ne_u64 vcc, s[34:35], v[166:167]\n"
"s_nop 3\n"
"s_cbranch_vccnz pF2D_sp_gated_mix_fused\n"
"pF2D_ok_gated_mix_fused:\n"
"v_mfma_f32_16x16x32_bf16 v[208:211], v[56:59], a[108:111], v[208:211]\n"
"ds_read_b64_tr_b16 v[52:53], v157 offset:21824\n"
"ds_read_b64_tr_b16 v[54:55], v157 offset:22336\n"
"v_fmac_f32 v60, s86, v64\n"
"v_mfma_f32_16x16x32_bf16 v[212:215], v[124:127], a[104:107], v[212:215]\n"
"ds_read_b64_tr_b16 v[56:57], v6 offset:30720\n"
"ds_read_b64_tr_b16 v[58:59], v6 offset:30976\n"
"v_fmac_f32 v61, s86, v65\n"
"v_mfma_f32_16x16x32_bf16 v[216:219], v[124:127], a[108:111], v[216:219]\n"
"s_add_u32 s44, s64, 12288\n"
"buffer_load_dwordx4 a[96:99], v162, s[28:31], s44 offen offset:0 sc1\n"
"v_fmac_f32 v62, s86, v66\n"
"v_mfma_f32_16x16x32_bf16 v[224:227], v[128:131], a[104:107], v[224:227]\n"
"s_waitcnt lgkmcnt(6)\n"
"v_fmac_f32 v63, s86, v67\n"
"v_cvt_pk_bf16_f32 v68, v60, v61\n"
"v_add_f32 v76, v76, v80\n"
"v_mfma_f32_16x16x32_bf16 v[188:191], v[132:135], a[120:123], v[188:191]\n"
"ds_read_b64_tr_b16 v[124:125], v156 offset:26112\n"
"ds_read_b64_tr_b16 v[126:127], v156 offset:26624\n"
"v_cvt_pk_bf16_f32 v69, v62, v63\n"
"v_mfma_f32_16x16x32_bf16 v[192:195], v[132:135], a[124:127], v[192:195]\n"
"ds_read_b64_tr_b16 v[128:129], v157 offset:26112\n"
"ds_read_b64_tr_b16 v[130:131], v157 offset:26624\n"
"v_add_f32 v84, v84, v88\n"
"v_mfma_f32_16x16x32_bf16 v[196:199], v[136:139], a[120:123], v[196:199]\n"
"buffer_load_dwordx4 a[100:103], v163, s[28:31], s44 offen offset:0 sc1\n"
"v_add_f32 v77, v77, v81\n"
"v_mfma_f32_16x16x32_bf16 v[200:203], v[136:139], a[124:127], v[200:203]\n"
"ds_read_b64_tr_b16 v[132:133], v1 offset:20480\n"
"ds_read_b64_tr_b16 v[134:135], v1 offset:20736\n"
"v_add_f32 v85, v85, v89\n"
"v_mfma_f32_16x16x32_bf16 v[220:223], v[44:47], a[120:123], v[220:223]\n"
"s_waitcnt lgkmcnt(6)\n"
"ds_read_b64_tr_b16 v[136:137], v156 offset:26176\n"
"ds_read_b64_tr_b16 v[138:139], v156 offset:26688\n"
"v_add_f32 v78, v78, v82\n"
"v_mfma_f32_16x16x32_bf16 v[204:207], v[48:51], a[120:123], v[204:207]\n"
"ds_read_b64_tr_b16 v[44:45], v157 offset:26176\n"
"ds_read_b64_tr_b16 v[46:47], v157 offset:26688\n"
"v_add_f32 v86, v86, v90\n"
"v_mfma_f32_16x16x32_bf16 v[208:211], v[48:51], a[124:127], v[208:211]\n"
"s_add_u32 s44, s64, 86016\n"
"buffer_load_dwordx4 a[112:115], v162, s[28:31], s44 offen offset:0 sc1\n"
"v_add_f32 v79, v79, v83\n"
"v_mfma_f32_16x16x32_bf16 v[212:215], v[52:55], a[120:123], v[212:215]\n"
"ds_read_b64_tr_b16 v[48:49], v4 offset:20480\n"
"ds_read_b64_tr_b16 v[50:51], v4 offset:20736\n"
"v_add_f32 v87, v87, v91\n"
"v_mfma_f32_16x16x32_bf16 v[216:219], v[52:55], a[124:127], v[216:219]\n"
"buffer_store_dwordx2 v[68:69], v179, s[28:31], s89 offen offset:0 sc1\n"
"v_lshlrev_b32 v83, 16, v94\n"
"v_mfma_f32_16x16x32_bf16 v[224:227], v[56:59], a[120:123], v[224:227]\n"
"s_waitcnt lgkmcnt(6)\n"
"buffer_load_dwordx4 a[116:119], v163, s[28:31], s44 offen offset:0 sc1\n"
"v_and_b32 v82, s81, v94\n"
"v_mfma_f32_16x16x32_bf16 v[188:191], v[124:127], a[136:139], v[188:191]\n"
"ds_read_b64_tr_b16 v[52:53], v156 offset:30464\n"
"ds_read_b64_tr_b16 v[54:55], v156 offset:30976\n"
"v_lshlrev_b32 v81, 16, v95\n"
"v_mfma_f32_16x16x32_bf16 v[192:195], v[124:127], a[140:143], v[192:195]\n"
"ds_read_b64_tr_b16 v[56:57], v157 offset:30464\n"
"ds_read_b64_tr_b16 v[58:59], v157 offset:30976\n"
"v_and_b32 v80, s81, v95\n"
"v_mfma_f32_16x16x32_bf16 v[196:199], v[128:131], a[136:139], v[196:199]\n"
"s_add_u32 s44, s65, 12288\n"
"buffer_load_dwordx4 a[128:131], v162, s[28:31], s44 offen offset:0 sc1\n"
"v_add_f32 v76, v76, v84\n"
"v_mfma_f32_16x16x32_bf16 v[200:203], v[128:131], a[140:143], v[200:203]\n"
"ds_read_b64_tr_b16 v[124:125], v1 offset:22528\n"
"ds_read_b64_tr_b16 v[126:127], v1 offset:22784\n"
"v_add_f32 v77, v77, v85\n"
"v_mfma_f32_16x16x32_bf16 v[220:223], v[132:135], a[136:139], v[220:223]\n"
"s_waitcnt lgkmcnt(6)\n"
"ds_read_b64_tr_b16 v[128:129], v156 offset:30528\n"
"ds_read_b64_tr_b16 v[130:131], v156 offset:31040\n"
"v_add_f32 v78, v78, v86\n"
"v_mfma_f32_16x16x32_bf16 v[204:207], v[136:139], a[136:139], v[204:207]\n"
"buffer_load_dwordx4 a[132:135], v163, s[28:31], s44 offen offset:0 sc1\n"
"v_add_f32 v79, v79, v87\n"
"v_mfma_f32_16x16x32_bf16 v[208:211], v[136:139], a[140:143], v[208:211]\n"
"ds_read_b64_tr_b16 v[132:133], v157 offset:30528\n"
"ds_read_b64_tr_b16 v[134:135], v157 offset:31040\n"
"v_fmac_f32 v76, s86, v80\n"
"v_mfma_f32_16x16x32_bf16 v[212:215], v[44:47], a[136:139], v[212:215]\n"
"ds_read_b64_tr_b16 v[136:137], v4 offset:22528\n"
"ds_read_b64_tr_b16 v[138:139], v4 offset:22784\n"
"v_fmac_f32 v77, s86, v81\n"
"v_mfma_f32_16x16x32_bf16 v[216:219], v[44:47], a[140:143], v[216:219]\n"
"s_add_u32 s44, s65, 86016\n"
"buffer_load_dwordx4 a[144:147], v162, s[28:31], s44 offen offset:0 sc1\n"
"v_fmac_f32 v78, s86, v82\n"
"v_mfma_f32_16x16x32_bf16 v[224:227], v[48:51], a[136:139], v[224:227]\n"
"s_waitcnt vmcnt(14) lgkmcnt(6)\n"
"s_barrier\n"
"v_fmac_f32 v79, s86, v83\n"
"v_cvt_pk_bf16_f32 v84, v76, v77\n"
"v_mfma_f32_16x16x32_bf16 v[188:191], v[52:55], a[152:155], v[188:191]\n"
"ds_read_b64_tr_b16 v[44:45], v156 offset:34816\n"
"ds_read_b64_tr_b16 v[46:47], v156 offset:35328\n"
"v_cvt_pk_bf16_f32 v85, v78, v79\n"
"v_mfma_f32_16x16x32_bf16 v[192:195], v[52:55], a[156:159], v[192:195]\n"
"ds_read_b64_tr_b16 v[48:49], v157 offset:34816\n"
"ds_read_b64_tr_b16 v[50:51], v157 offset:35328\n"
"v_mfma_f32_16x16x32_bf16 v[196:199], v[56:59], a[152:155], v[196:199]\n"
"buffer_load_dwordx4 a[148:151], v163, s[28:31], s44 offen offset:0 sc1\n"
"v_mfma_f32_16x16x32_bf16 v[200:203], v[56:59], a[156:159], v[200:203]\n"
"ds_read_b64_tr_b16 v[52:53], v1 offset:24576\n"
"ds_read_b64_tr_b16 v[54:55], v1 offset:24832\n"
"v_mfma_f32_16x16x32_bf16 v[220:223], v[124:127], a[152:155], v[220:223]\n"
"s_waitcnt lgkmcnt(6)\n"
"ds_read_b64_tr_b16 v[56:57], v156 offset:34880\n"
"ds_read_b64_tr_b16 v[58:59], v156 offset:35392\n"
"v_mfma_f32_16x16x32_bf16 v[204:207], v[128:131], a[152:155], v[204:207]\n"
"s_add_u32 m0, s54, 74240\n"
"ds_read_b64_tr_b16 v[124:125], v157 offset:34880\n"
"ds_read_b64_tr_b16 v[126:127], v157 offset:35392\n"
"v_mfma_f32_16x16x32_bf16 v[208:211], v[128:131], a[156:159], v[208:211]\n"
"s_add_u32 s84, s12, 2359296\n"
"buffer_load_dwordx4 v158, s[36:39], s84 offen offset:0 lds\n"
"v_mfma_f32_16x16x32_bf16 v[212:215], v[132:135], a[152:155], v[212:215]\n"
"ds_read_b64_tr_b16 v[128:129], v4 offset:24576\n"
"ds_read_b64_tr_b16 v[130:131], v4 offset:24832\n"
"v_mfma_f32_16x16x32_bf16 v[216:219], v[132:135], a[156:159], v[216:219]\n"
"s_add_u32 m0, s54, 75328\n"
"buffer_store_dwordx2 v[84:85], v179, s[28:31], s89 offen offset:512 sc1\n"
"v_mfma_f32_16x16x32_bf16 v[224:227], v[136:139], a[152:155], v[224:227]\n"
"s_waitcnt lgkmcnt(6)\n"
"buffer_load_dwordx4 v159, s[36:39], s84 offen offset:0 lds\n"
"v_mfma_f32_16x16x32_bf16 v[188:191], v[44:47], a[40:43], v[188:191]\n"
"ds_read_b64_tr_b16 v[132:133], v156 offset:39168\n"
"ds_read_b64_tr_b16 v[134:135], v156 offset:39680\n"
"v_add_f32 v100, v100, v104\n"
"v_mfma_f32_16x16x32_bf16 v[192:195], v[44:47], a[44:47], v[192:195]\n"
"s_add_u32 m0, s54, 82944\n"
"ds_read_b64_tr_b16 v[136:137], v157 offset:39168\n"
"ds_read_b64_tr_b16 v[138:139], v157 offset:39680\n"
"v_add_f32 v108, v108, v112\n"
"v_mfma_f32_16x16x32_bf16 v[196:199], v[48:51], a[40:43], v[196:199]\n"
"s_add_u32 s84, s13, 2359296\n"
"buffer_load_dwordx4 v158, s[36:39], s84 offen offset:0 lds\n"
"v_add_f32 v101, v101, v105\n"
"v_mfma_f32_16x16x32_bf16 v[200:203], v[48:51], a[44:47], v[200:203]\n"
"s_add_u32 m0, s54, 84032\n"
"ds_read_b64_tr_b16 v[44:45], v1 offset:26624\n"
"ds_read_b64_tr_b16 v[46:47], v1 offset:26880\n"
"v_add_f32 v109, v109, v113\n"
"v_mfma_f32_16x16x32_bf16 v[220:223], v[52:55], a[40:43], v[220:223]\n"
"s_waitcnt lgkmcnt(6)\n"
"ds_read_b64_tr_b16 v[48:49], v156 offset:39232\n"
"ds_read_b64_tr_b16 v[50:51], v156 offset:39744\n"
"v_add_f32 v102, v102, v106\n"
"v_mfma_f32_16x16x32_bf16 v[204:207], v[56:59], a[40:43], v[204:207]\n"
"buffer_load_dwordx4 v159, s[36:39], s84 offen offset:0 lds\n"
"v_add_f32 v110, v110, v114\n"
"v_mfma_f32_16x16x32_bf16 v[208:211], v[56:59], a[44:47], v[208:211]\n"
"ds_read_b64_tr_b16 v[52:53], v157 offset:39232\n"
"ds_read_b64_tr_b16 v[54:55], v157 offset:39744\n"
"v_add_f32 v103, v103, v107\n"
"v_mfma_f32_16x16x32_bf16 v[212:215], v[124:127], a[40:43], v[212:215]\n"
"s_add_u32 m0, s53, 155648\n"
"ds_read_b64_tr_b16 v[56:57], v4 offset:26624\n"
"ds_read_b64_tr_b16 v[58:59], v4 offset:26880\n"
"v_add_f32 v111, v111, v115\n"
"v_mfma_f32_16x16x32_bf16 v[216:219], v[124:127], a[44:47], v[216:219]\n"
"s_add_u32 s84, s12, 2359296\n"
"buffer_load_dwordx4 v171, s[36:39], s84 offen offset:0 lds\n"
"v_lshlrev_b32 v107, 16, v116\n"
"v_mfma_f32_16x16x32_bf16 v[224:227], v[128:131], a[40:43], v[224:227]\n"
"s_waitcnt lgkmcnt(6)\n"
"v_and_b32 v106, s81, v116\n"
"v_lshlrev_b32 v105, 16, v117\n"
"v_and_b32 v104, s81, v117\n"
"v_mfma_f32_16x16x32_bf16 v[188:191], v[132:135], a[56:59], v[188:191]\n"
"ds_read_b64_tr_b16 v[124:125], v7 offset:0\n"
"ds_read_b64_tr_b16 v[126:127], v7 offset:512\n"
"v_add_f32 v100, v100, v108\n"
"v_mfma_f32_16x16x32_bf16 v[192:195], v[132:135], a[60:63], v[192:195]\n"
"s_add_u32 m0, s53, 159744\n"
"ds_read_b64_tr_b16 v[128:129], v9 offset:0\n"
"ds_read_b64_tr_b16 v[130:131], v9 offset:512\n"
"v_add_f32 v101, v101, v109\n"
"v_mfma_f32_16x16x32_bf16 v[196:199], v[136:139], a[56:59], v[196:199]\n"
"s_add_u32 s84, s13, 2359296\n"
"buffer_load_dwordx4 v171, s[36:39], s84 offen offset:0 lds\n"
"v_add_f32 v102, v102, v110\n"
"v_mfma_f32_16x16x32_bf16 v[200:203], v[136:139], a[60:63], v[200:203]\n"
"ds_read_b64_tr_b16 v[132:133], v1 offset:28672\n"
"ds_read_b64_tr_b16 v[134:135], v1 offset:28928\n"
"v_add_f32 v103, v103, v111\n"
"v_mfma_f32_16x16x32_bf16 v[220:223], v[44:47], a[56:59], v[220:223]\n"
"s_waitcnt lgkmcnt(6)\n"
"ds_read_b64_tr_b16 v[136:137], v7 offset:64\n"
"ds_read_b64_tr_b16 v[138:139], v7 offset:576\n"
"v_fmac_f32 v100, s87, v104\n"
"v_mfma_f32_16x16x32_bf16 v[204:207], v[48:51], a[56:59], v[204:207]\n"
"ds_read_b64_tr_b16 v[44:45], v9 offset:64\n"
"ds_read_b64_tr_b16 v[46:47], v9 offset:576\n"
"v_fmac_f32 v101, s87, v105\n"
"v_mfma_f32_16x16x32_bf16 v[208:211], v[48:51], a[60:63], v[208:211]\n"
"s_add_u32 s44, s66, 12288\n"
"buffer_load_dwordx4 a[160:163], v162, s[28:31], s44 offen offset:0 sc1\n"
"v_fmac_f32 v102, s87, v106\n"
"v_mfma_f32_16x16x32_bf16 v[212:215], v[52:55], a[56:59], v[212:215]\n"
"ds_read_b64_tr_b16 v[48:49], v4 offset:28672\n"
"ds_read_b64_tr_b16 v[50:51], v4 offset:28928\n"
"v_fmac_f32 v103, s87, v107\n"
"v_mfma_f32_16x16x32_bf16 v[216:219], v[52:55], a[60:63], v[216:219]\n"
"v_cvt_pk_bf16_f32 v108, v100, v101\n"
"v_cvt_pk_bf16_f32 v109, v102, v103\n"
"v_mfma_f32_16x16x32_bf16 v[224:227], v[56:59], a[56:59], v[224:227]\n"
"s_waitcnt lgkmcnt(6)\n"
"buffer_load_dwordx4 a[164:167], v163, s[28:31], s44 offen offset:0 sc1\n"
"v_mfma_f32_16x16x32_bf16 v[188:191], v[124:127], a[200:203], v[188:191]\n"
"ds_read_b64_tr_b16 v[52:53], v7 offset:4352\n"
"ds_read_b64_tr_b16 v[54:55], v7 offset:4864\n"
"v_mfma_f32_16x16x32_bf16 v[192:195], v[124:127], a[204:207], v[192:195]\n"
"ds_read_b64_tr_b16 v[56:57], v9 offset:4352\n"
"ds_read_b64_tr_b16 v[58:59], v9 offset:4864\n"
"v_mfma_f32_16x16x32_bf16 v[196:199], v[128:131], a[200:203], v[196:199]\n"
"s_add_u32 s44, s66, 86016\n"
"buffer_load_dwordx4 a[176:179], v162, s[28:31], s44 offen offset:0 sc1\n"
"v_mfma_f32_16x16x32_bf16 v[200:203], v[128:131], a[204:207], v[200:203]\n"
"ds_read_b64_tr_b16 v[124:125], v1 offset:30720\n"
"ds_read_b64_tr_b16 v[126:127], v1 offset:30976\n"
"v_mfma_f32_16x16x32_bf16 v[220:223], v[132:135], a[200:203], v[220:223]\n"
"s_waitcnt lgkmcnt(6)\n"
"ds_read_b64_tr_b16 v[128:129], v7 offset:4416\n"
"ds_read_b64_tr_b16 v[130:131], v7 offset:4928\n"
"v_mfma_f32_16x16x32_bf16 v[204:207], v[136:139], a[200:203], v[204:207]\n"
"buffer_load_dwordx4 a[180:183], v163, s[28:31], s44 offen offset:0 sc1\n"
"v_mfma_f32_16x16x32_bf16 v[208:211], v[136:139], a[204:207], v[208:211]\n"
"ds_read_b64_tr_b16 v[132:133], v9 offset:4416\n"
"ds_read_b64_tr_b16 v[134:135], v9 offset:4928\n"
"v_mfma_f32_16x16x32_bf16 v[212:215], v[44:47], a[200:203], v[212:215]\n"
"ds_read_b64_tr_b16 v[136:137], v4 offset:30720\n"
"ds_read_b64_tr_b16 v[138:139], v4 offset:30976\n"
"v_mfma_f32_16x16x32_bf16 v[216:219], v[44:47], a[204:207], v[216:219]\n"
"s_add_u32 s44, s67, 12288\n"
"buffer_load_dwordx4 a[192:195], v162, s[28:31], s44 offen offset:0 sc1\n"
"v_mfma_f32_16x16x32_bf16 v[224:227], v[48:51], a[200:203], v[224:227]\n"
"s_waitcnt vmcnt(21) lgkmcnt(6)\n"
"s_barrier\n"
"buffer_store_dwordx2 v[108:109], v179, s[28:31], s49 offen offset:0 sc1\n"
"v_mfma_f32_16x16x32_bf16 v[188:191], v[52:55], a[216:219], v[188:191]\n"
"s_lshl_b32 s56, s10, 2\n"
"ds_read_b64_tr_b16 v[44:45], v7 offset:8704\n"
"ds_read_b64_tr_b16 v[46:47], v7 offset:9216\n"
"v_lshrrev_b32 v184, 2, v0\n"
"v_mfma_f32_16x16x32_bf16 v[192:195], v[52:55], a[220:223], v[192:195]\n"
"s_add_u32 s56, s56, s8\n"
"ds_read_b64_tr_b16 v[48:49], v9 offset:8704\n"
"ds_read_b64_tr_b16 v[50:51], v9 offset:9216\n"
"v_mfma_f32_16x16x32_bf16 v[196:199], v[56:59], a[216:219], v[196:199]\n"
"s_lshl_b32 s56, s56, 4\n"
"buffer_load_dwordx4 a[196:199], v163, s[28:31], s44 offen offset:0 sc1\n"
"v_mfma_f32_16x16x32_bf16 v[200:203], v[56:59], a[220:223], v[200:203]\n"
"ds_read_b64_tr_b16 v[52:53], v1 offset:32768\n"
"ds_read_b64_tr_b16 v[54:55], v1 offset:33024\n"
"v_add_u32 v183, s56, v184\n"
"v_mfma_f32_16x16x32_bf16 v[220:223], v[124:127], a[216:219], v[220:223]\n"
"s_waitcnt lgkmcnt(6)\n"
"ds_read_b64_tr_b16 v[56:57], v7 offset:8768\n"
"ds_read_b64_tr_b16 v[58:59], v7 offset:9280\n"
"s_lshr_b32 s56, s9, 1\n"
"v_mfma_f32_16x16x32_bf16 v[204:207], v[128:131], a[216:219], v[204:207]\n"
"s_add_u32 m0, s54, 91648\n"
"ds_read_b64_tr_b16 v[124:125], v9 offset:8768\n"
"ds_read_b64_tr_b16 v[126:127], v9 offset:9280\n"
"s_sub_u32 s57, 7, s56\n"
"v_mfma_f32_16x16x32_bf16 v[208:211], v[128:131], a[220:223], v[208:211]\n"
"s_add_u32 s84, s14, 2359296\n"
"buffer_load_dwordx4 v158, s[36:39], s84 offen offset:0 lds\n"
"s_min_u32 s56, s56, s57\n"
"v_mfma_f32_16x16x32_bf16 v[212:215], v[132:135], a[216:219], v[212:215]\n"
"s_lshr_b32 s57, s10, 1\n"
"ds_read_b64_tr_b16 v[128:129], v4 offset:32768\n"
"ds_read_b64_tr_b16 v[130:131], v4 offset:33024\n"
"v_cmp_lt_u32 s[50:51], 15, v184\n"
"v_mfma_f32_16x16x32_bf16 v[216:219], v[132:135], a[220:223], v[216:219]\n"
"s_add_u32 m0, s54, 92736\n"
"s_lshl_b32 s57, s57, 7\n"
"v_mfma_f32_16x16x32_bf16 v[224:227], v[136:139], a[216:219], v[224:227]\n"
"s_waitcnt lgkmcnt(6)\n"
"buffer_load_dwordx4 v159, s[36:39], s84 offen offset:0 lds\n"
"s_add_u32 s56, s56, s57\n"
"v_mfma_f32_16x16x32_bf16 v[188:191], v[44:47], a[232:235], v[188:191]\n"
"s_sub_u32 s56, s56, 64\n"
"ds_read_b64_tr_b16 v[132:133], v7 offset:13056\n"
"ds_read_b64_tr_b16 v[134:135], v7 offset:13568\n"
"s_lshl_b32 s57, s9, 4\n"
"v_mfma_f32_16x16x32_bf16 v[192:195], v[44:47], a[236:239], v[192:195]\n"
"s_add_u32 m0, s54, 100352\n"
"ds_read_b64_tr_b16 v[136:137], v9 offset:13056\n"
"ds_read_b64_tr_b16 v[138:139], v9 offset:13568\n"
"v_lshl_add_u32 v252, v184, 2, s56\n"
"v_mfma_f32_16x16x32_bf16 v[196:199], v[48:51], a[232:235], v[196:199]\n"
"s_add_u32 s84, s15, 2359296\n"
"buffer_load_dwordx4 v158, s[36:39], s84 offen offset:0 lds\n"
"v_cndmask_b32 v183, v183, v252, s[50:51]\n"
"v_mfma_f32_16x16x32_bf16 v[200:203], v[48:51], a[236:239], v[200:203]\n"
"s_add_u32 m0, s54, 101440\n"
"ds_read_b64_tr_b16 v[44:45], v1 offset:34816\n"
"ds_read_b64_tr_b16 v[46:47], v1 offset:35072\n"
"s_sub_u32 s57, s57, 48\n"
"v_mfma_f32_16x16x32_bf16 v[220:223], v[52:55], a[232:235], v[220:223]\n"
"s_waitcnt lgkmcnt(6)\n"
"ds_read_b64_tr_b16 v[48:49], v7 offset:13120\n"
"ds_read_b64_tr_b16 v[50:51], v7 offset:13632\n"
"v_add_u32 v252, s57, v184\n"
"v_mfma_f32_16x16x32_bf16 v[204:207], v[56:59], a[232:235], v[204:207]\n"
"s_andn2_b32 s62, s33, 6\n"
"buffer_load_dwordx4 v159, s[36:39], s84 offen offset:0 lds\n"
"v_cmp_lt_u32 s[50:51], 47, v184\n"
"v_mfma_f32_16x16x32_bf16 v[208:211], v[56:59], a[236:239], v[208:211]\n"
"s_lshl_b32 s62, s62, 10\n"
"ds_read_b64_tr_b16 v[52:53], v9 offset:13120\n"
"ds_read_b64_tr_b16 v[54:55], v9 offset:13632\n"
"v_cndmask_b32 v183, v183, v252, s[50:51]\n"
"v_mfma_f32_16x16x32_bf16 v[212:215], v[124:127], a[232:235], v[212:215]\n"
"s_add_u32 m0, s53, 20480\n"
"ds_read_b64_tr_b16 v[56:57], v4 offset:34816\n"
"ds_read_b64_tr_b16 v[58:59], v4 offset:35072\n"
"v_lshlrev_b32 v183, 10, v183\n"
"v_mfma_f32_16x16x32_bf16 v[216:219], v[124:127], a[236:239], v[216:219]\n"
"s_add_u32 s84, s14, 2359296\n"
"buffer_load_dwordx4 v171, s[36:39], s84 offen offset:0 lds\n"
"v_bfe_u32 v170, v0, 0, 2\n"
"v_mfma_f32_16x16x32_bf16 v[224:227], v[128:131], a[232:235], v[224:227]\n"
"s_waitcnt lgkmcnt(6)\n"
"v_lshlrev_b32 v170, 11, v170\n"
"v_and_b32 v184, 15, v0\n"
"v_bfe_u32 v180, v0, 4, 2\n"
"v_mfma_f32_16x16x32_bf16 v[188:191], v[132:135], a[248:251], v[188:191]\n"
"ds_read_b64_tr_b16 v[124:125], v156 offset:0\n"
"ds_read_b64_tr_b16 v[126:127], v156 offset:512\n"
"v_add_u32 v170, s62, v170\n"
"v_mfma_f32_16x16x32_bf16 v[192:195], v[132:135], a[252:255], v[192:195]\n"
"s_add_u32 m0, s53, 24576\n"
"ds_read_b64_tr_b16 v[128:129], v157 offset:0\n"
"ds_read_b64_tr_b16 v[130:131], v157 offset:512\n"
"v_lshlrev_b32 v180, 3, v180\n"
"v_mfma_f32_16x16x32_bf16 v[196:199], v[136:139], a[248:251], v[196:199]\n"
"s_add_u32 s84, s15, 2359296\n"
"buffer_load_dwordx4 v171, s[36:39], s84 offen offset:0 lds\n"
"v_mul_u32_u24 v254, 1152, v184\n"
"v_mfma_f32_16x16x32_bf16 v[200:203], v[136:139], a[252:255], v[200:203]\n"
"s_lshl_b32 s58, s11, 5\n"
"ds_read_b64_tr_b16 v[132:133], v3 offset:20480\n"
"ds_read_b64_tr_b16 v[134:135], v3 offset:20736\n"
"v_add_u32 v180, v180, v254\n"
"v_mfma_f32_16x16x32_bf16 v[220:223], v[44:47], a[248:251], v[220:223]\n"
"s_waitcnt lgkmcnt(6)\n"
"ds_read_b64_tr_b16 v[136:137], v156 offset:64\n"
"ds_read_b64_tr_b16 v[138:139], v156 offset:576\n"
"v_lshlrev_b32 v172, 4, v184\n"
"v_mfma_f32_16x16x32_bf16 v[204:207], v[48:51], a[248:251], v[204:207]\n"
"s_lshl_b32 s59, s52, 12\n"
"ds_read_b64_tr_b16 v[44:45], v157 offset:64\n"
"ds_read_b64_tr_b16 v[46:47], v157 offset:576\n"
"v_bfe_u32 v184, v0, 2, 4\n"
"v_mfma_f32_16x16x32_bf16 v[208:211], v[48:51], a[252:255], v[208:211]\n"
"s_add_u32 s44, s67, 86016\n"
"buffer_load_dwordx4 a[208:211], v162, s[28:31], s44 offen offset:0 sc1\n"
"v_and_b32 v173, 3, v0\n"
"v_mfma_f32_16x16x32_bf16 v[212:215], v[52:55], a[248:251], v[212:215]\n"
"ds_read_b64_tr_b16 v[48:49], v6 offset:20480\n"
"ds_read_b64_tr_b16 v[50:51], v6 offset:20736\n"
"v_lshlrev_b32 v173, 4, v173\n"
"v_mfma_f32_16x16x32_bf16 v[216:219], v[52:55], a[252:255], v[216:219]\n"
"s_and_b32 s60, s52, 1\n"
"v_mul_u32_u24 v254, 1152, v184\n"
"v_xor_b32 v252, s58, v3\n"
"v_mfma_f32_16x16x32_bf16 v[224:227], v[56:59], a[248:251], v[224:227]\n"
"s_waitcnt lgkmcnt(6)\n"
"buffer_load_dwordx4 a[212:215], v163, s[28:31], s44 offen offset:0 sc1\n"
"v_add_u32 v173, v173, v254\n"
"v_mfma_f32_16x16x32_bf16 v[188:191], v[124:127], v[20:23], v[188:191]\n"
"s_lshl_b32 s61, s60, 8\n"
"ds_read_b64_tr_b16 v[52:53], v156 offset:4352\n"
"ds_read_b64_tr_b16 v[54:55], v156 offset:4864\n"
"v_lshlrev_b32 v184, 6, v184\n"
"v_mfma_f32_16x16x32_bf16 v[192:195], v[124:127], v[24:27], v[192:195]\n"
"s_andn2_b32 s58, s33, 6\n"
"ds_read_b64_tr_b16 v[56:57], v157 offset:4352\n"
"ds_read_b64_tr_b16 v[58:59], v157 offset:4864\n"
"v_add_u32 v252, s59, v252\n"
"v_mfma_f32_16x16x32_bf16 v[196:199], v[128:131], v[20:23], v[196:199]\n"
"s_add_u32 s44, s94, 0\n"
"buffer_load_dwordx4 a[224:227], v162, s[28:31], s44 offen offset:0 sc1\n"
"v_xor_b32 v253, 32, v252\n"
"v_mfma_f32_16x16x32_bf16 v[200:203], v[128:131], v[24:27], v[200:203]\n"
"s_lshl_b32 s59, s8, 2\n"
"ds_read_b64_tr_b16 v[124:125], v3 offset:22528\n"
"ds_read_b64_tr_b16 v[126:127], v3 offset:22784\n"
"v_add_u32 v254, s61, v252\n"
"v_mfma_f32_16x16x32_bf16 v[220:223], v[132:135], v[20:23], v[220:223]\n"
"s_waitcnt lgkmcnt(6)\n"
"ds_read_b64_tr_b16 v[128:129], v156 offset:4416\n"
"ds_read_b64_tr_b16 v[130:131], v156 offset:4928\n"
"s_add_u32 s59, s59, s52\n"
"v_mfma_f32_16x16x32_bf16 v[204:207], v[136:139], v[20:23], v[204:207]\n"
"s_lshl_b32 s59, s59, 11\n"
"buffer_load_dwordx4 a[228:231], v163, s[28:31], s44 offen offset:0 sc1\n"
"s_xor_b32 s56, s8, 2\n"
"v_mfma_f32_16x16x32_bf16 v[208:211], v[136:139], v[24:27], v[208:211]\n"
"s_add_u32 s59, s59, 19271680\n"
"ds_read_b64_tr_b16 v[132:133], v157 offset:4416\n"
"ds_read_b64_tr_b16 v[134:135], v157 offset:4928\n"
"s_andn2_b32 s56, s56, 1\n"
"v_mfma_f32_16x16x32_bf16 v[212:215], v[44:47], v[20:23], v[212:215]\n"
"s_lshl_b32 s61, s56, 1\n"
"ds_read_b64_tr_b16 v[136:137], v6 offset:22528\n"
"ds_read_b64_tr_b16 v[138:139], v6 offset:22784\n"
"v_mfma_f32_16x16x32_bf16 v[216:219], v[44:47], v[24:27], v[216:219]\n"
"s_add_u32 s44, s94, 73728\n"
"buffer_load_dwordx4 a[240:243], v162, s[28:31], s44 offen offset:0 sc1\n"
"s_or_b32 s61, s61, s58\n"
"v_mfma_f32_16x16x32_bf16 v[224:227], v[48:51], v[20:23], v[224:227]\n"
"s_waitcnt vmcnt(18) lgkmcnt(6)\n"
"s_barrier\n"
"s_mul_i32 s61, s61, 36864\n"
"s_xor_b32 s56, s8, 1\n"
"v_mfma_f32_16x16x32_bf16 v[188:191], v[52:55], v[36:39], v[188:191]\n"
"s_add_u32 s77, s61, s59\n"
"ds_read_b64_tr_b16 v[44:45], v156 offset:8704\n"
"ds_read_b64_tr_b16 v[46:47], v156 offset:9216\n"
"v_mfma_f32_16x16x32_bf16 v[192:195], v[52:55], v[40:43], v[192:195]\n"
"s_add_u32 s78, s77, 73728\n"
"ds_read_b64_tr_b16 v[48:49], v157 offset:8704\n"
"ds_read_b64_tr_b16 v[50:51], v157 offset:9216\n"
"s_lshl_b32 s61, s56, 1\n"
"v_mfma_f32_16x16x32_bf16 v[196:199], v[56:59], v[36:39], v[196:199]\n"
"s_or_b32 s61, s61, s58\n"
"buffer_load_dwordx4 a[244:247], v163, s[28:31], s44 offen offset:0 sc1\n"
"v_mfma_f32_16x16x32_bf16 v[200:203], v[56:59], v[40:43], v[200:203]\n"
"s_mul_i32 s61, s61, 36864\n"
"ds_read_b64_tr_b16 v[52:53], v3 offset:24576\n"
"ds_read_b64_tr_b16 v[54:55], v3 offset:24832\n"
"v_mfma_f32_16x16x32_bf16 v[220:223], v[124:127], v[36:39], v[220:223]\n"
"s_waitcnt lgkmcnt(6)\n"
"ds_read_b64_tr_b16 v[56:57], v156 offset:8768\n"
"ds_read_b64_tr_b16 v[58:59], v156 offset:9280\n"
"s_add_u32 s79, s61, s59\n"
"v_mfma_f32_16x16x32_bf16 v[204:207], v[128:131], v[36:39], v[204:207]\n"
"s_add_u32 m0, s54, 0\n"
"ds_read_b64_tr_b16 v[124:125], v157 offset:8768\n"
"ds_read_b64_tr_b16 v[126:127], v157 offset:9280\n"
"s_lshl_b32 s59, s8, 10\n"
"v_mfma_f32_16x16x32_bf16 v[208:211], v[128:131], v[40:43], v[208:211]\n"
"s_add_u32 s84, s12, 2396160\n"
"buffer_load_dwordx4 v158, s[36:39], s84 offen offset:0 lds\n"
"s_add_u32 s59, s59, 19304448\n"
"v_mfma_f32_16x16x32_bf16 v[212:215], v[132:135], v[36:39], v[212:215]\n"
"s_lshl_b32 s61, s52, 1\n"
"ds_read_b64_tr_b16 v[128:129], v6 offset:24576\n"
"ds_read_b64_tr_b16 v[130:131], v6 offset:24832\n"
"v_mfma_f32_16x16x32_bf16 v[216:219], v[132:135], v[40:43], v[216:219]\n"
"s_add_u32 m0, s54, 1088\n"
"s_or_b32 s61, s61, s58\n"
"v_mfma_f32_16x16x32_bf16 v[224:227], v[136:139], v[36:39], v[224:227]\n"
"s_waitcnt lgkmcnt(6)\n"
"buffer_load_dwordx4 v159, s[36:39], s84 offen offset:0 lds\n"
"s_mul_i32 s61, s61, 36864\n"
"v_mfma_f32_16x16x32_bf16 v[188:191], v[44:47], a[168:171], v[188:191]\n"
"s_add_u32 s80, s61, s59\n"
"ds_read_b64_tr_b16 v[132:133], v156 offset:13056\n"
"ds_read_b64_tr_b16 v[134:135], v156 offset:13568\n"
"s_lshl_b32 s58, s9, 2\n"
"v_mfma_f32_16x16x32_bf16 v[192:195], v[44:47], a[172:175], v[192:195]\n"
"s_add_u32 m0, s54, 8704\n"
"ds_read_b64_tr_b16 v[136:137], v157 offset:13056\n"
"ds_read_b64_tr_b16 v[138:139], v157 offset:13568\n"
"s_add_u32 s59, s58, s52\n"
"v_mfma_f32_16x16x32_bf16 v[196:199], v[48:51], a[168:171], v[196:199]\n"
"s_add_u32 s84, s13, 2396160\n"
"buffer_load_dwordx4 v158, s[36:39], s84 offen offset:0 lds\n"
"s_lshl_b32 s59, s59, 5\n"
"v_mfma_f32_16x16x32_bf16 v[200:203], v[48:51], a[172:175], v[200:203]\n"
"s_add_u32 m0, s54, 9792\n"
"ds_read_b64_tr_b16 v[44:45], v3 offset:26624\n"
"ds_read_b64_tr_b16 v[46:47], v3 offset:26880\n"
"s_add_u32 s58, s58, s8\n"
"v_mfma_f32_16x16x32_bf16 v[220:223], v[52:55], a[168:171], v[220:223]\n"
"s_waitcnt lgkmcnt(6)\n"
"ds_read_b64_tr_b16 v[48:49], v156 offset:13120\n"
"ds_read_b64_tr_b16 v[50:51], v156 offset:13632\n"
"s_lshl_b32 s58, s58, 5\n"
"v_mfma_f32_16x16x32_bf16 v[204:207], v[56:59], a[168:171], v[204:207]\n"
"s_lshl_b32 s61, s10, 2\n"
"buffer_load_dwordx4 v159, s[36:39], s84 offen offset:0 lds\n"
"s_lshr_b32 s62, s10, 1\n"
"v_mfma_f32_16x16x32_bf16 v[208:211], v[56:59], a[172:175], v[208:211]\n"
"s_add_u32 s61, s61, s8\n"
"ds_read_b64_tr_b16 v[52:53], v157 offset:13120\n"
"ds_read_b64_tr_b16 v[54:55], v157 offset:13632\n"
"s_lshl_b32 s62, s62, 5\n"
"v_mfma_f32_16x16x32_bf16 v[212:215], v[124:127], a[168:171], v[212:215]\n"
"s_add_u32 m0, s53, 28672\n"
"ds_read_b64_tr_b16 v[56:57], v6 offset:26624\n"
"ds_read_b64_tr_b16 v[58:59], v6 offset:26880\n"
"s_lshl_b32 s61, s61, 5\n"
"v_mfma_f32_16x16x32_bf16 v[216:219], v[124:127], a[172:175], v[216:219]\n"
"s_add_u32 s84, s12, 2396160\n"
"buffer_load_dwordx4 v171, s[36:39], s84 offen offset:0 lds\n"
"s_add_u32 s62, s62, 512\n"
"v_mfma_f32_16x16x32_bf16 v[224:227], v[128:131], a[168:171], v[224:227]\n"
"s_waitcnt lgkmcnt(6)\n"
"s_lshl_b32 s56, s61, 11\n"
"s_lshl_b32 s63, s11, 4\n"
"v_mfma_f32_16x16x32_bf16 v[188:191], v[132:135], a[184:187], v[188:191]\n"
"s_add_u32 s56, s56, s59\n"
"ds_read_b64_tr_b16 v[124:125], v156 offset:17408\n"
"ds_read_b64_tr_b16 v[126:127], v156 offset:17920\n"
"v_mfma_f32_16x16x32_bf16 v[192:195], v[132:135], a[188:191], v[192:195]\n"
"s_add_u32 m0, s53, 32768\n"
"ds_read_b64_tr_b16 v[128:129], v157 offset:17408\n"
"ds_read_b64_tr_b16 v[130:131], v157 offset:17920\n"
"s_lshl_b32 s56, s56, 1\n"
"v_mfma_f32_16x16x32_bf16 v[196:199], v[136:139], a[184:187], v[196:199]\n"
"s_add_u32 s84, s13, 2396160\n"
"buffer_load_dwordx4 v171, s[36:39], s84 offen offset:0 lds\n"
"s_add_u32 s24, s56, 28708864\n"
"v_mfma_f32_16x16x32_bf16 v[200:203], v[136:139], a[188:191], v[200:203]\n"
"s_lshl_b32 s56, s62, 11\n"
"ds_read_b64_tr_b16 v[132:133], v3 offset:28672\n"
"ds_read_b64_tr_b16 v[134:135], v3 offset:28928\n"
"s_add_u32 s59, s59, s63\n"
"v_mfma_f32_16x16x32_bf16 v[220:223], v[44:47], a[184:187], v[220:223]\n"
"s_waitcnt lgkmcnt(6)\n"
"ds_read_b64_tr_b16 v[136:137], v156 offset:17472\n"
"ds_read_b64_tr_b16 v[138:139], v156 offset:17984\n"
"s_add_u32 s56, s56, s58\n"
"v_mfma_f32_16x16x32_bf16 v[204:207], v[48:51], a[184:187], v[204:207]\n"
"s_lshl_b32 s56, s56, 1\n"
"ds_read_b64_tr_b16 v[44:45], v157 offset:17472\n"
"ds_read_b64_tr_b16 v[46:47], v157 offset:17984\n"
"s_add_u32 s58, s58, s63\n"
"v_mfma_f32_16x16x32_bf16 v[208:211], v[48:51], a[188:191], v[208:211]\n"
"s_add_u32 s44, s95, 0\n"
"buffer_load_dwordx4 v[12:15], v162, s[28:31], s44 offen offset:0 sc1\n"
"s_add_u32 s88, s56, 28708864\n"
"v_mfma_f32_16x16x32_bf16 v[212:215], v[52:55], a[184:187], v[212:215]\n"
"s_lshl_b32 s56, s59, 6\n"
"ds_read_b64_tr_b16 v[48:49], v6 offset:28672\n"
"ds_read_b64_tr_b16 v[50:51], v6 offset:28928\n"
"s_lshl_b32 s61, s61, 1\n"
"v_mfma_f32_16x16x32_bf16 v[216:219], v[52:55], a[188:191], v[216:219]\n"
"s_add_u32 s89, s56, 266240\n"
"s_lshl_b32 s62, s62, 1\n"
"v_mfma_f32_16x16x32_bf16 v[224:227], v[56:59], a[184:187], v[224:227]\n"
"s_waitcnt lgkmcnt(6)\n"
"buffer_load_dwordx4 v[16:19], v163, s[28:31], s44 offen offset:0 sc1\n"
"s_xor_b32 s90, s89, 1024\n"
"v_mfma_f32_16x16x32_bf16 v[188:191], v[124:127], a[0:3], v[188:191]\n"
"s_lshl_b32 s56, s58, 6\n"
"ds_read_b64_tr_b16 v[52:53], v156 offset:21760\n"
"ds_read_b64_tr_b16 v[54:55], v156 offset:22272\n"
"v_mfma_f32_16x16x32_bf16 v[192:195], v[124:127], a[4:7], v[192:195]\n"
"s_add_u32 m0, s53, 36864\n"
"ds_read_b64_tr_b16 v[56:57], v157 offset:21760\n"
"ds_read_b64_tr_b16 v[58:59], v157 offset:22272\n"
"s_add_u32 s96, s56, 266240\n"
"v_mfma_f32_16x16x32_bf16 v[196:199], v[128:131], a[0:3], v[196:199]\n"
"s_add_u32 s84, s12, 0\n"
"buffer_load_dwordx4 v160, s[36:39], s84 offen offset:0 lds\n"
"s_mul_i32 s56, s59, 1152\n"
"v_mfma_f32_16x16x32_bf16 v[200:203], v[128:131], a[4:7], v[200:203]\n"
"s_add_u32 m0, s53, 40960\n"
"ds_read_b64_tr_b16 v[124:125], v3 offset:30720\n"
"ds_read_b64_tr_b16 v[126:127], v3 offset:30976\n"
"s_add_u32 s71, s56, s61\n"
"v_mfma_f32_16x16x32_bf16 v[220:223], v[132:135], a[0:3], v[220:223]\n"
"s_waitcnt lgkmcnt(6)\n"
"ds_read_b64_tr_b16 v[128:129], v156 offset:21824\n"
"ds_read_b64_tr_b16 v[130:131], v156 offset:22336\n"
"s_xor_b32 s59, s59, 16\n"
"v_mfma_f32_16x16x32_bf16 v[204:207], v[136:139], a[0:3], v[204:207]\n"
"s_mul_i32 s56, s59, 1152\n"
"buffer_load_dwordx4 v181, s[36:39], s84 offen offset:0 lds\n"
"s_lshl_b32 s63, s60, 5\n"
"v_mfma_f32_16x16x32_bf16 v[208:211], v[136:139], a[4:7], v[208:211]\n"
"s_add_u32 s72, s56, s61\n"
"ds_read_b64_tr_b16 v[132:133], v157 offset:21824\n"
"ds_read_b64_tr_b16 v[134:135], v157 offset:22336\n"
"s_lshl_b32 s59, s52, 11\n"
"v_mfma_f32_16x16x32_bf16 v[212:215], v[44:47], a[0:3], v[212:215]\n"
"s_add_u32 m0, s53, 45056\n"
"ds_read_b64_tr_b16 v[136:137], v6 offset:30720\n"
"ds_read_b64_tr_b16 v[138:139], v6 offset:30976\n"
"s_mul_i32 s56, s58, 1152\n"
"v_mfma_f32_16x16x32_bf16 v[216:219], v[44:47], a[4:7], v[216:219]\n"
"s_add_u32 s84, s13, 0\n"
"buffer_load_dwordx4 v160, s[36:39], s84 offen offset:0 lds\n"
"s_add_u32 s56, s56, s62\n"
"v_mfma_f32_16x16x32_bf16 v[224:227], v[48:51], a[0:3], v[224:227]\n"
"s_waitcnt vmcnt(17) lgkmcnt(6)\n"
"s_barrier\n"
"s_add_u32 s73, s56, s63\n"
"s_mul_i32 s58, s33, 36864\n"
"v_mfma_f32_16x16x32_bf16 v[188:191], v[52:55], a[16:19], v[188:191]\n"
"s_add_u32 s58, s58, 19271680\n"
"ds_read_b64_tr_b16 v[44:45], v156 offset:26112\n"
"ds_read_b64_tr_b16 v[46:47], v156 offset:26624\n"
"s_xor_b32 s61, s8, 2\n"
"v_mfma_f32_16x16x32_bf16 v[192:195], v[52:55], a[20:23], v[192:195]\n"
"s_add_u32 m0, s53, 49152\n"
"ds_read_b64_tr_b16 v[48:49], v157 offset:26112\n"
"ds_read_b64_tr_b16 v[50:51], v157 offset:26624\n"
"s_add_u32 s59, s59, s58\n"
"v_mfma_f32_16x16x32_bf16 v[196:199], v[56:59], a[16:19], v[196:199]\n"
"s_lshl_b32 s61, s61, 13\n"
"buffer_load_dwordx4 v181, s[36:39], s84 offen offset:0 lds\n"
"s_lshl_b32 s60, s60, 3\n"
"v_mfma_f32_16x16x32_bf16 v[200:203], v[56:59], a[20:23], v[200:203]\n"
"s_add_u32 s74, s61, s59\n"
"ds_read_b64_tr_b16 v[52:53], v1 offset:20480\n"
"ds_read_b64_tr_b16 v[54:55], v1 offset:20736\n"
"v_add_u32 v182, s60, v10\n"
"v_mfma_f32_16x16x32_bf16 v[220:223], v[124:127], a[16:19], v[220:223]\n"
"s_waitcnt lgkmcnt(6)\n"
"ds_read_b64_tr_b16 v[56:57], v156 offset:26176\n"
"ds_read_b64_tr_b16 v[58:59], v156 offset:26688\n"
"s_xor_b32 s61, s8, 3\n"
"v_mfma_f32_16x16x32_bf16 v[204:207], v[128:131], a[16:19], v[204:207]\n"
"s_lshl_b32 s61, s61, 13\n"
"ds_read_b64_tr_b16 v[124:125], v157 offset:26176\n"
"ds_read_b64_tr_b16 v[126:127], v157 offset:26688\n"
"s_add_u32 s69, s58, 32768\n"
"v_mfma_f32_16x16x32_bf16 v[208:211], v[128:131], a[20:23], v[208:211]\n"
"s_add_u32 s75, s61, s59\n"
"buffer_store_dwordx2 v[164:165], v168, s[28:31], s82 offen offset:640 sc1\n"
"s_min_u32 s25, s71, s72\n"
"v_mfma_f32_16x16x32_bf16 v[212:215], v[132:135], a[16:19], v[212:215]\n"
"s_add_u32 s84, s14, 2396160\n"
"ds_read_b64_tr_b16 v[128:129], v4 offset:20480\n"
"ds_read_b64_tr_b16 v[130:131], v4 offset:20736\n"
"s_xor_b32 s61, s8, 1\n"
"v_mfma_f32_16x16x32_bf16 v[216:219], v[132:135], a[20:23], v[216:219]\n"
"s_add_u32 m0, s54, 65536\n"
"s_lshl_b32 s61, s61, 13\n"
"s_add_u32 s26, s25, 18432\n"
"s_lshl_b32 s60, s60, 2\n"
"v_mfma_f32_16x16x32_bf16 v[224:227], v[136:139], a[16:19], v[224:227]\n"
"s_waitcnt lgkmcnt(6)\n"
"buffer_load_dwordx4 v158, s[36:39], s84 offen offset:0 lds\n"
"s_add_u32 s76, s61, s59\n"
"v_mfma_f32_16x16x32_bf16 v[188:191], v[44:47], a[32:35], v[188:191]\n"
"s_sub_u32 s27, s73, s60\n"
"ds_read_b64_tr_b16 v[132:133], v156 offset:30464\n"
"ds_read_b64_tr_b16 v[134:135], v156 offset:30976\n"
"v_mfma_f32_16x16x32_bf16 v[192:195], v[44:47], a[36:39], v[192:195]\n"
"s_add_u32 m0, s54, 66624\n"
"ds_read_b64_tr_b16 v[136:137], v157 offset:30464\n"
"ds_read_b64_tr_b16 v[138:139], v157 offset:30976\n"
"v_mfma_f32_16x16x32_bf16 v[196:199], v[48:51], a[32:35], v[196:199]\n"
"buffer_load_dwordx4 v159, s[36:39], s84 offen offset:0 lds\n"
"v_mfma_f32_16x16x32_bf16 v[200:203], v[48:51], a[36:39], v[200:203]\n"
"s_add_u32 m0, s54, 74240\n"
"ds_read_b64_tr_b16 v[44:45], v1 offset:22528\n"
"ds_read_b64_tr_b16 v[46:47], v1 offset:22784\n"
"v_mfma_f32_16x16x32_bf16 v[220:223], v[52:55], a[32:35], v[220:223]\n"
"s_waitcnt lgkmcnt(6)\n"
"ds_read_b64_tr_b16 v[48:49], v156 offset:30528\n"
"ds_read_b64_tr_b16 v[50:51], v156 offset:31040\n"
"v_mfma_f32_16x16x32_bf16 v[204:207], v[56:59], a[32:35], v[204:207]\n"
"s_add_u32 s84, s15, 2396160\n"
"buffer_load_dwordx4 v158, s[36:39], s84 offen offset:0 lds\n"
"v_mfma_f32_16x16x32_bf16 v[208:211], v[56:59], a[36:39], v[208:211]\n"
"ds_read_b64_tr_b16 v[52:53], v157 offset:30528\n"
"ds_read_b64_tr_b16 v[54:55], v157 offset:31040\n"
"v_mfma_f32_16x16x32_bf16 v[212:215], v[124:127], a[32:35], v[212:215]\n"
"s_add_u32 m0, s54, 75328\n"
"ds_read_b64_tr_b16 v[56:57], v4 offset:22528\n"
"ds_read_b64_tr_b16 v[58:59], v4 offset:22784\n"
"v_mfma_f32_16x16x32_bf16 v[216:219], v[124:127], a[36:39], v[216:219]\n"
"buffer_load_dwordx4 v159, s[36:39], s84 offen offset:0 lds\n"
"v_mfma_f32_16x16x32_bf16 v[224:227], v[128:131], a[32:35], v[224:227]\n"
"s_waitcnt lgkmcnt(6)\n"
"v_mfma_f32_16x16x32_bf16 v[188:191], v[132:135], a[48:51], v[188:191]\n"
"ds_read_b64_tr_b16 v[124:125], v156 offset:34816\n"
"ds_read_b64_tr_b16 v[126:127], v156 offset:35328\n"
"v_mfma_f32_16x16x32_bf16 v[192:195], v[132:135], a[52:55], v[192:195]\n"
"s_add_u32 m0, s53, 151552\n"
"ds_read_b64_tr_b16 v[128:129], v157 offset:34816\n"
"ds_read_b64_tr_b16 v[130:131], v157 offset:35328\n"
"v_mfma_f32_16x16x32_bf16 v[196:199], v[136:139], a[48:51], v[196:199]\n"
"s_add_u32 s84, s14, 2396160\n"
"buffer_load_dwordx4 v171, s[36:39], s84 offen offset:0 lds\n"
"v_mfma_f32_16x16x32_bf16 v[200:203], v[136:139], a[52:55], v[200:203]\n"
"ds_read_b64_tr_b16 v[132:133], v1 offset:24576\n"
"ds_read_b64_tr_b16 v[134:135], v1 offset:24832\n"
"v_mfma_f32_16x16x32_bf16 v[220:223], v[44:47], a[48:51], v[220:223]\n"
"s_waitcnt lgkmcnt(6)\n"
"ds_read_b64_tr_b16 v[136:137], v156 offset:34880\n"
"ds_read_b64_tr_b16 v[138:139], v156 offset:35392\n"
"v_mfma_f32_16x16x32_bf16 v[204:207], v[48:51], a[48:51], v[204:207]\n"
"s_add_u32 m0, s53, 155648\n"
"ds_read_b64_tr_b16 v[44:45], v157 offset:34880\n"
"ds_read_b64_tr_b16 v[46:47], v157 offset:35392\n"
"v_mfma_f32_16x16x32_bf16 v[208:211], v[48:51], a[52:55], v[208:211]\n"
"s_add_u32 s84, s15, 2396160\n"
"buffer_load_dwordx4 v171, s[36:39], s84 offen offset:0 lds\n"
"v_mfma_f32_16x16x32_bf16 v[212:215], v[52:55], a[48:51], v[212:215]\n"
"s_add_u32 s84, s14, 0\n"
"ds_read_b64_tr_b16 v[48:49], v4 offset:24576\n"
"ds_read_b64_tr_b16 v[50:51], v4 offset:24832\n"
"v_mfma_f32_16x16x32_bf16 v[216:219], v[52:55], a[52:55], v[216:219]\n"
"s_add_u32 m0, s53, 53248\n"
"v_mfma_f32_16x16x32_bf16 v[224:227], v[56:59], a[48:51], v[224:227]\n"
"s_waitcnt lgkmcnt(6)\n"
"buffer_load_dwordx4 v160, s[36:39], s84 offen offset:0 lds\n"
"v_mfma_f32_16x16x32_bf16 v[188:191], v[124:127], a[64:67], v[188:191]\n"
"ds_read_b64_tr_b16 v[52:53], v156 offset:39168\n"
"ds_read_b64_tr_b16 v[54:55], v156 offset:39680\n"
"v_mfma_f32_16x16x32_bf16 v[192:195], v[124:127], a[68:71], v[192:195]\n"
"s_add_u32 m0, s53, 57344\n"
"ds_read_b64_tr_b16 v[56:57], v157 offset:39168\n"
"ds_read_b64_tr_b16 v[58:59], v157 offset:39680\n"
"v_mfma_f32_16x16x32_bf16 v[196:199], v[128:131], a[64:67], v[196:199]\n"
"buffer_load_dwordx4 v181, s[36:39], s84 offen offset:0 lds\n"
"v_mfma_f32_16x16x32_bf16 v[200:203], v[128:131], a[68:71], v[200:203]\n"
"ds_read_b64_tr_b16 v[124:125], v1 offset:26624\n"
"ds_read_b64_tr_b16 v[126:127], v1 offset:26880\n"
"v_mfma_f32_16x16x32_bf16 v[220:223], v[132:135], a[64:67], v[220:223]\n"
"s_waitcnt lgkmcnt(6)\n"
"ds_read_b64_tr_b16 v[128:129], v156 offset:39232\n"
"ds_read_b64_tr_b16 v[130:131], v156 offset:39744\n"
"v_mfma_f32_16x16x32_bf16 v[204:207], v[136:139], a[64:67], v[204:207]\n"
"s_add_u32 s44, s95, 73728\n"
"buffer_load_dwordx4 v[28:31], v162, s[28:31], s44 offen offset:0 sc1\n"
"v_mfma_f32_16x16x32_bf16 v[208:211], v[136:139], a[68:71], v[208:211]\n"
"ds_read_b64_tr_b16 v[132:133], v157 offset:39232\n"
"ds_read_b64_tr_b16 v[134:135], v157 offset:39744\n"
"v_mfma_f32_16x16x32_bf16 v[212:215], v[44:47], a[64:67], v[212:215]\n"
"ds_read_b64_tr_b16 v[136:137], v4 offset:26624\n"
"ds_read_b64_tr_b16 v[138:139], v4 offset:26880\n"
"v_mfma_f32_16x16x32_bf16 v[216:219], v[44:47], a[68:71], v[216:219]\n"
"buffer_load_dwordx4 v[32:35], v163, s[28:31], s44 offen offset:0 sc1\n"
"v_mfma_f32_16x16x32_bf16 v[224:227], v[48:51], a[64:67], v[224:227]\n"
"s_waitcnt vmcnt(17) lgkmcnt(6)\n"
"s_barrier\n"
"v_mfma_f32_16x16x32_bf16 v[188:191], v[52:55], a[80:83], v[188:191]\n"
"ds_read_b64_tr_b16 v[44:45], v7 offset:0\n"
"ds_read_b64_tr_b16 v[46:47], v7 offset:512\n"
"v_mfma_f32_16x16x32_bf16 v[192:195], v[52:55], a[84:87], v[192:195]\n"
"s_add_u32 m0, s54, 82944\n"
"ds_read_b64_tr_b16 v[48:49], v9 offset:0\n"
"ds_read_b64_tr_b16 v[50:51], v9 offset:512\n"
"v_mfma_f32_16x16x32_bf16 v[196:199], v[56:59], a[80:83], v[196:199]\n"
"s_add_u32 s84, s55, 0\n"
"buffer_load_dwordx4 v158, s[36:39], s84 offen offset:0 lds\n"
"v_mfma_f32_16x16x32_bf16 v[200:203], v[56:59], a[84:87], v[200:203]\n"
"ds_read_b64_tr_b16 v[52:53], v1 offset:28672\n"
"ds_read_b64_tr_b16 v[54:55], v1 offset:28928\n"
"v_mfma_f32_16x16x32_bf16 v[220:223], v[124:127], a[80:83], v[220:223]\n"
"s_waitcnt lgkmcnt(6)\n"
"ds_read_b64_tr_b16 v[56:57], v7 offset:64\n"
"ds_read_b64_tr_b16 v[58:59], v7 offset:576\n"
"v_mfma_f32_16x16x32_bf16 v[204:207], v[128:131], a[80:83], v[204:207]\n"
"s_add_u32 m0, s54, 84032\n"
"ds_read_b64_tr_b16 v[124:125], v9 offset:64\n"
"ds_read_b64_tr_b16 v[126:127], v9 offset:576\n"
"v_mfma_f32_16x16x32_bf16 v[208:211], v[128:131], a[84:87], v[208:211]\n"
"buffer_load_dwordx4 v159, s[36:39], s84 offen offset:0 lds\n"
"v_mfma_f32_16x16x32_bf16 v[212:215], v[132:135], a[80:83], v[212:215]\n"
"ds_read_b64_tr_b16 v[128:129], v4 offset:28672\n"
"ds_read_b64_tr_b16 v[130:131], v4 offset:28928\n"
"v_mfma_f32_16x16x32_bf16 v[216:219], v[132:135], a[84:87], v[216:219]\n"
"buffer_load_dwordx2 v[166:167], v170, s[28:31], 0 offen offset:384 sc1\n"
"v_mfma_f32_16x16x32_bf16 v[224:227], v[136:139], a[80:83], v[224:227]\n"
"s_waitcnt lgkmcnt(6)\n"
"v_mfma_f32_16x16x32_bf16 v[188:191], v[44:47], a[96:99], v[188:191]\n"
"ds_read_b64_tr_b16 v[132:133], v7 offset:4352\n"
"ds_read_b64_tr_b16 v[134:135], v7 offset:4864\n"
"v_mfma_f32_16x16x32_bf16 v[192:195], v[44:47], a[100:103], v[192:195]\n"
"s_add_u32 m0, s54, 91648\n"
"ds_read_b64_tr_b16 v[136:137], v9 offset:4352\n"
"ds_read_b64_tr_b16 v[138:139], v9 offset:4864\n"
"v_mfma_f32_16x16x32_bf16 v[196:199], v[48:51], a[96:99], v[196:199]\n"
"s_add_u32 s84, s68, 0\n"
"buffer_load_dwordx4 v158, s[36:39], s84 offen offset:0 lds\n"
"v_mfma_f32_16x16x32_bf16 v[200:203], v[48:51], a[100:103], v[200:203]\n"
"s_add_u32 m0, s54, 92736\n"
"ds_read_b64_tr_b16 v[44:45], v1 offset:30720\n"
"ds_read_b64_tr_b16 v[46:47], v1 offset:30976\n"
"v_mfma_f32_16x16x32_bf16 v[220:223], v[52:55], a[96:99], v[220:223]\n"
"s_waitcnt lgkmcnt(6)\n"
"ds_read_b64_tr_b16 v[48:49], v7 offset:4416\n"
"ds_read_b64_tr_b16 v[50:51], v7 offset:4928\n"
"v_mfma_f32_16x16x32_bf16 v[204:207], v[56:59], a[96:99], v[204:207]\n"
"buffer_load_dwordx4 v159, s[36:39], s84 offen offset:0 lds\n"
"v_mfma_f32_16x16x32_bf16 v[208:211], v[56:59], a[100:103], v[208:211]\n"
"ds_read_b64_tr_b16 v[52:53], v9 offset:4416\n"
"ds_read_b64_tr_b16 v[54:55], v9 offset:4928\n"
"v_mfma_f32_16x16x32_bf16 v[212:215], v[124:127], a[96:99], v[212:215]\n"
"s_add_u32 m0, s53, 20480\n"
"ds_read_b64_tr_b16 v[56:57], v4 offset:30720\n"
"ds_read_b64_tr_b16 v[58:59], v4 offset:30976\n"
"v_mfma_f32_16x16x32_bf16 v[216:219], v[124:127], a[100:103], v[216:219]\n"
"s_add_u32 s84, s55, 0\n"
"buffer_load_dwordx4 v171, s[36:39], s84 offen offset:0 lds\n"
"v_mfma_f32_16x16x32_bf16 v[224:227], v[128:131], a[96:99], v[224:227]\n"
"s_waitcnt lgkmcnt(6)\n"
"v_mfma_f32_16x16x32_bf16 v[188:191], v[132:135], a[112:115], v[188:191]\n"
"ds_read_b64_tr_b16 v[124:125], v7 offset:8704\n"
"ds_read_b64_tr_b16 v[126:127], v7 offset:9216\n"
"v_mfma_f32_16x16x32_bf16 v[192:195], v[132:135], a[116:119], v[192:195]\n"
"s_add_u32 m0, s53, 159744\n"
"ds_read_b64_tr_b16 v[128:129], v9 offset:8704\n"
"ds_read_b64_tr_b16 v[130:131], v9 offset:9216\n"
"v_mfma_f32_16x16x32_bf16 v[196:199], v[136:139], a[112:115], v[196:199]\n"
"s_add_u32 s84, s68, 0\n"
"buffer_load_dwordx4 v171, s[36:39], s84 offen offset:0 lds\n"
"v_mfma_f32_16x16x32_bf16 v[200:203], v[136:139], a[116:119], v[200:203]\n"
"ds_read_b64_tr_b16 v[132:133], v1 offset:32768\n"
"ds_read_b64_tr_b16 v[134:135], v1 offset:33024\n"
"v_mfma_f32_16x16x32_bf16 v[220:223], v[44:47], a[112:115], v[220:223]\n"
"s_waitcnt lgkmcnt(6)\n"
"ds_read_b64_tr_b16 v[136:137], v7 offset:8768\n"
"ds_read_b64_tr_b16 v[138:139], v7 offset:9280\n"
"v_mfma_f32_16x16x32_bf16 v[204:207], v[48:51], a[112:115], v[204:207]\n"
"s_add_u32 m0, s53, 61440\n"
"ds_read_b64_tr_b16 v[44:45], v9 offset:8768\n"
"ds_read_b64_tr_b16 v[46:47], v9 offset:9280\n"
"v_mfma_f32_16x16x32_bf16 v[208:211], v[48:51], a[116:119], v[208:211]\n"
"s_add_u32 s84, s15, 0\n"
"buffer_load_dwordx4 v160, s[36:39], s84 offen offset:0 lds\n"
"v_mfma_f32_16x16x32_bf16 v[212:215], v[52:55], a[112:115], v[212:215]\n"
"ds_read_b64_tr_b16 v[48:49], v4 offset:32768\n"
"ds_read_b64_tr_b16 v[50:51], v4 offset:33024\n"
"v_mfma_f32_16x16x32_bf16 v[216:219], v[52:55], a[116:119], v[216:219]\n"
"s_add_u32 m0, s53, 110592\n"
"v_mfma_f32_16x16x32_bf16 v[224:227], v[56:59], a[112:115], v[224:227]\n"
"s_waitcnt lgkmcnt(6)\n"
"buffer_load_dwordx4 v181, s[36:39], s84 offen offset:0 lds\n"
"v_mfma_f32_16x16x32_bf16 v[188:191], v[124:127], a[128:131], v[188:191]\n"
"ds_read_b64_tr_b16 v[52:53], v7 offset:13056\n"
"ds_read_b64_tr_b16 v[54:55], v7 offset:13568\n"
"v_mfma_f32_16x16x32_bf16 v[192:195], v[124:127], a[132:135], v[192:195]\n"
"s_add_u32 m0, s53, 114688\n"
"ds_read_b64_tr_b16 v[56:57], v9 offset:13056\n"
"ds_read_b64_tr_b16 v[58:59], v9 offset:13568\n"
"v_mfma_f32_16x16x32_bf16 v[196:199], v[128:131], a[128:131], v[196:199]\n"
"s_add_u32 s84, s12, 36864\n"
"buffer_load_dwordx4 v160, s[36:39], s84 offen offset:0 lds\n"
"v_mfma_f32_16x16x32_bf16 v[200:203], v[128:131], a[132:135], v[200:203]\n"
"s_add_u32 m0, s53, 118784\n"
"ds_read_b64_tr_b16 v[124:125], v1 offset:34816\n"
"ds_read_b64_tr_b16 v[126:127], v1 offset:35072\n"
"v_mfma_f32_16x16x32_bf16 v[220:223], v[132:135], a[128:131], v[220:223]\n"
"s_waitcnt lgkmcnt(6)\n"
"ds_read_b64_tr_b16 v[128:129], v7 offset:13120\n"
"ds_read_b64_tr_b16 v[130:131], v7 offset:13632\n"
"v_mfma_f32_16x16x32_bf16 v[204:207], v[136:139], a[128:131], v[204:207]\n"
"buffer_load_dwordx4 v181, s[36:39], s84 offen offset:0 lds\n"
"v_mfma_f32_16x16x32_bf16 v[208:211], v[136:139], a[132:135], v[208:211]\n"
"ds_read_b64_tr_b16 v[132:133], v9 offset:13120\n"
"ds_read_b64_tr_b16 v[134:135], v9 offset:13632\n"
"v_mfma_f32_16x16x32_bf16 v[212:215], v[44:47], a[128:131], v[212:215]\n"
"s_add_u32 m0, s53, 122880\n"
"ds_read_b64_tr_b16 v[136:137], v4 offset:34816\n"
"ds_read_b64_tr_b16 v[138:139], v4 offset:35072\n"
"v_mfma_f32_16x16x32_bf16 v[216:219], v[44:47], a[132:135], v[216:219]\n"
"s_add_u32 s84, s13, 36864\n"
"buffer_load_dwordx4 v160, s[36:39], s84 offen offset:0 lds\n"
"v_mfma_f32_16x16x32_bf16 v[224:227], v[48:51], a[128:131], v[224:227]\n"
"s_waitcnt vmcnt(16) lgkmcnt(6)\n"
"s_barrier\n"
"v_mfma_f32_16x16x32_bf16 v[188:191], v[52:55], a[144:147], v[188:191]\n"
"ds_read_b64_tr_b16 v[44:45], v156 offset:0\n"
"ds_read_b64_tr_b16 v[46:47], v156 offset:512\n"
"v_mfma_f32_16x16x32_bf16 v[192:195], v[52:55], a[148:151], v[192:195]\n"
"s_add_u32 m0, s53, 126976\n"
"ds_read_b64_tr_b16 v[48:49], v157 offset:0\n"
"ds_read_b64_tr_b16 v[50:51], v157 offset:512\n"
"v_mfma_f32_16x16x32_bf16 v[196:199], v[56:59], a[144:147], v[196:199]\n"
"buffer_load_dwordx4 v181, s[36:39], s84 offen offset:0 lds\n"
"v_mfma_f32_16x16x32_bf16 v[200:203], v[56:59], a[148:151], v[200:203]\n"
"ds_read_b64_tr_b16 v[52:53], v3 offset:20480\n"
"ds_read_b64_tr_b16 v[54:55], v3 offset:20736\n"
"v_mfma_f32_16x16x32_bf16 v[220:223], v[124:127], a[144:147], v[220:223]\n"
"s_waitcnt lgkmcnt(6)\n"
"ds_read_b64_tr_b16 v[56:57], v156 offset:64\n"
"ds_read_b64_tr_b16 v[58:59], v156 offset:576\n"
"v_mfma_f32_16x16x32_bf16 v[204:207], v[128:131], a[144:147], v[204:207]\n"
"s_add_u32 m0, s53, 147456\n"
"ds_read_b64_tr_b16 v[124:125], v157 offset:64\n"
"ds_read_b64_tr_b16 v[126:127], v157 offset:576\n"
"v_mfma_f32_16x16x32_bf16 v[208:211], v[128:131], a[148:151], v[208:211]\n"
"s_add_u32 s84, s14, 36864\n"
"buffer_load_dwordx4 v160, s[36:39], s84 offen offset:0 lds\n"
"v_mfma_f32_16x16x32_bf16 v[212:215], v[132:135], a[144:147], v[212:215]\n"
"ds_read_b64_tr_b16 v[128:129], v6 offset:20480\n"
"ds_read_b64_tr_b16 v[130:131], v6 offset:20736\n"
"v_mfma_f32_16x16x32_bf16 v[216:219], v[132:135], a[148:151], v[216:219]\n"
"s_add_u32 m0, s53, 0\n"
"v_mfma_f32_16x16x32_bf16 v[224:227], v[136:139], a[144:147], v[224:227]\n"
"s_waitcnt lgkmcnt(6)\n"
"buffer_load_dwordx4 v181, s[36:39], s84 offen offset:0 lds\n"
"v_mfma_f32_16x16x32_bf16 v[188:191], v[44:47], a[160:163], v[188:191]\n"
"ds_read_b64_tr_b16 v[132:133], v156 offset:4352\n"
"ds_read_b64_tr_b16 v[134:135], v156 offset:4864\n"
"v_mfma_f32_16x16x32_bf16 v[192:195], v[44:47], a[164:167], v[192:195]\n"
"ds_read_b64_tr_b16 v[136:137], v157 offset:4352\n"
"ds_read_b64_tr_b16 v[138:139], v157 offset:4864\n"
"v_mfma_f32_16x16x32_bf16 v[196:199], v[48:51], a[160:163], v[196:199]\n"
"s_waitcnt vmcnt(12)\n"
"v_cmp_ne_u64 vcc, s[34:35], v[166:167]\n"
"s_nop 3\n"
"s_cbranch_vccz pFX_ok_gated_mix_fused\n"
"s_mov_b32 s83, 0\n"
"pFX_sp_gated_mix_fused:\n"
"buffer_load_dwordx2 v[166:167], v170, s[28:31], 0 offen offset:384 sc1\n"
"s_waitcnt vmcnt(0)\n"
"s_add_u32 s83, s83, 1\n"
"s_cmp_gt_u32 s83, 1048576\n"
"s_cbranch_scc1 pFX_ok_gated_mix_fused\n"
"v_cmp_ne_u64 vcc, s[34:35], v[166:167]\n"
"s_nop 3\n"
"s_cbranch_vccnz pFX_sp_gated_mix_fused\n"
"pFX_ok_gated_mix_fused:\n"
"v_mfma_f32_16x16x32_bf16 v[200:203], v[48:51], a[164:167], v[200:203]\n"
"s_add_u32 m0, s53, 4096\n"
"ds_read_b64_tr_b16 v[44:45], v3 offset:22528\n"
"ds_read_b64_tr_b16 v[46:47], v3 offset:22784\n"
"v_mfma_f32_16x16x32_bf16 v[220:223], v[52:55], a[160:163], v[220:223]\n"
"s_waitcnt lgkmcnt(6)\n"
"ds_read_b64_tr_b16 v[48:49], v156 offset:4416\n"
"ds_read_b64_tr_b16 v[50:51], v156 offset:4928\n"
"v_mfma_f32_16x16x32_bf16 v[204:207], v[56:59], a[160:163], v[204:207]\n"
"s_add_u32 s84, s15, 36864\n"
"buffer_load_dwordx4 v160, s[36:39], s84 offen offset:0 lds\n"
"v_mfma_f32_16x16x32_bf16 v[208:211], v[56:59], a[164:167], v[208:211]\n"
"ds_read_b64_tr_b16 v[52:53], v157 offset:4416\n"
"ds_read_b64_tr_b16 v[54:55], v157 offset:4928\n"
"v_mfma_f32_16x16x32_bf16 v[212:215], v[124:127], a[160:163], v[212:215]\n"
"s_add_u32 m0, s53, 24576\n"
"ds_read_b64_tr_b16 v[56:57], v6 offset:22528\n"
"ds_read_b64_tr_b16 v[58:59], v6 offset:22784\n"
"v_mfma_f32_16x16x32_bf16 v[216:219], v[124:127], a[164:167], v[216:219]\n"
"buffer_load_dwordx4 v181, s[36:39], s84 offen offset:0 lds\n"
"v_mfma_f32_16x16x32_bf16 v[224:227], v[128:131], a[160:163], v[224:227]\n"
"s_waitcnt lgkmcnt(6)\n"
"v_mfma_f32_16x16x32_bf16 v[188:191], v[132:135], a[176:179], v[188:191]\n"
"ds_read_b64_tr_b16 v[124:125], v156 offset:8704\n"
"ds_read_b64_tr_b16 v[126:127], v156 offset:9216\n"
"v_mfma_f32_16x16x32_bf16 v[192:195], v[132:135], a[180:183], v[192:195]\n"
"s_add_u32 m0, s53, 28672\n"
"ds_read_b64_tr_b16 v[128:129], v157 offset:8704\n"
"ds_read_b64_tr_b16 v[130:131], v157 offset:9216\n"
"v_mfma_f32_16x16x32_bf16 v[196:199], v[136:139], a[176:179], v[196:199]\n"
"s_add_u32 s84, s12, 2359296\n"
"buffer_load_dwordx4 v160, s[36:39], s84 offen offset:0 lds\n"
"v_mfma_f32_16x16x32_bf16 v[200:203], v[136:139], a[180:183], v[200:203]\n"
"ds_read_b64_tr_b16 v[132:133], v3 offset:24576\n"
"ds_read_b64_tr_b16 v[134:135], v3 offset:24832\n"
"v_mfma_f32_16x16x32_bf16 v[220:223], v[44:47], a[176:179], v[220:223]\n"
"s_waitcnt lgkmcnt(6)\n"
"ds_read_b64_tr_b16 v[136:137], v156 offset:8768\n"
"ds_read_b64_tr_b16 v[138:139], v156 offset:9280\n"
"v_mfma_f32_16x16x32_bf16 v[204:207], v[48:51], a[176:179], v[204:207]\n"
"s_add_u32 m0, s53, 102400\n"
"ds_read_b64_tr_b16 v[44:45], v157 offset:8768\n"
"ds_read_b64_tr_b16 v[46:47], v157 offset:9280\n"
"v_mfma_f32_16x16x32_bf16 v[208:211], v[48:51], a[180:183], v[208:211]\n"
"buffer_load_dwordx4 v181, s[36:39], s84 offen offset:0 lds\n"
"v_mfma_f32_16x16x32_bf16 v[212:215], v[52:55], a[176:179], v[212:215]\n"
"ds_read_b64_tr_b16 v[48:49], v6 offset:24576\n"
"ds_read_b64_tr_b16 v[50:51], v6 offset:24832\n"
"v_mfma_f32_16x16x32_bf16 v[216:219], v[52:55], a[180:183], v[216:219]\n"
"v_mfma_f32_16x16x32_bf16 v[224:227], v[56:59], a[176:179], v[224:227]\n"
"s_waitcnt lgkmcnt(6)\n"
"v_mfma_f32_16x16x32_bf16 v[188:191], v[124:127], a[192:195], v[188:191]\n"
"ds_read_b64_tr_b16 v[52:53], v156 offset:13056\n"
"ds_read_b64_tr_b16 v[54:55], v156 offset:13568\n"
"v_mfma_f32_16x16x32_bf16 v[192:195], v[124:127], a[196:199], v[192:195]\n"
"ds_read_b64_tr_b16 v[56:57], v157 offset:13056\n"
"ds_read_b64_tr_b16 v[58:59], v157 offset:13568\n"
"v_mfma_f32_16x16x32_bf16 v[196:199], v[128:131], a[192:195], v[196:199]\n"
"buffer_load_dwordx2 v[166:167], v183, s[28:31], 0 offen offset:640 sc1\n"
"v_mfma_f32_16x16x32_bf16 v[200:203], v[128:131], a[196:199], v[200:203]\n"
"ds_read_b64_tr_b16 v[124:125], v3 offset:26624\n"
"ds_read_b64_tr_b16 v[126:127], v3 offset:26880\n"
"v_mfma_f32_16x16x32_bf16 v[220:223], v[132:135], a[192:195], v[220:223]\n"
"s_waitcnt lgkmcnt(6)\n"
"ds_read_b64_tr_b16 v[128:129], v156 offset:13120\n"
"ds_read_b64_tr_b16 v[130:131], v156 offset:13632\n"
"v_mfma_f32_16x16x32_bf16 v[204:207], v[136:139], a[192:195], v[204:207]\n"
"v_mfma_f32_16x16x32_bf16 v[208:211], v[136:139], a[196:199], v[208:211]\n"
"ds_read_b64_tr_b16 v[132:133], v157 offset:13120\n"
"ds_read_b64_tr_b16 v[134:135], v157 offset:13632\n"
"v_mfma_f32_16x16x32_bf16 v[212:215], v[44:47], a[192:195], v[212:215]\n"
"ds_read_b64_tr_b16 v[136:137], v6 offset:26624\n"
"ds_read_b64_tr_b16 v[138:139], v6 offset:26880\n"
"v_mfma_f32_16x16x32_bf16 v[216:219], v[44:47], a[196:199], v[216:219]\n"
"v_mfma_f32_16x16x32_bf16 v[224:227], v[48:51], a[192:195], v[224:227]\n"
"s_waitcnt vmcnt(13) lgkmcnt(6)\n"
"s_barrier\n"
"v_mfma_f32_16x16x32_bf16 v[188:191], v[52:55], a[208:211], v[188:191]\n"
"ds_read_b64_tr_b16 v[44:45], v156 offset:17408\n"
"ds_read_b64_tr_b16 v[46:47], v156 offset:17920\n"
"v_mfma_f32_16x16x32_bf16 v[192:195], v[52:55], a[212:215], v[192:195]\n"
"s_add_u32 m0, s53, 106496\n"
"ds_read_b64_tr_b16 v[48:49], v157 offset:17408\n"
"ds_read_b64_tr_b16 v[50:51], v157 offset:17920\n"
"v_mfma_f32_16x16x32_bf16 v[196:199], v[56:59], a[208:211], v[196:199]\n"
"s_add_u32 s84, s13, 2359296\n"
"buffer_load_dwordx4 v160, s[36:39], s84 offen offset:0 lds\n"
"v_mfma_f32_16x16x32_bf16 v[200:203], v[56:59], a[212:215], v[200:203]\n"
"ds_read_b64_tr_b16 v[52:53], v1 offset:20480\n"
"ds_read_b64_tr_b16 v[54:55], v1 offset:20736\n"
"v_mfma_f32_16x16x32_bf16 v[220:223], v[124:127], a[208:211], v[220:223]\n"
"s_waitcnt lgkmcnt(6)\n"
"ds_read_b64_tr_b16 v[56:57], v156 offset:17472\n"
"ds_read_b64_tr_b16 v[58:59], v156 offset:17984\n"
"v_mfma_f32_16x16x32_bf16 v[204:207], v[128:131], a[208:211], v[204:207]\n"
"s_add_u32 m0, s53, 8192\n"
"ds_read_b64_tr_b16 v[124:125], v157 offset:17472\n"
"ds_read_b64_tr_b16 v[126:127], v157 offset:17984\n"
"v_mfma_f32_16x16x32_bf16 v[208:211], v[128:131], a[212:215], v[208:211]\n"
"buffer_load_dwordx4 v181, s[36:39], s84 offen offset:0 lds\n"
"v_mfma_f32_16x16x32_bf16 v[212:215], v[132:135], a[208:211], v[212:215]\n"
"s_add_u32 s84, s14, 2359296\n"
"ds_read_b64_tr_b16 v[128:129], v4 offset:20480\n"
"ds_read_b64_tr_b16 v[130:131], v4 offset:20736\n"
"v_mfma_f32_16x16x32_bf16 v[216:219], v[132:135], a[212:215], v[216:219]\n"
"s_add_u32 m0, s53, 12288\n"
"v_mfma_f32_16x16x32_bf16 v[224:227], v[136:139], a[208:211], v[224:227]\n"
"s_waitcnt lgkmcnt(6)\n"
"buffer_load_dwordx4 v160, s[36:39], s84 offen offset:0 lds\n"
"v_mfma_f32_16x16x32_bf16 v[188:191], v[44:47], a[224:227], v[188:191]\n"
"ds_read_b64_tr_b16 v[132:133], v156 offset:21760\n"
"ds_read_b64_tr_b16 v[134:135], v156 offset:22272\n"
"v_mfma_f32_16x16x32_bf16 v[192:195], v[44:47], a[228:231], v[192:195]\n"
"s_add_u32 m0, s53, 16384\n"
"ds_read_b64_tr_b16 v[136:137], v157 offset:21760\n"
"ds_read_b64_tr_b16 v[138:139], v157 offset:22272\n"
"v_mfma_f32_16x16x32_bf16 v[196:199], v[48:51], a[224:227], v[196:199]\n"
"buffer_load_dwordx4 v181, s[36:39], s84 offen offset:0 lds\n"
"v_mfma_f32_16x16x32_bf16 v[200:203], v[48:51], a[228:231], v[200:203]\n"
"s_add_u32 m0, s53, 32768\n"
"ds_read_b64_tr_b16 v[44:45], v1 offset:22528\n"
"ds_read_b64_tr_b16 v[46:47], v1 offset:22784\n"
"v_mfma_f32_16x16x32_bf16 v[220:223], v[52:55], a[224:227], v[220:223]\n"
"s_waitcnt lgkmcnt(6)\n"
"ds_read_b64_tr_b16 v[48:49], v156 offset:21824\n"
"ds_read_b64_tr_b16 v[50:51], v156 offset:22336\n"
"v_mfma_f32_16x16x32_bf16 v[204:207], v[56:59], a[224:227], v[204:207]\n"
"s_add_u32 s84, s15, 2359296\n"
"buffer_load_dwordx4 v160, s[36:39], s84 offen offset:0 lds\n"
"v_mfma_f32_16x16x32_bf16 v[208:211], v[56:59], a[228:231], v[208:211]\n"
"ds_read_b64_tr_b16 v[52:53], v157 offset:21824\n"
"ds_read_b64_tr_b16 v[54:55], v157 offset:22336\n"
"v_mfma_f32_16x16x32_bf16 v[212:215], v[124:127], a[224:227], v[212:215]\n"
"s_add_u32 m0, s53, 65536\n"
"ds_read_b64_tr_b16 v[56:57], v4 offset:22528\n"
"ds_read_b64_tr_b16 v[58:59], v4 offset:22784\n"
"v_mfma_f32_16x16x32_bf16 v[216:219], v[124:127], a[228:231], v[216:219]\n"
"buffer_load_dwordx4 v181, s[36:39], s84 offen offset:0 lds\n"
"v_mfma_f32_16x16x32_bf16 v[224:227], v[128:131], a[224:227], v[224:227]\n"
"s_waitcnt lgkmcnt(6)\n"
"v_mfma_f32_16x16x32_bf16 v[188:191], v[132:135], a[240:243], v[188:191]\n"
"ds_read_b64_tr_b16 v[124:125], v156 offset:26112\n"
"ds_read_b64_tr_b16 v[126:127], v156 offset:26624\n"
"v_mfma_f32_16x16x32_bf16 v[192:195], v[132:135], a[244:247], v[192:195]\n"
"s_add_u32 m0, s53, 69632\n"
"ds_read_b64_tr_b16 v[128:129], v157 offset:26112\n"
"ds_read_b64_tr_b16 v[130:131], v157 offset:26624\n"
"v_mfma_f32_16x16x32_bf16 v[196:199], v[136:139], a[240:243], v[196:199]\n"
"s_add_u32 s84, s12, 2396160\n"
"buffer_load_dwordx4 v160, s[36:39], s84 offen offset:0 lds\n"
"v_mfma_f32_16x16x32_bf16 v[200:203], v[136:139], a[244:247], v[200:203]\n"
"ds_read_b64_tr_b16 v[132:133], v3 offset:28672\n"
"ds_read_b64_tr_b16 v[134:135], v3 offset:28928\n"
"v_mfma_f32_16x16x32_bf16 v[220:223], v[44:47], a[240:243], v[220:223]\n"
"s_waitcnt lgkmcnt(6)\n"
"ds_read_b64_tr_b16 v[136:137], v156 offset:26176\n"
"ds_read_b64_tr_b16 v[138:139], v156 offset:26688\n"
"v_mfma_f32_16x16x32_bf16 v[204:207], v[48:51], a[240:243], v[204:207]\n"
"s_add_u32 m0, s53, 151552\n"
"ds_read_b64_tr_b16 v[44:45], v157 offset:26176\n"
"ds_read_b64_tr_b16 v[46:47], v157 offset:26688\n"
"v_mfma_f32_16x16x32_bf16 v[208:211], v[48:51], a[244:247], v[208:211]\n"
"buffer_load_dwordx4 v181, s[36:39], s84 offen offset:0 lds\n"
"v_mfma_f32_16x16x32_bf16 v[212:215], v[52:55], a[240:243], v[212:215]\n"
"ds_read_b64_tr_b16 v[48:49], v6 offset:28672\n"
"ds_read_b64_tr_b16 v[50:51], v6 offset:28928\n"
"v_mfma_f32_16x16x32_bf16 v[216:219], v[52:55], a[244:247], v[216:219]\n"
"v_mfma_f32_16x16x32_bf16 v[224:227], v[56:59], a[240:243], v[224:227]\n"
"s_waitcnt lgkmcnt(6)\n"
"v_mfma_f32_16x16x32_bf16 v[188:191], v[124:127], v[12:15], v[188:191]\n"
"ds_read_b64_tr_b16 v[52:53], v156 offset:30464\n"
"ds_read_b64_tr_b16 v[54:55], v156 offset:30976\n"
"v_mfma_f32_16x16x32_bf16 v[192:195], v[124:127], v[16:19], v[192:195]\n"
"ds_read_b64_tr_b16 v[56:57], v157 offset:30464\n"
"ds_read_b64_tr_b16 v[58:59], v157 offset:30976\n"
"v_mfma_f32_16x16x32_bf16 v[196:199], v[128:131], v[12:15], v[196:199]\n"
"v_mfma_f32_16x16x32_bf16 v[200:203], v[128:131], v[16:19], v[200:203]\n"
"ds_read_b64_tr_b16 v[124:125], v3 offset:30720\n"
"ds_read_b64_tr_b16 v[126:127], v3 offset:30976\n"
"v_mfma_f32_16x16x32_bf16 v[220:223], v[132:135], v[12:15], v[220:223]\n"
"s_waitcnt lgkmcnt(6)\n"
"ds_read_b64_tr_b16 v[128:129], v156 offset:30528\n"
"ds_read_b64_tr_b16 v[130:131], v156 offset:31040\n"
"v_mfma_f32_16x16x32_bf16 v[204:207], v[136:139], v[12:15], v[204:207]\n"
"s_waitcnt vmcnt(8)\n"
"v_cmp_ne_u64 vcc, s[34:35], v[166:167]\n"
"s_nop 3\n"
"s_cbranch_vccz pF3_ok_gated_mix_fused\n"
"s_mov_b32 s83, 0\n"
"pF3_sp_gated_mix_fused:\n"
"buffer_load_dwordx2 v[166:167], v183, s[28:31], 0 offen offset:640 sc1\n"
"s_waitcnt vmcnt(0)\n"
"s_add_u32 s83, s83, 1\n"
"s_cmp_gt_u32 s83, 1048576\n"
"s_cbranch_scc1 pF3_ok_gated_mix_fused\n"
"v_cmp_ne_u64 vcc, s[34:35], v[166:167]\n"
"s_nop 3\n"
"s_cbranch_vccnz pF3_sp_gated_mix_fused\n"
"pF3_ok_gated_mix_fused:\n"
"v_mfma_f32_16x16x32_bf16 v[208:211], v[136:139], v[16:19], v[208:211]\n"
"ds_read_b64_tr_b16 v[132:133], v157 offset:30528\n"
"ds_read_b64_tr_b16 v[134:135], v157 offset:31040\n"
"v_mfma_f32_16x16x32_bf16 v[212:215], v[44:47], v[12:15], v[212:215]\n"
"ds_read_b64_tr_b16 v[136:137], v6 offset:30720\n"
"ds_read_b64_tr_b16 v[138:139], v6 offset:30976\n"
"v_mfma_f32_16x16x32_bf16 v[216:219], v[44:47], v[16:19], v[216:219]\n"
"v_mfma_f32_16x16x32_bf16 v[224:227], v[48:51], v[12:15], v[224:227]\n"
"s_waitcnt lgkmcnt(6)\n"
"s_barrier\n"
"v_mfma_f32_16x16x32_bf16 v[188:191], v[52:55], v[28:31], v[188:191]\n"
"ds_read_b64_tr_b16 v[44:45], v1 offset:36864\n"
"ds_read_b64_tr_b16 v[46:47], v1 offset:37120\n"
"v_mfma_f32_16x16x32_bf16 v[192:195], v[52:55], v[32:35], v[192:195]\n"
"s_lshl_b32 m0, s52, 12\ns_add_u32 m0, m0, 133120\n"
"ds_read_b64_tr_b16 v[48:49], v4 offset:36864\n"
"ds_read_b64_tr_b16 v[50:51], v4 offset:37120\n"
"v_mfma_f32_16x16x32_bf16 v[196:199], v[56:59], v[28:31], v[196:199]\n"
"s_add_u32 s84, s24, 0\n"
"buffer_load_dwordx4 v161, s[28:31], s84 offen offset:0 sc1 lds\n"
"v_mfma_f32_16x16x32_bf16 v[200:203], v[56:59], v[32:35], v[200:203]\n"
"ds_read_b64_tr_b16 v[52:53], v1 offset:38912\n"
"ds_read_b64_tr_b16 v[54:55], v1 offset:39168\n"
"v_mfma_f32_16x16x32_bf16 v[220:223], v[124:127], v[28:31], v[220:223]\n"
"s_waitcnt lgkmcnt(6)\n"
"ds_read_b64_tr_b16 v[56:57], v4 offset:38912\n"
"ds_read_b64_tr_b16 v[58:59], v4 offset:39168\n"
"v_mfma_f32_16x16x32_bf16 v[204:207], v[128:131], v[28:31], v[204:207]\n"
"s_lshl_b32 m0, s52, 12\ns_add_u32 m0, m0, 134144\n"
"ds_read_b64_tr_b16 v[124:125], v1 offset:45056\n"
"ds_read_b64_tr_b16 v[126:127], v1 offset:45312\n"
"v_mfma_f32_16x16x32_bf16 v[208:211], v[128:131], v[32:35], v[208:211]\n"
"s_add_u32 s84, s24, 32768\n"
"buffer_load_dwordx4 v161, s[28:31], s84 offen offset:0 sc1 lds\n"
"v_mfma_f32_16x16x32_bf16 v[212:215], v[132:135], v[28:31], v[212:215]\n"
"s_lshl_b32 m0, s52, 12\ns_add_u32 m0, m0, 131072\n"
"ds_read_b64_tr_b16 v[128:129], v4 offset:45056\n"
"ds_read_b64_tr_b16 v[130:131], v4 offset:45312\n"
"v_mfma_f32_16x16x32_bf16 v[216:219], v[132:135], v[32:35], v[216:219]\n"
"s_add_u32 s84, s88, 0\n"
"buffer_load_dwordx4 v161, s[28:31], s84 offen offset:0 sc1 lds\n"
"v_mfma_f32_16x16x32_bf16 v[224:227], v[136:139], v[28:31], v[224:227]\n"
"s_waitcnt lgkmcnt(6)\n"
"v_mfma_f32_16x16x32_bf16 v[228:231], v[44:47], a[8:11], 0\n"
"ds_read_b64_tr_b16 v[132:133], v1 offset:47104\n"
"ds_read_b64_tr_b16 v[134:135], v1 offset:47360\n"
"v_mfma_f32_16x16x32_bf16 v[232:235], v[44:47], a[12:15], 0\n"
"ds_read_b64_tr_b16 v[136:137], v4 offset:47104\n"
"ds_read_b64_tr_b16 v[138:139], v4 offset:47360\n"
"v_mfma_f32_16x16x32_bf16 v[236:239], v[48:51], a[8:11], 0\n"
"s_lshl_b32 m0, s52, 12\ns_add_u32 m0, m0, 132096\n"
"ds_read_b64_tr_b16 v[44:45], v1 offset:53248\n"
"ds_read_b64_tr_b16 v[46:47], v1 offset:53504\n"
"v_mfma_f32_16x16x32_bf16 v[240:243], v[48:51], a[12:15], 0\n"
"s_add_u32 s84, s88, 32768\n"
"buffer_load_dwordx4 v161, s[28:31], s84 offen offset:0 sc1 lds\n"
"v_mfma_f32_16x16x32_bf16 v[228:231], v[52:55], a[24:27], v[228:231]\n"
"ds_read_b64_tr_b16 v[48:49], v4 offset:53248\n"
"ds_read_b64_tr_b16 v[50:51], v4 offset:53504\n"
"v_mfma_f32_16x16x32_bf16 v[232:235], v[52:55], a[28:31], v[232:235]\n"
"s_waitcnt lgkmcnt(8)\n"
"buffer_load_dword v104, v184, s[28:31], s89 offen offset:0 sc1\n"
"v_cvt_pk_bf16_f32 v188, v188, v189\n"
"v_mfma_f32_16x16x32_bf16 v[236:239], v[56:59], a[24:27], v[236:239]\n"
"ds_read_b64_tr_b16 v[52:53], v1 offset:55296\n"
"ds_read_b64_tr_b16 v[54:55], v1 offset:55552\n"
"v_cvt_pk_bf16_f32 v189, v190, v191\n"
"v_mfma_f32_16x16x32_bf16 v[240:243], v[56:59], a[28:31], v[240:243]\n"
"buffer_load_dword v105, v184, s[28:31], s90 offen offset:0 sc1\n"
"v_cvt_pk_bf16_f32 v190, v192, v193\n"
"v_mfma_f32_16x16x32_bf16 v[228:231], v[124:127], a[72:75], v[228:231]\n"
"ds_read_b64_tr_b16 v[56:57], v4 offset:55296\n"
"ds_read_b64_tr_b16 v[58:59], v4 offset:55552\n"
"v_cvt_pk_bf16_f32 v191, v194, v195\n"
"v_mfma_f32_16x16x32_bf16 v[232:235], v[124:127], a[76:79], v[232:235]\n"
"s_barrier\n"
"v_cvt_pk_bf16_f32 v192, v196, v197\n"
"v_cvt_pk_bf16_f32 v193, v198, v199\n"
"v_mfma_f32_16x16x32_bf16 v[236:239], v[128:131], a[72:75], v[236:239]\n"
"ds_read_b64_tr_b16 v[124:125], v1 offset:61440\n"
"ds_read_b64_tr_b16 v[126:127], v1 offset:61696\n"
"v_cvt_pk_bf16_f32 v194, v200, v201\n"
"v_mfma_f32_16x16x32_bf16 v[240:243], v[128:131], a[76:79], v[240:243]\n"
"s_waitcnt lgkmcnt(8)\n"
"buffer_load_dword v106, v184, s[28:31], s96 offen offset:0 sc1\n"
"v_cvt_pk_bf16_f32 v195, v202, v203\n"
"v_mfma_f32_16x16x32_bf16 v[228:231], v[132:135], a[88:91], v[228:231]\n"
"ds_read_b64_tr_b16 v[128:129], v4 offset:61440\n"
"ds_read_b64_tr_b16 v[130:131], v4 offset:61696\n"
"v_cvt_pk_bf16_f32 v196, v204, v205\n"
"v_mfma_f32_16x16x32_bf16 v[232:235], v[132:135], a[92:95], v[232:235]\n"
"v_cvt_pk_bf16_f32 v197, v206, v207\n"
"v_cvt_pk_bf16_f32 v198, v208, v209\n"
"v_cvt_pk_bf16_f32 v199, v210, v211\n"
"v_mfma_f32_16x16x32_bf16 v[236:239], v[136:139], a[88:91], v[236:239]\n"
"s_add_u32 m0, s53, 20480\n"
"ds_read_b64_tr_b16 v[132:133], v1 offset:63488\n"
"ds_read_b64_tr_b16 v[134:135], v1 offset:63744\n"
"v_cvt_pk_bf16_f32 v200, v212, v213\n"
"v_mfma_f32_16x16x32_bf16 v[240:243], v[136:139], a[92:95], v[240:243]\n"
"s_add_u32 s84, s13, 2396160\n"
"buffer_load_dwordx4 v160, s[36:39], s84 offen offset:0 lds\n"
"v_cvt_pk_bf16_f32 v201, v214, v215\n"
"v_mfma_f32_16x16x32_bf16 v[228:231], v[44:47], a[104:107], v[228:231]\n"
"ds_read_b64_tr_b16 v[136:137], v4 offset:63488\n"
"ds_read_b64_tr_b16 v[138:139], v4 offset:63744\n"
"v_cvt_pk_bf16_f32 v202, v216, v217\n"
"v_mfma_f32_16x16x32_bf16 v[232:235], v[44:47], a[108:111], v[232:235]\n"
"s_waitcnt lgkmcnt(8)\n"
"v_cvt_pk_bf16_f32 v203, v218, v219\n"
"v_cvt_pk_bf16_f32 v220, v220, v221\n"
"v_mfma_f32_16x16x32_bf16 v[236:239], v[48:51], a[104:107], v[236:239]\n"
"s_add_u32 m0, s53, 73728\n"
"ds_read_b64_tr_b16 v[44:45], v2 offset:49152\n"
"ds_read_b64_tr_b16 v[46:47], v2 offset:49408\n"
"v_cvt_pk_bf16_f32 v221, v222, v223\n"
"v_mfma_f32_16x16x32_bf16 v[240:243], v[48:51], a[108:111], v[240:243]\n"
"buffer_load_dwordx4 v181, s[36:39], s84 offen offset:0 lds\n"
"v_cvt_pk_bf16_f32 v222, v224, v225\n"
"v_mfma_f32_16x16x32_bf16 v[228:231], v[52:55], a[120:123], v[228:231]\n"
"ds_read_b64_tr_b16 v[48:49], v5 offset:49152\n"
"ds_read_b64_tr_b16 v[50:51], v5 offset:49408\n"
"v_cvt_pk_bf16_f32 v223, v226, v227\n"
"v_mfma_f32_16x16x32_bf16 v[232:235], v[52:55], a[124:127], v[232:235]\n"
"s_add_u32 s84, s14, 2396160\n"
"v_mfma_f32_16x16x32_bf16 v[236:239], v[56:59], a[120:123], v[236:239]\n"
"s_add_u32 m0, s53, 77824\n"
"ds_read_b64_tr_b16 v[52:53], v2 offset:51200\n"
"ds_read_b64_tr_b16 v[54:55], v2 offset:51456\n"
"v_mfma_f32_16x16x32_bf16 v[240:243], v[56:59], a[124:127], v[240:243]\n"
"s_waitcnt lgkmcnt(8)\n"
"buffer_load_dwordx4 v160, s[36:39], s84 offen offset:0 lds\n"
"v_mfma_f32_16x16x32_bf16 v[228:231], v[124:127], a[136:139], v[228:231]\n"
"ds_read_b64_tr_b16 v[56:57], v5 offset:51200\n"
"ds_read_b64_tr_b16 v[58:59], v5 offset:51456\n"
"v_mfma_f32_16x16x32_bf16 v[232:235], v[124:127], a[140:143], v[232:235]\n"
"v_mfma_f32_16x16x32_bf16 v[236:239], v[128:131], a[136:139], v[236:239]\n"
"s_add_u32 m0, s53, 81920\n"
"ds_read_b64_tr_b16 v[124:125], v2 offset:57344\n"
"ds_read_b64_tr_b16 v[126:127], v2 offset:57600\n"
"v_mfma_f32_16x16x32_bf16 v[240:243], v[128:131], a[140:143], v[240:243]\n"
"buffer_load_dwordx4 v181, s[36:39], s84 offen offset:0 lds\n"
"v_mfma_f32_16x16x32_bf16 v[228:231], v[132:135], a[152:155], v[228:231]\n"
"ds_read_b64_tr_b16 v[128:129], v5 offset:57344\n"
"ds_read_b64_tr_b16 v[130:131], v5 offset:57600\n"
"v_mfma_f32_16x16x32_bf16 v[232:235], v[132:135], a[156:159], v[232:235]\n"
"s_waitcnt lgkmcnt(8)\n"
"v_mfma_f32_16x16x32_bf16 v[236:239], v[136:139], a[152:155], v[236:239]\n"
"s_add_u32 m0, s53, 86016\n"
"ds_read_b64_tr_b16 v[132:133], v2 offset:59392\n"
"ds_read_b64_tr_b16 v[134:135], v2 offset:59648\n"
"v_mfma_f32_16x16x32_bf16 v[240:243], v[136:139], a[156:159], v[240:243]\n"
"s_add_u32 s84, s15, 2396160\n"
"buffer_load_dwordx4 v160, s[36:39], s84 offen offset:0 lds\n"
"v_mfma_f32_16x16x32_bf16 v[228:231], v[44:47], a[40:43], v[228:231]\n"
"ds_read_b64_tr_b16 v[136:137], v5 offset:59392\n"
"ds_read_b64_tr_b16 v[138:139], v5 offset:59648\n"
"v_mfma_f32_16x16x32_bf16 v[232:235], v[44:47], a[44:47], v[232:235]\n"
"v_mfma_f32_16x16x32_bf16 v[236:239], v[48:51], a[40:43], v[236:239]\n"
"s_add_u32 m0, s53, 155648\n"
"ds_read_b64_tr_b16 v[44:45], v3 offset:16384\n"
"ds_read_b64_tr_b16 v[46:47], v3 offset:16640\n"
"v_mfma_f32_16x16x32_bf16 v[240:243], v[48:51], a[44:47], v[240:243]\n"
"s_waitcnt lgkmcnt(8)\n"
"buffer_load_dwordx4 v181, s[36:39], s84 offen offset:0 lds\n"
"v_mfma_f32_16x16x32_bf16 v[228:231], v[52:55], a[56:59], v[228:231]\n"
"ds_read_b64_tr_b16 v[48:49], v6 offset:16384\n"
"ds_read_b64_tr_b16 v[50:51], v6 offset:16640\n"
"v_mfma_f32_16x16x32_bf16 v[232:235], v[52:55], a[60:63], v[232:235]\n"
"v_mfma_f32_16x16x32_bf16 v[236:239], v[56:59], a[56:59], v[236:239]\n"
"s_add_u32 m0, s53, 36864\n"
"ds_read_b64_tr_b16 v[52:53], v3 offset:18432\n"
"ds_read_b64_tr_b16 v[54:55], v3 offset:18688\n"
"v_mfma_f32_16x16x32_bf16 v[240:243], v[56:59], a[60:63], v[240:243]\n"
"s_add_u32 s84, s55, 0\n"
"buffer_load_dwordx4 v160, s[36:39], s84 offen offset:0 lds\n"
"v_mfma_f32_16x16x32_bf16 v[228:231], v[124:127], a[200:203], v[228:231]\n"
"ds_read_b64_tr_b16 v[56:57], v6 offset:18432\n"
"ds_read_b64_tr_b16 v[58:59], v6 offset:18688\n"
"v_mfma_f32_16x16x32_bf16 v[232:235], v[124:127], a[204:207], v[232:235]\n"
"s_waitcnt lgkmcnt(8)\n"
"v_mfma_f32_16x16x32_bf16 v[236:239], v[128:131], a[200:203], v[236:239]\n"
"s_add_u32 m0, s53, 90112\n"
"ds_read_b64_tr_b16 v[124:125], v1 offset:4096\n"
"ds_read_b64_tr_b16 v[126:127], v1 offset:4352\n"
"v_mfma_f32_16x16x32_bf16 v[240:243], v[128:131], a[204:207], v[240:243]\n"
"buffer_load_dwordx4 v181, s[36:39], s84 offen offset:0 lds\n"
"v_mfma_f32_16x16x32_bf16 v[228:231], v[132:135], a[216:219], v[228:231]\n"
"ds_read_b64_tr_b16 v[128:129], v4 offset:4096\n"
"ds_read_b64_tr_b16 v[130:131], v4 offset:4352\n"
"v_mfma_f32_16x16x32_bf16 v[232:235], v[132:135], a[220:223], v[232:235]\n"
"s_add_u32 s84, s68, 0\n"
"v_mfma_f32_16x16x32_bf16 v[236:239], v[136:139], a[216:219], v[236:239]\n"
"s_add_u32 m0, s53, 94208\n"
"ds_read_b64_tr_b16 v[132:133], v1 offset:6144\n"
"ds_read_b64_tr_b16 v[134:135], v1 offset:6400\n"
"v_mfma_f32_16x16x32_bf16 v[240:243], v[136:139], a[220:223], v[240:243]\n"
"s_waitcnt lgkmcnt(8)\n"
"buffer_load_dwordx4 v160, s[36:39], s84 offen offset:0 lds\n"
"v_mfma_f32_16x16x32_bf16 v[228:231], v[44:47], a[232:235], v[228:231]\n"
"ds_read_b64_tr_b16 v[136:137], v4 offset:6144\n"
"ds_read_b64_tr_b16 v[138:139], v4 offset:6400\n"
"v_mfma_f32_16x16x32_bf16 v[232:235], v[44:47], a[236:239], v[232:235]\n"
"s_waitcnt vmcnt(17)\n"
"s_barrier\n"
"v_mfma_f32_16x16x32_bf16 v[236:239], v[48:51], a[232:235], v[236:239]\n"
"s_add_u32 m0, s53, 98304\n"
"ds_read_b64_tr_b16 v[44:45], v1 offset:28672\n"
"ds_read_b64_tr_b16 v[46:47], v1 offset:28928\n"
"v_mfma_f32_16x16x32_bf16 v[240:243], v[48:51], a[236:239], v[240:243]\n"
"buffer_load_dwordx4 v181, s[36:39], s84 offen offset:0 lds\n"
"v_mfma_f32_16x16x32_bf16 v[228:231], v[52:55], a[248:251], v[228:231]\n"
"ds_read_b64_tr_b16 v[48:49], v4 offset:28672\n"
"ds_read_b64_tr_b16 v[50:51], v4 offset:28928\n"
"v_mfma_f32_16x16x32_bf16 v[232:235], v[52:55], a[252:255], v[232:235]\n"
"s_waitcnt lgkmcnt(8)\n"
"v_mfma_f32_16x16x32_bf16 v[236:239], v[56:59], a[248:251], v[236:239]\n"
"ds_read_b64_tr_b16 v[52:53], v1 offset:30720\n"
"ds_read_b64_tr_b16 v[54:55], v1 offset:30976\n"
"v_mfma_f32_16x16x32_bf16 v[240:243], v[56:59], a[252:255], v[240:243]\n"
"buffer_store_dwordx4 v[188:191], v10, s[28:31], s77 offen offset:0 sc1\n"
"v_mfma_f32_16x16x32_bf16 v[228:231], v[124:127], v[20:23], v[228:231]\n"
"ds_read_b64_tr_b16 v[56:57], v4 offset:30720\n"
"ds_read_b64_tr_b16 v[58:59], v4 offset:30976\n"
"v_mfma_f32_16x16x32_bf16 v[232:235], v[124:127], v[24:27], v[232:235]\n"
"v_mfma_f32_16x16x32_bf16 v[236:239], v[128:131], v[20:23], v[236:239]\n"
"ds_read_b64_tr_b16 v[124:125], v2 offset:40960\n"
"ds_read_b64_tr_b16 v[126:127], v2 offset:41216\n"
"v_mfma_f32_16x16x32_bf16 v[240:243], v[128:131], v[24:27], v[240:243]\n"
"s_waitcnt lgkmcnt(8)\n"
"buffer_store_dwordx4 v[192:195], v10, s[28:31], s77 offen offset:1024 sc1\n"
"v_mfma_f32_16x16x32_bf16 v[228:231], v[132:135], v[36:39], v[228:231]\n"
"ds_read_b64_tr_b16 v[128:129], v5 offset:40960\n"
"ds_read_b64_tr_b16 v[130:131], v5 offset:41216\n"
"v_mfma_f32_16x16x32_bf16 v[232:235], v[132:135], v[40:43], v[232:235]\n"
"s_waitcnt vmcnt(12)\n"
"v_mfma_f32_16x16x32_bf16 v[236:239], v[136:139], v[36:39], v[236:239]\n"
"ds_read_b64_tr_b16 v[132:133], v2 offset:43008\n"
"ds_read_b64_tr_b16 v[134:135], v2 offset:43264\n"
"v_mfma_f32_16x16x32_bf16 v[240:243], v[136:139], v[40:43], v[240:243]\n"
"buffer_store_dwordx4 v[196:199], v10, s[28:31], s78 offen offset:0 sc1\n"
"v_mfma_f32_16x16x32_bf16 v[228:231], v[44:47], a[168:171], v[228:231]\n"
"ds_read_b64_tr_b16 v[136:137], v5 offset:43008\n"
"ds_read_b64_tr_b16 v[138:139], v5 offset:43264\n"
"v_mfma_f32_16x16x32_bf16 v[232:235], v[44:47], a[172:175], v[232:235]\n"
"s_waitcnt lgkmcnt(8)\n"
"ds_read_b64_tr_b16 v[84:85], v252 offset:2048\n"
"v_mfma_f32_16x16x32_bf16 v[236:239], v[48:51], a[168:171], v[236:239]\n"
"ds_read_b64_tr_b16 v[44:45], v1 offset:12288\n"
"ds_read_b64_tr_b16 v[46:47], v1 offset:12544\n"
"v_mfma_f32_16x16x32_bf16 v[240:243], v[48:51], a[172:175], v[240:243]\n"
"buffer_store_dwordx4 v[200:203], v10, s[28:31], s78 offen offset:1024 sc1\n"
"v_mfma_f32_16x16x32_bf16 v[228:231], v[52:55], a[184:187], v[228:231]\n"
"ds_read_b64_tr_b16 v[48:49], v4 offset:12288\n"
"ds_read_b64_tr_b16 v[50:51], v4 offset:12544\n"
"v_mfma_f32_16x16x32_bf16 v[232:235], v[52:55], a[188:191], v[232:235]\n"
"ds_read_b64_tr_b16 v[88:89], v253 offset:2048\n"
"v_mfma_f32_16x16x32_bf16 v[236:239], v[56:59], a[184:187], v[236:239]\n"
"ds_read_b64_tr_b16 v[52:53], v1 offset:14336\n"
"ds_read_b64_tr_b16 v[54:55], v1 offset:14592\n"
"v_mfma_f32_16x16x32_bf16 v[240:243], v[56:59], a[188:191], v[240:243]\n"
"s_waitcnt lgkmcnt(10)\n"
"buffer_store_dwordx4 v[220:223], v10, s[28:31], s80 offen offset:0 sc1\n"
"v_mfma_f32_16x16x32_bf16 v[228:231], v[124:127], a[0:3], v[228:231]\n"
"ds_read_b64_tr_b16 v[56:57], v4 offset:14336\n"
"ds_read_b64_tr_b16 v[58:59], v4 offset:14592\n"
"v_mfma_f32_16x16x32_bf16 v[232:235], v[124:127], a[4:7], v[232:235]\n"
"ds_read_b64_tr_b16 v[92:93], v252 offset:2304\n"
"v_mfma_f32_16x16x32_bf16 v[236:239], v[128:131], a[0:3], v[236:239]\n"
"ds_read_b64_tr_b16 v[124:125], v1 offset:32768\n"
"ds_read_b64_tr_b16 v[126:127], v1 offset:33024\n"
"v_mfma_f32_16x16x32_bf16 v[240:243], v[128:131], a[4:7], v[240:243]\n"
"ds_read_b64_tr_b16 v[96:97], v253 offset:2304\n"
"v_mfma_f32_16x16x32_bf16 v[228:231], v[132:135], a[16:19], v[228:231]\n"
"ds_read_b64_tr_b16 v[128:129], v4 offset:32768\n"
"ds_read_b64_tr_b16 v[130:131], v4 offset:33024\n"
"v_mfma_f32_16x16x32_bf16 v[232:235], v[132:135], a[20:23], v[232:235]\n"
"s_waitcnt lgkmcnt(11)\n"
"ds_read_b64_tr_b16 v[100:101], v254 offset:0\n"
"v_mfma_f32_16x16x32_bf16 v[236:239], v[136:139], a[16:19], v[236:239]\n"
"ds_read_b64_tr_b16 v[132:133], v1 offset:34816\n"
"ds_read_b64_tr_b16 v[134:135], v1 offset:35072\n"
"v_mfma_f32_16x16x32_bf16 v[240:243], v[136:139], a[20:23], v[240:243]\n"
"ds_bpermute_b32 v104, v172, v104\n"
"v_mfma_f32_16x16x32_bf16 v[228:231], v[44:47], a[32:35], v[228:231]\n"
"ds_read_b64_tr_b16 v[136:137], v4 offset:34816\n"
"ds_read_b64_tr_b16 v[138:139], v4 offset:35072\n"
"v_mfma_f32_16x16x32_bf16 v[232:235], v[44:47], a[36:39], v[232:235]\n"
"ds_bpermute_b32 v105, v172, v105\n"
"v_mfma_f32_16x16x32_bf16 v[236:239], v[48:51], a[32:35], v[236:239]\n"
"ds_read_b64_tr_b16 v[44:45], v2 offset:4096\n"
"ds_read_b64_tr_b16 v[46:47], v2 offset:4352\n"
"v_mfma_f32_16x16x32_bf16 v[240:243], v[48:51], a[36:39], v[240:243]\n"
"s_waitcnt lgkmcnt(12)\n"
"ds_bpermute_b32 v106, v172, v106\n"
"v_mfma_f32_16x16x32_bf16 v[228:231], v[52:55], a[48:51], v[228:231]\n"
"s_waitcnt lgkmcnt(0)\n"
"ds_read_b64_tr_b16 v[48:49], v5 offset:4096\n"
"ds_read_b64_tr_b16 v[50:51], v5 offset:4352\n"
"v_mfma_f32_16x16x32_bf16 v[232:235], v[52:55], a[52:55], v[232:235]\n"
"v_sub_f32 v185, 1.0, v104\n"
"v_sub_f32 v186, 1.0, v105\n"
"v_mfma_f32_16x16x32_bf16 v[236:239], v[56:59], a[48:51], v[236:239]\n"
"ds_read_b64_tr_b16 v[52:53], v2 offset:6144\n"
"ds_read_b64_tr_b16 v[54:55], v2 offset:6400\n"
"v_mfma_f32_16x16x32_bf16 v[240:243], v[56:59], a[52:55], v[240:243]\n"
"s_lshl_b32 s56, s52, 12\n"
"v_sub_f32 v187, 1.0, v106\n"
"v_lshlrev_b32 v244, 16, v84\n"
"v_mfma_f32_16x16x32_bf16 v[228:231], v[124:127], a[64:67], v[228:231]\n"
"ds_read_b64_tr_b16 v[56:57], v5 offset:6144\n"
"ds_read_b64_tr_b16 v[58:59], v5 offset:6400\n"
"v_mfma_f32_16x16x32_bf16 v[232:235], v[124:127], a[68:71], v[232:235]\n"
"s_waitcnt vmcnt(6)\n"
"s_barrier\n"
"v_and_b32 v245, s81, v84\n"
"v_mfma_f32_16x16x32_bf16 v[236:239], v[128:131], a[64:67], v[236:239]\n"
"s_add_u32 s56, s56, 131072\n"
"ds_read_b64_tr_b16 v[124:125], v1 offset:20480\n"
"ds_read_b64_tr_b16 v[126:127], v1 offset:20736\n"
"v_mfma_f32_16x16x32_bf16 v[240:243], v[128:131], a[68:71], v[240:243]\n"
"s_mul_i32 s57, s11, 1280\n"
"v_lshlrev_b32 v246, 16, v85\n"
"v_and_b32 v247, s81, v85\n"
"v_mfma_f32_16x16x32_bf16 v[228:231], v[132:135], a[80:83], v[228:231]\n"
"s_add_u32 s58, s56, s57\n"
"ds_read_b64_tr_b16 v[128:129], v4 offset:20480\n"
"ds_read_b64_tr_b16 v[130:131], v4 offset:20736\n"
"v_mfma_f32_16x16x32_bf16 v[232:235], v[132:135], a[84:87], v[232:235]\n"
"v_mul_f32 v244, v244, v104\n"
"v_mul_f32 v245, v245, v104\n"
"v_mfma_f32_16x16x32_bf16 v[236:239], v[136:139], a[80:83], v[236:239]\n"
"ds_read_b64_tr_b16 v[132:133], v1 offset:22528\n"
"ds_read_b64_tr_b16 v[134:135], v1 offset:22784\n"
"v_mfma_f32_16x16x32_bf16 v[240:243], v[136:139], a[84:87], v[240:243]\n"
"s_waitcnt lgkmcnt(8)\n"
"v_mul_f32 v246, v246, v104\n"
"v_mul_f32 v247, v247, v104\n"
"v_mfma_f32_16x16x32_bf16 v[228:231], v[44:47], a[96:99], v[228:231]\n"
"s_lshl_b32 s60, s11, 10\n"
"ds_read_b64_tr_b16 v[136:137], v4 offset:22528\n"
"ds_read_b64_tr_b16 v[138:139], v4 offset:22784\n"
"v_mfma_f32_16x16x32_bf16 v[232:235], v[44:47], a[100:103], v[232:235]\n"
"s_add_u32 s60, s60, s56\n"
"v_lshlrev_b32 v248, 16, v88\n"
"v_and_b32 v249, s81, v88\n"
"v_mfma_f32_16x16x32_bf16 v[236:239], v[48:51], a[96:99], v[236:239]\n"
"s_and_b32 s61, s52, 1\n"
"ds_read_b64_tr_b16 v[44:45], v2 offset:12288\n"
"ds_read_b64_tr_b16 v[46:47], v2 offset:12544\n"
"v_mfma_f32_16x16x32_bf16 v[240:243], v[48:51], a[100:103], v[240:243]\n"
"s_lshl_b32 s61, s61, 5\n"
"v_lshlrev_b32 v250, 16, v89\n"
"v_and_b32 v251, s81, v89\n"
"v_mfma_f32_16x16x32_bf16 v[228:231], v[52:55], a[112:115], v[228:231]\n"
"s_lshl_b32 m0, s52, 12\ns_add_u32 m0, m0, 131072\n"
"ds_read_b64_tr_b16 v[48:49], v5 offset:12288\n"
"ds_read_b64_tr_b16 v[50:51], v5 offset:12544\n"
"v_mfma_f32_16x16x32_bf16 v[232:235], v[52:55], a[116:119], v[232:235]\n"
"s_waitcnt lgkmcnt(8)\n"
"buffer_load_dwordx4 v173, s[4:7], s25 offen offset:0 lds\n"
"v_mfma_f32_16x16x32_bf16 v[236:239], v[56:59], a[112:115], v[236:239]\n"
"s_add_u32 s61, s61, s56\n"
"ds_read_b64_tr_b16 v[52:53], v2 offset:14336\n"
"ds_read_b64_tr_b16 v[54:55], v2 offset:14592\n"
"v_mfma_f32_16x16x32_bf16 v[240:243], v[56:59], a[116:119], v[240:243]\n"
"v_mul_f32 v248, v248, v105\n"
"v_mul_f32 v249, v249, v105\n"
"v_mfma_f32_16x16x32_bf16 v[228:231], v[124:127], a[128:131], v[228:231]\n"
"s_lshl_b32 m0, s52, 12\ns_add_u32 m0, m0, 132096\n"
"ds_read_b64_tr_b16 v[56:57], v5 offset:14336\n"
"ds_read_b64_tr_b16 v[58:59], v5 offset:14592\n"
"v_mfma_f32_16x16x32_bf16 v[232:235], v[124:127], a[132:135], v[232:235]\n"
"buffer_load_dwordx4 v173, s[4:7], s26 offen offset:0 lds\n"
"v_mfma_f32_16x16x32_bf16 v[236:239], v[128:131], a[128:131], v[236:239]\n"
"ds_read_b64_tr_b16 v[124:125], v2 offset:20480\n"
"ds_read_b64_tr_b16 v[126:127], v2 offset:20736\n"
"v_mfma_f32_16x16x32_bf16 v[240:243], v[128:131], a[132:135], v[240:243]\n"
"s_waitcnt lgkmcnt(8)\n"
"v_mul_f32 v250, v250, v105\n"
"v_mul_f32 v251, v251, v105\n"
"v_mfma_f32_16x16x32_bf16 v[228:231], v[132:135], a[144:147], v[228:231]\n"
"s_lshl_b32 m0, s52, 12\ns_add_u32 m0, m0, 133120\n"
"ds_read_b64_tr_b16 v[128:129], v5 offset:20480\n"
"ds_read_b64_tr_b16 v[130:131], v5 offset:20736\n"
"v_mfma_f32_16x16x32_bf16 v[232:235], v[132:135], a[148:151], v[232:235]\n"
"buffer_load_dwordx4 v173, s[4:7], s27 offen offset:0 lds\n"
"v_mfma_f32_16x16x32_bf16 v[236:239], v[136:139], a[144:147], v[236:239]\n"
"ds_read_b64_tr_b16 v[132:133], v2 offset:22528\n"
"ds_read_b64_tr_b16 v[134:135], v2 offset:22784\n"
"v_mfma_f32_16x16x32_bf16 v[240:243], v[136:139], a[148:151], v[240:243]\n"
"s_sub_u32 s59, s56, s57\n"
"v_lshlrev_b32 v220, 16, v92\n"
"v_and_b32 v221, s81, v92\n"
"v_mfma_f32_16x16x32_bf16 v[228:231], v[44:47], a[160:163], v[228:231]\n"
"ds_read_b64_tr_b16 v[136:137], v5 offset:22528\n"
"ds_read_b64_tr_b16 v[138:139], v5 offset:22784\n"
"v_mfma_f32_16x16x32_bf16 v[232:235], v[44:47], a[164:167], v[232:235]\n"
"s_waitcnt lgkmcnt(8)\n"
"v_lshlrev_b32 v222, 16, v93\n"
"v_and_b32 v223, s81, v93\n"
"v_mfma_f32_16x16x32_bf16 v[236:239], v[48:51], a[160:163], v[236:239]\n"
"s_add_u32 s59, s59, 1280\n"
"ds_read_b64_tr_b16 v[44:45], v1 offset:36864\n"
"ds_read_b64_tr_b16 v[46:47], v1 offset:37120\n"
"v_mfma_f32_16x16x32_bf16 v[240:243], v[48:51], a[164:167], v[240:243]\n"
"v_mul_f32 v220, v220, v104\n"
"v_mul_f32 v221, v221, v104\n"
"v_mfma_f32_16x16x32_bf16 v[228:231], v[52:55], a[176:179], v[228:231]\n"
"ds_read_b64_tr_b16 v[48:49], v4 offset:36864\n"
"ds_read_b64_tr_b16 v[50:51], v4 offset:37120\n"
"v_mfma_f32_16x16x32_bf16 v[232:235], v[52:55], a[180:183], v[232:235]\n"
"v_mul_f32 v222, v222, v104\n"
"v_mul_f32 v223, v223, v104\n"
"v_mfma_f32_16x16x32_bf16 v[236:239], v[56:59], a[176:179], v[236:239]\n"
"ds_read_b64_tr_b16 v[52:53], v1 offset:38912\n"
"ds_read_b64_tr_b16 v[54:55], v1 offset:39168\n"
"v_mfma_f32_16x16x32_bf16 v[240:243], v[56:59], a[180:183], v[240:243]\n"
"s_waitcnt lgkmcnt(8)\n"
"v_lshlrev_b32 v224, 16, v96\n"
"v_and_b32 v225, s81, v96\n"
"v_mfma_f32_16x16x32_bf16 v[228:231], v[124:127], a[192:195], v[228:231]\n"
"ds_read_b64_tr_b16 v[56:57], v4 offset:38912\n"
"ds_read_b64_tr_b16 v[58:59], v4 offset:39168\n"
"v_mfma_f32_16x16x32_bf16 v[232:235], v[124:127], a[196:199], v[232:235]\n"
"v_lshlrev_b32 v226, 16, v97\n"
"v_and_b32 v227, s81, v97\n"
"v_mfma_f32_16x16x32_bf16 v[236:239], v[128:131], a[192:195], v[236:239]\n"
"ds_read_b64_tr_b16 v[124:125], v2 offset:28672\n"
"ds_read_b64_tr_b16 v[126:127], v2 offset:28928\n"
"v_mfma_f32_16x16x32_bf16 v[240:243], v[128:131], a[196:199], v[240:243]\n"
"v_mul_f32 v224, v224, v105\n"
"v_mul_f32 v225, v225, v105\n"
"v_mfma_f32_16x16x32_bf16 v[228:231], v[132:135], a[208:211], v[228:231]\n"
"ds_read_b64_tr_b16 v[128:129], v5 offset:28672\n"
"ds_read_b64_tr_b16 v[130:131], v5 offset:28928\n"
"v_mfma_f32_16x16x32_bf16 v[232:235], v[132:135], a[212:215], v[232:235]\n"
"s_waitcnt lgkmcnt(8)\n"
"v_mul_f32 v226, v226, v105\n"
"v_mul_f32 v227, v227, v105\n"
"v_mfma_f32_16x16x32_bf16 v[236:239], v[136:139], a[208:211], v[236:239]\n"
"ds_read_b64_tr_b16 v[132:133], v2 offset:30720\n"
"ds_read_b64_tr_b16 v[134:135], v2 offset:30976\n"
"v_mfma_f32_16x16x32_bf16 v[240:243], v[136:139], a[212:215], v[240:243]\n"
"v_lshlrev_b32 v204, 16, v100\n"
"v_and_b32 v205, s81, v100\n"
"v_mfma_f32_16x16x32_bf16 v[228:231], v[44:47], a[224:227], v[228:231]\n"
"ds_read_b64_tr_b16 v[136:137], v5 offset:30720\n"
"ds_read_b64_tr_b16 v[138:139], v5 offset:30976\n"
"v_mfma_f32_16x16x32_bf16 v[232:235], v[44:47], a[228:231], v[232:235]\n"
"s_waitcnt vmcnt(8)\n"
"s_barrier\n"
"v_lshlrev_b32 v206, 16, v101\n"
"v_mfma_f32_16x16x32_bf16 v[236:239], v[48:51], a[224:227], v[236:239]\n"
"ds_read_b64_tr_b16 v[44:45], v1 offset:40960\n"
"ds_read_b64_tr_b16 v[46:47], v1 offset:41216\n"
"v_mfma_f32_16x16x32_bf16 v[240:243], v[48:51], a[228:231], v[240:243]\n"
"s_waitcnt lgkmcnt(8)\n"
"v_and_b32 v207, s81, v101\n"
"v_mul_f32 v204, v204, v106\n"
"v_mfma_f32_16x16x32_bf16 v[228:231], v[52:55], a[240:243], v[228:231]\n"
"ds_read_b64_tr_b16 v[48:49], v4 offset:40960\n"
"ds_read_b64_tr_b16 v[50:51], v4 offset:41216\n"
"v_mfma_f32_16x16x32_bf16 v[232:235], v[52:55], a[244:247], v[232:235]\n"
"v_mul_f32 v205, v205, v106\n"
"v_mul_f32 v206, v206, v106\n"
"v_mfma_f32_16x16x32_bf16 v[236:239], v[56:59], a[240:243], v[236:239]\n"
"ds_read_b64_tr_b16 v[52:53], v1 offset:43008\n"
"ds_read_b64_tr_b16 v[54:55], v1 offset:43264\n"
"v_mfma_f32_16x16x32_bf16 v[240:243], v[56:59], a[244:247], v[240:243]\n"
"v_mul_f32 v207, v207, v106\n"
"v_bfe_u32 v253, v0, 4, 2\n"
"v_mfma_f32_16x16x32_bf16 v[228:231], v[124:127], v[12:15], v[228:231]\n"
"ds_read_b64_tr_b16 v[56:57], v4 offset:43008\n"
"ds_read_b64_tr_b16 v[58:59], v4 offset:43264\n"
"v_mfma_f32_16x16x32_bf16 v[232:235], v[124:127], v[16:19], v[232:235]\n"
"s_waitcnt lgkmcnt(8)\n"
"v_lshlrev_b32 v253, 3, v253\n"
"v_mul_u32_u24 v252, 5, v172\n"
"v_mfma_f32_16x16x32_bf16 v[236:239], v[128:131], v[12:15], v[236:239]\n"
"ds_read_b64_tr_b16 v[124:125], v1 offset:49152\n"
"ds_read_b64_tr_b16 v[126:127], v1 offset:49408\n"
"v_mfma_f32_16x16x32_bf16 v[240:243], v[128:131], v[16:19], v[240:243]\n"
"v_lshl_add_u32 v112, v172, 2, v253\n"
"v_add_u32 v252, v252, v253\n"
"v_mfma_f32_16x16x32_bf16 v[228:231], v[132:135], v[28:31], v[228:231]\n"
"ds_read_b64_tr_b16 v[128:129], v4 offset:49152\n"
"ds_read_b64_tr_b16 v[130:131], v4 offset:49408\n"
"v_mfma_f32_16x16x32_bf16 v[232:235], v[132:135], v[32:35], v[232:235]\n"
"v_add_u32 v114, s61, v112\n"
"v_add_u32 v253, s59, v252\n"
"v_mfma_f32_16x16x32_bf16 v[236:239], v[136:139], v[28:31], v[236:239]\n"
"ds_read_b64_tr_b16 v[132:133], v1 offset:51200\n"
"ds_read_b64_tr_b16 v[134:135], v1 offset:51456\n"
"v_mfma_f32_16x16x32_bf16 v[240:243], v[136:139], v[32:35], v[240:243]\n"
"s_waitcnt lgkmcnt(8)\n"
"v_add_u32 v112, s60, v112\n"
"v_add_u32 v252, s58, v252\n"
"v_mfma_f32_16x16x32_bf16 v[140:143], v[44:47], a[8:11], 0\n"
"ds_read_b64_tr_b16 v[136:137], v4 offset:51200\n"
"ds_read_b64_tr_b16 v[138:139], v4 offset:51456\n"
"v_mfma_f32_16x16x32_bf16 v[144:147], v[44:47], a[12:15], 0\n"
"v_xor_b32 v113, 1024, v112\n"
"v_bfe_u32 v184, v0, 2, 4\n"
"v_mfma_f32_16x16x32_bf16 v[148:151], v[48:51], a[8:11], 0\n"
"ds_read_b64_tr_b16 v[44:45], v1 offset:57344\n"
"ds_read_b64_tr_b16 v[46:47], v1 offset:57600\n"
"v_mfma_f32_16x16x32_bf16 v[152:155], v[48:51], a[12:15], 0\n"
"v_and_b32 v183, 3, v0\n"
"v_mul_u32_u24 v184, 80, v184\n"
"v_mfma_f32_16x16x32_bf16 v[140:143], v[52:55], a[24:27], v[140:143]\n"
"ds_read_b64_tr_b16 v[48:49], v4 offset:57344\n"
"ds_read_b64_tr_b16 v[50:51], v4 offset:57600\n"
"v_mfma_f32_16x16x32_bf16 v[144:147], v[52:55], a[28:31], v[144:147]\n"
"s_waitcnt lgkmcnt(8)\n"
"v_cvt_pk_bf16_f32 v228, v228, v229\n"
"v_lshlrev_b32 v183, 4, v183\n"
"v_mfma_f32_16x16x32_bf16 v[148:151], v[56:59], a[24:27], v[148:151]\n"
"ds_read_b64_tr_b16 v[52:53], v1 offset:59392\n"
"ds_read_b64_tr_b16 v[54:55], v1 offset:59648\n"
"v_cvt_pk_bf16_f32 v229, v230, v231\n"
"v_mfma_f32_16x16x32_bf16 v[152:155], v[56:59], a[28:31], v[152:155]\n"
"v_cvt_pk_bf16_f32 v230, v232, v233\n"
"v_cvt_pk_bf16_f32 v231, v234, v235\n"
"v_mfma_f32_16x16x32_bf16 v[140:143], v[124:127], a[72:75], v[140:143]\n"
"ds_read_b64_tr_b16 v[56:57], v4 offset:59392\n"
"ds_read_b64_tr_b16 v[58:59], v4 offset:59648\n"
"v_cvt_pk_bf16_f32 v232, v236, v237\n"
"v_cvt_pk_bf16_f32 v233, v238, v239\n"
"v_mfma_f32_16x16x32_bf16 v[144:147], v[124:127], a[76:79], v[144:147]\n"
"v_cvt_pk_bf16_f32 v234, v240, v241\n"
"v_cvt_pk_bf16_f32 v235, v242, v243\n"
"v_mfma_f32_16x16x32_bf16 v[148:151], v[128:131], a[72:75], v[148:151]\n"
"ds_read_b64_tr_b16 v[124:125], v2 offset:45056\n"
"ds_read_b64_tr_b16 v[126:127], v2 offset:45312\n"
"v_mfma_f32_16x16x32_bf16 v[152:155], v[128:131], a[76:79], v[152:155]\n"
"s_waitcnt lgkmcnt(8)\n"
"buffer_store_dwordx4 v[228:231], v10, s[28:31], s79 offen offset:0 sc1\n"
"v_mfma_f32_16x16x32_bf16 v[140:143], v[132:135], a[88:91], v[140:143]\n"
"ds_read_b64_tr_b16 v[128:129], v5 offset:45056\n"
"ds_read_b64_tr_b16 v[130:131], v5 offset:45312\n"
"v_mfma_f32_16x16x32_bf16 v[144:147], v[132:135], a[92:95], v[144:147]\n"
"v_add3_u32 v184, v184, v183, s56\n"
"v_mfma_f32_16x16x32_bf16 v[148:151], v[136:139], a[88:91], v[148:151]\n"
"ds_read_b64_tr_b16 v[132:133], v2 offset:47104\n"
"ds_read_b64_tr_b16 v[134:135], v2 offset:47360\n"
"v_mfma_f32_16x16x32_bf16 v[152:155], v[136:139], a[92:95], v[152:155]\n"
"buffer_store_dwordx4 v[232:235], v10, s[28:31], s79 offen offset:1024 sc1\n"
"v_mfma_f32_16x16x32_bf16 v[140:143], v[44:47], a[104:107], v[140:143]\n"
"ds_read_b64_tr_b16 v[136:137], v5 offset:47104\n"
"ds_read_b64_tr_b16 v[138:139], v5 offset:47360\n"
"v_mfma_f32_16x16x32_bf16 v[144:147], v[44:47], a[108:111], v[144:147]\n"
"s_waitcnt lgkmcnt(8)\n"
"v_mfma_f32_16x16x32_bf16 v[148:151], v[48:51], a[104:107], v[148:151]\n"
"ds_read_b64_tr_b16 v[44:45], v2 offset:53248\n"
"ds_read_b64_tr_b16 v[46:47], v2 offset:53504\n"
"v_mfma_f32_16x16x32_bf16 v[152:155], v[48:51], a[108:111], v[152:155]\n"
"v_mfma_f32_16x16x32_bf16 v[140:143], v[52:55], a[120:123], v[140:143]\n"
"ds_read_b64_tr_b16 v[48:49], v5 offset:53248\n"
"ds_read_b64_tr_b16 v[50:51], v5 offset:53504\n"
"v_mfma_f32_16x16x32_bf16 v[144:147], v[52:55], a[124:127], v[144:147]\n"
"v_mfma_f32_16x16x32_bf16 v[148:151], v[56:59], a[120:123], v[148:151]\n"
"ds_read_b64_tr_b16 v[52:53], v2 offset:55296\n"
"ds_read_b64_tr_b16 v[54:55], v2 offset:55552\n"
"v_mfma_f32_16x16x32_bf16 v[152:155], v[56:59], a[124:127], v[152:155]\n"
"s_waitcnt lgkmcnt(8)\n"
"v_mfma_f32_16x16x32_bf16 v[140:143], v[124:127], a[136:139], v[140:143]\n"
"ds_read_b64_tr_b16 v[56:57], v5 offset:55296\n"
"ds_read_b64_tr_b16 v[58:59], v5 offset:55552\n"
"v_mfma_f32_16x16x32_bf16 v[144:147], v[124:127], a[140:143], v[144:147]\n"
"s_waitcnt vmcnt(2)\n"
"v_mfma_f32_16x16x32_bf16 v[148:151], v[128:131], a[136:139], v[148:151]\n"
"ds_read_b64_tr_b16 v[124:125], v2 offset:61440\n"
"ds_read_b64_tr_b16 v[126:127], v2 offset:61696\n"
"v_mfma_f32_16x16x32_bf16 v[152:155], v[128:131], a[140:143], v[152:155]\n"
"ds_read_b64 v[86:87], v112 offset:0\n"
"v_mfma_f32_16x16x32_bf16 v[140:143], v[132:135], a[152:155], v[140:143]\n"
"ds_read_b64_tr_b16 v[128:129], v5 offset:61440\n"
"ds_read_b64_tr_b16 v[130:131], v5 offset:61696\n"
"v_mfma_f32_16x16x32_bf16 v[144:147], v[132:135], a[156:159], v[144:147]\n"
"s_waitcnt lgkmcnt(9)\n"
"ds_read_b64 v[90:91], v113 offset:0\n"
"v_mfma_f32_16x16x32_bf16 v[148:151], v[136:139], a[152:155], v[148:151]\n"
"ds_read_b64_tr_b16 v[132:133], v2 offset:63488\n"
"ds_read_b64_tr_b16 v[134:135], v2 offset:63744\n"
"v_mfma_f32_16x16x32_bf16 v[152:155], v[136:139], a[156:159], v[152:155]\n"
"ds_read_b64 v[94:95], v112 offset:32\n"
"v_mfma_f32_16x16x32_bf16 v[140:143], v[44:47], a[40:43], v[140:143]\n"
"ds_read_b64_tr_b16 v[136:137], v5 offset:63488\n"
"ds_read_b64_tr_b16 v[138:139], v5 offset:63744\n"
"v_mfma_f32_16x16x32_bf16 v[144:147], v[44:47], a[44:47], v[144:147]\n"
"ds_read_b64 v[98:99], v113 offset:32\n"
"v_mfma_f32_16x16x32_bf16 v[148:151], v[48:51], a[40:43], v[148:151]\n"
"ds_read_b64_tr_b16 v[44:45], v1 offset:0\n"
"ds_read_b64_tr_b16 v[46:47], v1 offset:256\n"
"v_mfma_f32_16x16x32_bf16 v[152:155], v[48:51], a[44:47], v[152:155]\n"
"s_waitcnt lgkmcnt(12)\n"
"ds_read_b64 v[102:103], v114 offset:2048\n"
"v_mfma_f32_16x16x32_bf16 v[140:143], v[52:55], a[56:59], v[140:143]\n"
"s_waitcnt lgkmcnt(0)\n"
"ds_read_b64_tr_b16 v[48:49], v4 offset:0\n"
"ds_read_b64_tr_b16 v[50:51], v4 offset:256\n"
"v_mfma_f32_16x16x32_bf16 v[144:147], v[52:55], a[60:63], v[144:147]\n"
"buffer_load_dwordx4 v[60:63], v10, s[28:31], s74 offen offset:0 sc1\n"
"v_lshlrev_b32 v108, 16, v86\n"
"v_mfma_f32_16x16x32_bf16 v[148:151], v[56:59], a[56:59], v[148:151]\n"
"ds_read_b64_tr_b16 v[52:53], v1 offset:2048\n"
"ds_read_b64_tr_b16 v[54:55], v1 offset:2304\n"
"v_and_b32 v109, s81, v86\n"
"v_mfma_f32_16x16x32_bf16 v[152:155], v[56:59], a[60:63], v[152:155]\n"
"v_lshlrev_b32 v110, 16, v87\n"
"v_and_b32 v111, s81, v87\n"
"v_mul_f32 v244, v244, v108\n"
"v_mfma_f32_16x16x32_bf16 v[140:143], v[124:127], a[200:203], v[140:143]\n"
"ds_read_b64_tr_b16 v[56:57], v4 offset:2048\n"
"ds_read_b64_tr_b16 v[58:59], v4 offset:2304\n"
"v_mul_f32 v245, v245, v109\n"
"v_mfma_f32_16x16x32_bf16 v[144:147], v[124:127], a[204:207], v[144:147]\n"
"buffer_load_dwordx4 v[64:67], v10, s[28:31], s74 offen offset:1024 sc1\n"
"v_mul_f32 v246, v246, v110\n"
"v_mfma_f32_16x16x32_bf16 v[148:151], v[128:131], a[200:203], v[148:151]\n"
"ds_read_b64_tr_b16 v[124:125], v1 offset:24576\n"
"ds_read_b64_tr_b16 v[126:127], v1 offset:24832\n"
"v_mul_f32 v247, v247, v111\n"
"v_mfma_f32_16x16x32_bf16 v[152:155], v[128:131], a[204:207], v[152:155]\n"
"v_lshlrev_b32 v108, 16, v90\n"
"v_and_b32 v109, s81, v90\n"
"v_lshlrev_b32 v110, 16, v91\n"
"v_mfma_f32_16x16x32_bf16 v[140:143], v[132:135], a[216:219], v[140:143]\n"
"ds_read_b64_tr_b16 v[128:129], v4 offset:24576\n"
"ds_read_b64_tr_b16 v[130:131], v4 offset:24832\n"
"v_and_b32 v111, s81, v91\n"
"v_mfma_f32_16x16x32_bf16 v[144:147], v[132:135], a[220:223], v[144:147]\n"
"buffer_load_dwordx4 v[68:71], v10, s[28:31], s75 offen offset:0 sc1\n"
"v_mul_f32 v248, v248, v108\n"
"v_mfma_f32_16x16x32_bf16 v[148:151], v[136:139], a[216:219], v[148:151]\n"
"ds_read_b64_tr_b16 v[132:133], v1 offset:26624\n"
"ds_read_b64_tr_b16 v[134:135], v1 offset:26880\n"
"v_mul_f32 v249, v249, v109\n"
"v_mfma_f32_16x16x32_bf16 v[152:155], v[136:139], a[220:223], v[152:155]\n"
"s_waitcnt lgkmcnt(8)\n"
"v_mul_f32 v250, v250, v110\n"
"v_mul_f32 v251, v251, v111\n"
"v_lshlrev_b32 v108, 16, v94\n"
"v_mfma_f32_16x16x32_bf16 v[140:143], v[44:47], a[232:235], v[140:143]\n"
"ds_read_b64_tr_b16 v[136:137], v4 offset:26624\n"
"ds_read_b64_tr_b16 v[138:139], v4 offset:26880\n"
"v_and_b32 v109, s81, v94\n"
"v_mfma_f32_16x16x32_bf16 v[144:147], v[44:47], a[236:239], v[144:147]\n"
"buffer_load_dwordx4 v[72:75], v10, s[28:31], s75 offen offset:1024 sc1\n"
"v_lshlrev_b32 v110, 16, v95\n"
"v_mfma_f32_16x16x32_bf16 v[148:151], v[48:51], a[232:235], v[148:151]\n"
"ds_read_b64_tr_b16 v[44:45], v2 offset:36864\n"
"ds_read_b64_tr_b16 v[46:47], v2 offset:37120\n"
"v_and_b32 v111, s81, v95\n"
"v_mfma_f32_16x16x32_bf16 v[152:155], v[48:51], a[236:239], v[152:155]\n"
"v_mul_f32 v220, v220, v108\n"
"v_mul_f32 v221, v221, v109\n"
"v_mul_f32 v222, v222, v110\n"
"v_mfma_f32_16x16x32_bf16 v[140:143], v[52:55], a[248:251], v[140:143]\n"
"ds_read_b64_tr_b16 v[48:49], v5 offset:36864\n"
"ds_read_b64_tr_b16 v[50:51], v5 offset:37120\n"
"v_mul_f32 v223, v223, v111\n"
"v_mfma_f32_16x16x32_bf16 v[144:147], v[52:55], a[252:255], v[144:147]\n"
"s_waitcnt lgkmcnt(8)\n"
"buffer_load_dwordx2 v[116:117], v182, s[28:31], s69 offen offset:0 sc1\n"
"v_lshlrev_b32 v108, 16, v98\n"
"v_mfma_f32_16x16x32_bf16 v[148:151], v[56:59], a[248:251], v[148:151]\n"
"ds_read_b64_tr_b16 v[52:53], v2 offset:38912\n"
"ds_read_b64_tr_b16 v[54:55], v2 offset:39168\n"
"v_and_b32 v109, s81, v98\n"
"v_mfma_f32_16x16x32_bf16 v[152:155], v[56:59], a[252:255], v[152:155]\n"
"v_lshlrev_b32 v110, 16, v99\n"
"v_and_b32 v111, s81, v99\n"
"v_mul_f32 v224, v224, v108\n"
"v_mfma_f32_16x16x32_bf16 v[140:143], v[124:127], v[20:23], v[140:143]\n"
"ds_read_b64_tr_b16 v[56:57], v5 offset:38912\n"
"ds_read_b64_tr_b16 v[58:59], v5 offset:39168\n"
"v_mul_f32 v225, v225, v109\n"
"v_mfma_f32_16x16x32_bf16 v[144:147], v[124:127], v[24:27], v[144:147]\n"
"buffer_load_dwordx2 v[118:119], v182, s[28:31], s69 offen offset:1024 sc1\n"
"v_mul_f32 v226, v226, v110\n"
"v_mfma_f32_16x16x32_bf16 v[148:151], v[128:131], v[20:23], v[148:151]\n"
"ds_read_b64_tr_b16 v[124:125], v1 offset:8192\n"
"ds_read_b64_tr_b16 v[126:127], v1 offset:8448\n"
"v_mul_f32 v227, v227, v111\n"
"v_mfma_f32_16x16x32_bf16 v[152:155], v[128:131], v[24:27], v[152:155]\n"
"s_waitcnt lgkmcnt(8)\n"
"v_lshlrev_b32 v108, 16, v102\n"
"v_and_b32 v109, s81, v102\n"
"v_lshlrev_b32 v110, 16, v103\n"
"v_mfma_f32_16x16x32_bf16 v[140:143], v[132:135], v[36:39], v[140:143]\n"
"ds_read_b64_tr_b16 v[128:129], v4 offset:8192\n"
"ds_read_b64_tr_b16 v[130:131], v4 offset:8448\n"
"v_and_b32 v111, s81, v103\n"
"v_mfma_f32_16x16x32_bf16 v[144:147], v[132:135], v[40:43], v[144:147]\n"
"buffer_load_dwordx2 v[120:121], v182, s[28:31], s69 offen offset:2048 sc1\n"
"v_mul_f32 v204, v204, v108\n"
"v_mfma_f32_16x16x32_bf16 v[148:151], v[136:139], v[36:39], v[148:151]\n"
"ds_read_b64_tr_b16 v[132:133], v1 offset:10240\n"
"ds_read_b64_tr_b16 v[134:135], v1 offset:10496\n"
"v_mul_f32 v205, v205, v109\n"
"v_mfma_f32_16x16x32_bf16 v[152:155], v[136:139], v[40:43], v[152:155]\n"
"v_mul_f32 v206, v206, v110\n"
"v_mul_f32 v207, v207, v111\n"
"v_mfma_f32_16x16x32_bf16 v[140:143], v[44:47], a[168:171], v[140:143]\n"
"ds_read_b64_tr_b16 v[136:137], v4 offset:10240\n"
"ds_read_b64_tr_b16 v[138:139], v4 offset:10496\n"
"v_mfma_f32_16x16x32_bf16 v[144:147], v[44:47], a[172:175], v[144:147]\n"
"s_waitcnt lgkmcnt(8)\n"
"buffer_load_dwordx2 v[122:123], v182, s[28:31], s69 offen offset:3072 sc1\n"
"v_mfma_f32_16x16x32_bf16 v[148:151], v[48:51], a[168:171], v[148:151]\n"
"ds_read_b64_tr_b16 v[44:45], v1 offset:16384\n"
"ds_read_b64_tr_b16 v[46:47], v1 offset:16640\n"
"v_mfma_f32_16x16x32_bf16 v[152:155], v[48:51], a[172:175], v[152:155]\n"
"v_mfma_f32_16x16x32_bf16 v[140:143], v[52:55], a[184:187], v[140:143]\n"
"ds_read_b64_tr_b16 v[48:49], v4 offset:16384\n"
"ds_read_b64_tr_b16 v[50:51], v4 offset:16640\n"
"v_mfma_f32_16x16x32_bf16 v[144:147], v[52:55], a[188:191], v[144:147]\n"
"v_mfma_f32_16x16x32_bf16 v[148:151], v[56:59], a[184:187], v[148:151]\n"
"ds_read_b64_tr_b16 v[52:53], v1 offset:18432\n"
"ds_read_b64_tr_b16 v[54:55], v1 offset:18688\n"
"v_mfma_f32_16x16x32_bf16 v[152:155], v[56:59], a[188:191], v[152:155]\n"
"s_waitcnt lgkmcnt(8)\n"
"v_mfma_f32_16x16x32_bf16 v[140:143], v[124:127], a[0:3], v[140:143]\n"
"ds_read_b64_tr_b16 v[56:57], v4 offset:18432\n"
"ds_read_b64_tr_b16 v[58:59], v4 offset:18688\n"
"v_mfma_f32_16x16x32_bf16 v[144:147], v[124:127], a[4:7], v[144:147]\n"
"v_mfma_f32_16x16x32_bf16 v[148:151], v[128:131], a[0:3], v[148:151]\n"
"ds_read_b64_tr_b16 v[124:125], v2 offset:0\n"
"ds_read_b64_tr_b16 v[126:127], v2 offset:256\n"
"v_mfma_f32_16x16x32_bf16 v[152:155], v[128:131], a[4:7], v[152:155]\n"
"v_mfma_f32_16x16x32_bf16 v[140:143], v[132:135], a[16:19], v[140:143]\n"
"ds_read_b64_tr_b16 v[128:129], v5 offset:0\n"
"ds_read_b64_tr_b16 v[130:131], v5 offset:256\n"
"v_mfma_f32_16x16x32_bf16 v[144:147], v[132:135], a[20:23], v[144:147]\n"
"s_waitcnt lgkmcnt(8)\n"
"v_mfma_f32_16x16x32_bf16 v[148:151], v[136:139], a[16:19], v[148:151]\n"
"ds_read_b64_tr_b16 v[132:133], v2 offset:2048\n"
"ds_read_b64_tr_b16 v[134:135], v2 offset:2304\n"
"v_mfma_f32_16x16x32_bf16 v[152:155], v[136:139], a[20:23], v[152:155]\n"
"v_mfma_f32_16x16x32_bf16 v[140:143], v[44:47], a[32:35], v[140:143]\n"
"ds_read_b64_tr_b16 v[136:137], v5 offset:2048\n"
"ds_read_b64_tr_b16 v[138:139], v5 offset:2304\n"
"v_mfma_f32_16x16x32_bf16 v[144:147], v[44:47], a[36:39], v[144:147]\n"
"v_mfma_f32_16x16x32_bf16 v[148:151], v[48:51], a[32:35], v[148:151]\n"
"ds_read_b64_tr_b16 v[44:45], v3 offset:20480\n"
"ds_read_b64_tr_b16 v[46:47], v3 offset:20736\n"
"v_mfma_f32_16x16x32_bf16 v[152:155], v[48:51], a[36:39], v[152:155]\n"
"s_waitcnt lgkmcnt(8)\n"
"v_mfma_f32_16x16x32_bf16 v[140:143], v[52:55], a[48:51], v[140:143]\n"
"ds_read_b64_tr_b16 v[48:49], v6 offset:20480\n"
"ds_read_b64_tr_b16 v[50:51], v6 offset:20736\n"
"v_mfma_f32_16x16x32_bf16 v[144:147], v[52:55], a[52:55], v[144:147]\n"
"v_mfma_f32_16x16x32_bf16 v[148:151], v[56:59], a[48:51], v[148:151]\n"
"ds_read_b64_tr_b16 v[52:53], v3 offset:22528\n"
"ds_read_b64_tr_b16 v[54:55], v3 offset:22784\n"
"v_mfma_f32_16x16x32_bf16 v[152:155], v[56:59], a[52:55], v[152:155]\n"
"v_mfma_f32_16x16x32_bf16 v[140:143], v[124:127], a[64:67], v[140:143]\n"
"ds_read_b64_tr_b16 v[56:57], v6 offset:22528\n"
"ds_read_b64_tr_b16 v[58:59], v6 offset:22784\n"
"v_mfma_f32_16x16x32_bf16 v[144:147], v[124:127], a[68:71], v[144:147]\n"
"s_waitcnt lgkmcnt(8)\n"
"v_mfma_f32_16x16x32_bf16 v[148:151], v[128:131], a[64:67], v[148:151]\n"
"ds_read_b64_tr_b16 v[124:125], v2 offset:8192\n"
"ds_read_b64_tr_b16 v[126:127], v2 offset:8448\n"
"v_mfma_f32_16x16x32_bf16 v[152:155], v[128:131], a[68:71], v[152:155]\n"
"v_mfma_f32_16x16x32_bf16 v[140:143], v[132:135], a[80:83], v[140:143]\n"
"ds_read_b64_tr_b16 v[128:129], v5 offset:8192\n"
"ds_read_b64_tr_b16 v[130:131], v5 offset:8448\n"
"v_mfma_f32_16x16x32_bf16 v[144:147], v[132:135], a[84:87], v[144:147]\n"
"v_mfma_f32_16x16x32_bf16 v[148:151], v[136:139], a[80:83], v[148:151]\n"
"ds_read_b64_tr_b16 v[132:133], v2 offset:10240\n"
"ds_read_b64_tr_b16 v[134:135], v2 offset:10496\n"
"v_mfma_f32_16x16x32_bf16 v[152:155], v[136:139], a[84:87], v[152:155]\n"
"s_waitcnt lgkmcnt(8)\n"
"v_mfma_f32_16x16x32_bf16 v[140:143], v[44:47], a[96:99], v[140:143]\n"
"ds_read_b64_tr_b16 v[136:137], v5 offset:10240\n"
"ds_read_b64_tr_b16 v[138:139], v5 offset:10496\n"
"v_mfma_f32_16x16x32_bf16 v[144:147], v[44:47], a[100:103], v[144:147]\n"
"v_mfma_f32_16x16x32_bf16 v[148:151], v[48:51], a[96:99], v[148:151]\n"
"ds_read_b64_tr_b16 v[44:45], v2 offset:16384\n"
"ds_read_b64_tr_b16 v[46:47], v2 offset:16640\n"
"v_mfma_f32_16x16x32_bf16 v[152:155], v[48:51], a[100:103], v[152:155]\n"
"v_mfma_f32_16x16x32_bf16 v[140:143], v[52:55], a[112:115], v[140:143]\n"
"ds_read_b64_tr_b16 v[48:49], v5 offset:16384\n"
"ds_read_b64_tr_b16 v[50:51], v5 offset:16640\n"
"v_mfma_f32_16x16x32_bf16 v[144:147], v[52:55], a[116:119], v[144:147]\n"
"s_waitcnt lgkmcnt(8)\n"
"v_mfma_f32_16x16x32_bf16 v[148:151], v[56:59], a[112:115], v[148:151]\n"
"ds_read_b64_tr_b16 v[52:53], v2 offset:18432\n"
"ds_read_b64_tr_b16 v[54:55], v2 offset:18688\n"
"v_mfma_f32_16x16x32_bf16 v[152:155], v[56:59], a[116:119], v[152:155]\n"
"v_mfma_f32_16x16x32_bf16 v[140:143], v[124:127], a[128:131], v[140:143]\n"
"ds_read_b64_tr_b16 v[56:57], v5 offset:18432\n"
"ds_read_b64_tr_b16 v[58:59], v5 offset:18688\n"
"v_mfma_f32_16x16x32_bf16 v[144:147], v[124:127], a[132:135], v[144:147]\n"
"v_mfma_f32_16x16x32_bf16 v[148:151], v[128:131], a[128:131], v[148:151]\n"
"ds_read_b64_tr_b16 v[124:125], v3 offset:24576\n"
"ds_read_b64_tr_b16 v[126:127], v3 offset:24832\n"
"v_mfma_f32_16x16x32_bf16 v[152:155], v[128:131], a[132:135], v[152:155]\n"
"s_waitcnt lgkmcnt(8)\n"
"v_mfma_f32_16x16x32_bf16 v[140:143], v[132:135], a[144:147], v[140:143]\n"
"ds_read_b64_tr_b16 v[128:129], v6 offset:24576\n"
"ds_read_b64_tr_b16 v[130:131], v6 offset:24832\n"
"v_mfma_f32_16x16x32_bf16 v[144:147], v[132:135], a[148:151], v[144:147]\n"
"v_mfma_f32_16x16x32_bf16 v[148:151], v[136:139], a[144:147], v[148:151]\n"
"ds_read_b64_tr_b16 v[132:133], v3 offset:26624\n"
"ds_read_b64_tr_b16 v[134:135], v3 offset:26880\n"
"v_mfma_f32_16x16x32_bf16 v[152:155], v[136:139], a[148:151], v[152:155]\n"
"v_mfma_f32_16x16x32_bf16 v[140:143], v[44:47], a[160:163], v[140:143]\n"
"ds_read_b64_tr_b16 v[136:137], v6 offset:26624\n"
"ds_read_b64_tr_b16 v[138:139], v6 offset:26880\n"
"v_mfma_f32_16x16x32_bf16 v[144:147], v[44:47], a[164:167], v[144:147]\n"
"s_waitcnt lgkmcnt(8)\n"
"v_mfma_f32_16x16x32_bf16 v[148:151], v[48:51], a[160:163], v[148:151]\n"
"ds_read_b64_tr_b16 v[44:45], v2 offset:24576\n"
"ds_read_b64_tr_b16 v[46:47], v2 offset:24832\n"
"v_mfma_f32_16x16x32_bf16 v[152:155], v[48:51], a[164:167], v[152:155]\n"
"s_waitcnt vmcnt(0)\n"
"v_max3_u32 v216, v60, v61, v62\n"
"v_max3_u32 v216, v216, v63, v64\n"
"v_max3_u32 v216, v216, v65, v66\n"
"v_max3_u32 v216, v216, v67, v68\n"
"v_max3_u32 v216, v216, v69, v70\n"
"v_max3_u32 v216, v216, v71, v72\n"
"v_max3_u32 v216, v216, v73, v74\n"
"v_max3_u32 v216, v216, v75, v116\n"
"v_max3_u32 v216, v216, v117, v118\n"
"v_max3_u32 v216, v216, v119, v120\n"
"v_max3_u32 v216, v216, v121, v122\n"
"v_max_u32 v216, v216, v123\n"
"v_cmp_eq_u32 vcc, -1, v216\n"
"s_nop 3\n"
"s_cbranch_vccz pFR_ok_gated_mix_fused\n"
"s_mov_b32 s83, 0\n"
"pFR_sp_gated_mix_fused:\n"
"buffer_load_dwordx4 v[60:63], v10, s[28:31], s74 offen offset:0 sc1\n"
"buffer_load_dwordx4 v[64:67], v10, s[28:31], s74 offen offset:1024 sc1\n"
"buffer_load_dwordx4 v[68:71], v10, s[28:31], s75 offen offset:0 sc1\n"
"buffer_load_dwordx4 v[72:75], v10, s[28:31], s75 offen offset:1024 sc1\n"
"buffer_load_dwordx2 v[116:117], v182, s[28:31], s69 offen offset:0 sc1\n"
"buffer_load_dwordx2 v[118:119], v182, s[28:31], s69 offen offset:1024 sc1\n"
"buffer_load_dwordx2 v[120:121], v182, s[28:31], s69 offen offset:2048 sc1\n"
"buffer_load_dwordx2 v[122:123], v182, s[28:31], s69 offen offset:3072 sc1\n"
"s_waitcnt vmcnt(0)\n"
"s_add_u32 s83, s83, 1\n"
"s_cmp_gt_u32 s83, 1048576\n"
"s_cbranch_scc1 pFR_ok_gated_mix_fused\n"
"v_max3_u32 v216, v60, v61, v62\n"
"v_max3_u32 v216, v216, v63, v64\n"
"v_max3_u32 v216, v216, v65, v66\n"
"v_max3_u32 v216, v216, v67, v68\n"
"v_max3_u32 v216, v216, v69, v70\n"
"v_max3_u32 v216, v216, v71, v72\n"
"v_max3_u32 v216, v216, v73, v74\n"
"v_max3_u32 v216, v216, v75, v116\n"
"v_max3_u32 v216, v216, v117, v118\n"
"v_max3_u32 v216, v216, v119, v120\n"
"v_max3_u32 v216, v216, v121, v122\n"
"v_max_u32 v216, v216, v123\n"
"v_cmp_eq_u32 vcc, -1, v216\n"
"s_nop 3\n"
"s_cbranch_vccnz pFR_sp_gated_mix_fused\n"
"pFR_ok_gated_mix_fused:\n"
"v_mfma_f32_16x16x32_bf16 v[140:143], v[52:55], a[176:179], v[140:143]\n"
"ds_read_b64_tr_b16 v[48:49], v5 offset:24576\n"
"ds_read_b64_tr_b16 v[50:51], v5 offset:24832\n"
"v_lshlrev_b32 v208, 16, v116\n"
"v_mfma_f32_16x16x32_bf16 v[144:147], v[52:55], a[180:183], v[144:147]\n"
"buffer_load_dwordx4 v[76:79], v10, s[28:31], s76 offen offset:0 sc1\n"
"v_and_b32 v209, s81, v116\n"
"v_mfma_f32_16x16x32_bf16 v[148:151], v[56:59], a[176:179], v[148:151]\n"
"ds_read_b64_tr_b16 v[52:53], v2 offset:26624\n"
"ds_read_b64_tr_b16 v[54:55], v2 offset:26880\n"
"v_lshlrev_b32 v210, 16, v117\n"
"v_mfma_f32_16x16x32_bf16 v[152:155], v[56:59], a[180:183], v[152:155]\n"
"s_waitcnt lgkmcnt(8)\n"
"v_and_b32 v211, s81, v117\n"
"v_lshlrev_b32 v212, 16, v118\n"
"v_and_b32 v213, s81, v118\n"
"v_mfma_f32_16x16x32_bf16 v[140:143], v[124:127], a[192:195], v[140:143]\n"
"ds_read_b64_tr_b16 v[56:57], v5 offset:26624\n"
"ds_read_b64_tr_b16 v[58:59], v5 offset:26880\n"
"v_lshlrev_b32 v214, 16, v119\n"
"v_mfma_f32_16x16x32_bf16 v[144:147], v[124:127], a[196:199], v[144:147]\n"
"buffer_load_dwordx4 v[80:83], v10, s[28:31], s76 offen offset:1024 sc1\n"
"v_and_b32 v215, s81, v119\n"
"v_mfma_f32_16x16x32_bf16 v[148:151], v[128:131], a[192:195], v[148:151]\n"
"ds_read_b64_tr_b16 v[124:125], v2 offset:32768\n"
"ds_read_b64_tr_b16 v[126:127], v2 offset:33024\n"
"v_add_f32 v208, v208, v212\n"
"v_mfma_f32_16x16x32_bf16 v[152:155], v[128:131], a[196:199], v[152:155]\n"
"v_add_f32 v209, v209, v213\n"
"v_add_f32 v210, v210, v214\n"
"v_add_f32 v211, v211, v215\n"
"v_mfma_f32_16x16x32_bf16 v[140:143], v[132:135], a[208:211], v[140:143]\n"
"ds_read_b64_tr_b16 v[128:129], v5 offset:32768\n"
"ds_read_b64_tr_b16 v[130:131], v5 offset:33024\n"
"v_lshlrev_b32 v212, 16, v120\n"
"v_mfma_f32_16x16x32_bf16 v[144:147], v[132:135], a[212:215], v[144:147]\n"
"s_waitcnt lgkmcnt(8)\n"
"v_and_b32 v213, s81, v120\n"
"v_lshlrev_b32 v214, 16, v121\n"
"v_and_b32 v215, s81, v121\n"
"v_mfma_f32_16x16x32_bf16 v[148:151], v[136:139], a[208:211], v[148:151]\n"
"ds_read_b64_tr_b16 v[132:133], v2 offset:34816\n"
"ds_read_b64_tr_b16 v[134:135], v2 offset:35072\n"
"v_add_f32 v208, v208, v212\n"
"v_mfma_f32_16x16x32_bf16 v[152:155], v[136:139], a[212:215], v[152:155]\n"
"v_add_f32 v209, v209, v213\n"
"v_add_f32 v210, v210, v214\n"
"v_add_f32 v211, v211, v215\n"
"v_mfma_f32_16x16x32_bf16 v[140:143], v[44:47], a[224:227], v[140:143]\n"
"ds_read_b64_tr_b16 v[136:137], v5 offset:34816\n"
"ds_read_b64_tr_b16 v[138:139], v5 offset:35072\n"
"v_lshlrev_b32 v212, 16, v122\n"
"v_mfma_f32_16x16x32_bf16 v[144:147], v[44:47], a[228:231], v[144:147]\n"
"v_and_b32 v213, s81, v122\n"
"v_lshlrev_b32 v214, 16, v123\n"
"v_and_b32 v215, s81, v123\n"
"v_mfma_f32_16x16x32_bf16 v[148:151], v[48:51], a[224:227], v[148:151]\n"
"v_add_f32 v208, v208, v212\n"
"v_add_f32 v209, v209, v213\n"
"v_add_f32 v210, v210, v214\n"
"v_mfma_f32_16x16x32_bf16 v[152:155], v[48:51], a[228:231], v[152:155]\n"
"s_waitcnt lgkmcnt(6)\n"
"v_add_f32 v211, v211, v215\n"
"v_fma_f32 v208, v208, v187, v204\n"
"v_fma_f32 v209, v209, v187, v205\n"
"v_mfma_f32_16x16x32_bf16 v[140:143], v[52:55], a[240:243], v[140:143]\n"
"v_fma_f32 v210, v210, v187, v206\n"
"v_fma_f32 v211, v211, v187, v207\n"
"v_cvt_pk_bf16_f32 v212, v208, v209\n"
"v_mfma_f32_16x16x32_bf16 v[144:147], v[52:55], a[244:247], v[144:147]\n"
"v_cvt_pk_bf16_f32 v213, v210, v211\n"
"v_lshlrev_b32 v188, 16, v60\n"
"v_and_b32 v189, s81, v60\n"
"v_mfma_f32_16x16x32_bf16 v[148:151], v[56:59], a[240:243], v[148:151]\n"
"buffer_store_dwordx2 v[212:213], v180, s[40:43], s73 offen offset:0\n"
"v_fmac_f32 v244, v188, v185\n"
"v_mfma_f32_16x16x32_bf16 v[152:155], v[56:59], a[244:247], v[152:155]\n"
"v_fmac_f32 v245, v189, v185\n"
"v_lshlrev_b32 v190, 16, v61\n"
"v_and_b32 v191, s81, v61\n"
"v_mfma_f32_16x16x32_bf16 v[140:143], v[124:127], v[12:15], v[140:143]\n"
"v_fmac_f32 v246, v190, v185\n"
"v_fmac_f32 v247, v191, v185\n"
"v_lshlrev_b32 v192, 16, v62\n"
"v_mfma_f32_16x16x32_bf16 v[144:147], v[124:127], v[16:19], v[144:147]\n"
"s_waitcnt lgkmcnt(0)\n"
"v_and_b32 v193, s81, v62\n"
"v_fmac_f32 v248, v192, v186\n"
"v_lshlrev_b32 v194, 16, v63\n"
"v_mfma_f32_16x16x32_bf16 v[148:151], v[128:131], v[12:15], v[148:151]\n"
"v_fmac_f32 v249, v193, v186\n"
"v_and_b32 v195, s81, v63\n"
"v_fmac_f32 v250, v194, v186\n"
"v_mfma_f32_16x16x32_bf16 v[152:155], v[128:131], v[16:19], v[152:155]\n"
"v_fmac_f32 v251, v195, v186\n"
"v_lshlrev_b32 v196, 16, v64\n"
"v_and_b32 v197, s81, v64\n"
"v_mfma_f32_16x16x32_bf16 v[140:143], v[132:135], v[28:31], v[140:143]\n"
"v_fmac_f32 v220, v196, v185\n"
"v_fmac_f32 v221, v197, v185\n"
"v_lshlrev_b32 v198, 16, v65\n"
"v_mfma_f32_16x16x32_bf16 v[144:147], v[132:135], v[32:35], v[144:147]\n"
"v_and_b32 v199, s81, v65\n"
"v_fmac_f32 v222, v198, v185\n"
"v_lshlrev_b32 v200, 16, v66\n"
"v_mfma_f32_16x16x32_bf16 v[148:151], v[136:139], v[28:31], v[148:151]\n"
"v_fmac_f32 v223, v199, v185\n"
"v_and_b32 v201, s81, v66\n"
"v_fmac_f32 v224, v200, v186\n"
"v_mfma_f32_16x16x32_bf16 v[152:155], v[136:139], v[32:35], v[152:155]\n"
"v_fmac_f32 v225, v201, v186\n"
"v_lshlrev_b32 v202, 16, v67\n"
"v_and_b32 v203, s81, v67\n"
"v_fmac_f32 v226, v202, v186\n"
"v_fmac_f32 v227, v203, v186\n"
"v_lshlrev_b32 v188, 16, v68\n"
"v_and_b32 v189, s81, v68\n"
"v_fmac_f32 v244, v188, v185\n"
"v_fmac_f32 v245, v189, v185\n"
"v_lshlrev_b32 v190, 16, v69\n"
"v_and_b32 v191, s81, v69\n"
"v_fmac_f32 v246, v190, v185\n"
"v_fmac_f32 v247, v191, v185\n"
"v_lshlrev_b32 v192, 16, v70\n"
"v_and_b32 v193, s81, v70\n"
"v_fmac_f32 v248, v192, v186\n"
"v_fmac_f32 v249, v193, v186\n"
"v_lshlrev_b32 v194, 16, v71\n"
"v_and_b32 v195, s81, v71\n"
"v_fmac_f32 v250, v194, v186\n"
"v_fmac_f32 v251, v195, v186\n"
"v_lshlrev_b32 v196, 16, v72\n"
"v_and_b32 v197, s81, v72\n"
"v_fmac_f32 v220, v196, v185\n"
"v_fmac_f32 v221, v197, v185\n"
"v_lshlrev_b32 v198, 16, v73\n"
"v_and_b32 v199, s81, v73\n"
"v_fmac_f32 v222, v198, v185\n"
"v_fmac_f32 v223, v199, v185\n"
"v_lshlrev_b32 v200, 16, v74\n"
"v_and_b32 v201, s81, v74\n"
"v_fmac_f32 v224, v200, v186\n"
"v_fmac_f32 v225, v201, v186\n"
"v_lshlrev_b32 v202, 16, v75\n"
"v_and_b32 v203, s81, v75\n"
"v_fmac_f32 v226, v202, v186\n"
"v_fmac_f32 v227, v203, v186\n"
"v_fma_f32 v140, v140, v185, v244\n"
"v_fma_f32 v141, v141, v185, v245\n"
"v_fma_f32 v142, v142, v185, v246\n"
"v_fma_f32 v143, v143, v185, v247\n"
"v_fma_f32 v144, v144, v186, v248\n"
"v_fma_f32 v145, v145, v186, v249\n"
"v_fma_f32 v146, v146, v186, v250\n"
"v_fma_f32 v147, v147, v186, v251\n"
"v_fma_f32 v148, v148, v185, v220\n"
"v_fma_f32 v149, v149, v185, v221\n"
"v_fma_f32 v150, v150, v185, v222\n"
"v_fma_f32 v151, v151, v185, v223\n"
"v_fma_f32 v152, v152, v186, v224\n"
"v_fma_f32 v153, v153, v186, v225\n"
"v_fma_f32 v154, v154, v186, v226\n"
"v_fma_f32 v155, v155, v186, v227\n"
"s_waitcnt vmcnt(1)\n"
"v_max3_u32 v216, v76, v77, v78\n"
"v_max3_u32 v216, v216, v79, v80\n"
"v_max3_u32 v216, v216, v81, v82\n"
"v_max_u32 v216, v216, v83\n"
"v_cmp_eq_u32 vcc, -1, v216\n"
"s_nop 3\n"
"s_cbranch_vccz pFS_ok_gated_mix_fused\n"
"s_mov_b32 s83, 0\n"
"pFS_sp_gated_mix_fused:\n"
"buffer_load_dwordx4 v[76:79], v10, s[28:31], s76 offen offset:0 sc1\n"
"buffer_load_dwordx4 v[80:83], v10, s[28:31], s76 offen offset:1024 sc1\n"
"s_waitcnt vmcnt(0)\n"
"s_add_u32 s83, s83, 1\n"
"s_cmp_gt_u32 s83, 1048576\n"
"s_cbranch_scc1 pFS_ok_gated_mix_fused\n"
"v_max3_u32 v216, v76, v77, v78\n"
"v_max3_u32 v216, v216, v79, v80\n"
"v_max3_u32 v216, v216, v81, v82\n"
"v_max_u32 v216, v216, v83\n"
"v_cmp_eq_u32 vcc, -1, v216\n"
"s_nop 3\n"
"s_cbranch_vccnz pFS_sp_gated_mix_fused\n"
"pFS_ok_gated_mix_fused:\n"
"v_lshlrev_b32 v188, 16, v76\n"
"v_and_b32 v189, s81, v76\n"
"v_lshlrev_b32 v190, 16, v77\n"
"v_and_b32 v191, s81, v77\n"
"v_fmac_f32 v140, v188, v185\n"
"v_fmac_f32 v141, v189, v185\n"
"v_fmac_f32 v142, v190, v185\n"
"v_fmac_f32 v143, v191, v185\n"
"v_cvt_pk_bf16_f32 v60, v140, v141\n"
"v_cvt_pk_bf16_f32 v61, v142, v143\n"
"ds_write_b64 v252, v[60:61] offset:0\n"
"v_lshlrev_b32 v192, 16, v78\n"
"v_and_b32 v193, s81, v78\n"
"v_lshlrev_b32 v194, 16, v79\n"
"v_and_b32 v195, s81, v79\n"
"v_fmac_f32 v144, v192, v186\n"
"v_fmac_f32 v145, v193, v186\n"
"v_fmac_f32 v146, v194, v186\n"
"v_fmac_f32 v147, v195, v186\n"
"v_cvt_pk_bf16_f32 v62, v144, v145\n"
"v_cvt_pk_bf16_f32 v63, v146, v147\n"
"ds_write_b64 v253, v[62:63] offset:0\n"
"v_lshlrev_b32 v196, 16, v80\n"
"v_and_b32 v197, s81, v80\n"
"v_lshlrev_b32 v198, 16, v81\n"
"v_and_b32 v199, s81, v81\n"
"v_fmac_f32 v148, v196, v185\n"
"v_fmac_f32 v149, v197, v185\n"
"v_fmac_f32 v150, v198, v185\n"
"v_fmac_f32 v151, v199, v185\n"
"v_cvt_pk_bf16_f32 v64, v148, v149\n"
"v_cvt_pk_bf16_f32 v65, v150, v151\n"
"ds_write_b64 v252, v[64:65] offset:32\n"
"v_lshlrev_b32 v200, 16, v82\n"
"v_and_b32 v201, s81, v82\n"
"v_lshlrev_b32 v202, 16, v83\n"
"v_and_b32 v203, s81, v83\n"
"v_fmac_f32 v152, v200, v186\n"
"v_fmac_f32 v153, v201, v186\n"
"v_fmac_f32 v154, v202, v186\n"
"v_fmac_f32 v155, v203, v186\n"
"v_cvt_pk_bf16_f32 v66, v152, v153\n"
"v_cvt_pk_bf16_f32 v67, v154, v155\n"
"ds_write_b64 v253, v[66:67] offset:32\n"
"s_waitcnt lgkmcnt(0)\n"
"ds_read_b128 v[68:71], v184 offset:0\n"
"ds_read_b128 v[72:75], v184 offset:1280\n"
"s_waitcnt lgkmcnt(1)\n"
"buffer_store_dwordx4 v[68:71], v173, s[40:43], s25 offen offset:0\n"
"s_waitcnt lgkmcnt(0)\n"
"buffer_store_dwordx4 v[72:75], v173, s[40:43], s26 offen offset:0\n"
"pFend_ok_gated_mix_fused:\n"
  :: [ka]"s"(__builtin_amdgcn_kernarg_segment_ptr()), [bid]"s"(blockIdx.x), [tid]"v"(threadIdx.x), [lds]"s"((unsigned)(size_t)smem)
  : "v1","v2","v3","v4","v5","v6","v7","v8","v9","v10","v11","v12","v13","v14","v15","v16","v17","v18","v19","v20","v21","v22","v23","v24","v25","v26","v27","v28","v29","v30","v31","v32","v33","v34","v35","v36","v37","v38","v39","v40","v41","v42","v43","v44","v45","v46","v47","v48","v49","v50","v51","v52","v53","v54","v55","v56","v57","v58","v59","v60","v61","v62","v63","v64","v65","v66","v67","v68","v69","v70","v71","v72","v73","v74","v75","v76","v77","v78","v79","v80","v81","v82","v83","v84","v85","v86","v87","v88","v89","v90","v91","v92","v93","v94","v95","v96","v97","v98","v99","v100","v101","v102","v103","v104","v105","v106","v107","v108","v109","v110","v111","v112","v113","v114","v115","v116","v117","v118","v119","v120","v121","v122","v123","v124","v125","v126","v127","v128","v129","v130","v131","v132","v133","v134","v135","v136","v137","v138","v139","v140","v141","v142","v143","v144","v145","v146","v147","v148","v149","v150","v151","v152","v153","v154","v155","v156","v157","v158","v159","v160","v161","v162","v163","v164","v165","v166","v167","v168","v169","v170","v171","v172","v173","v174","v175","v176","v177","v178","v179","v180","v181","v182","v183","v184","v185","v186","v187","v188","v189","v190","v191","v192","v193","v194","v195","v196","v197","v198","v199","v200","v201","v202","v203","v204","v205","v206","v207","v208","v209","v210","v211","v212","v213","v214","v215","v216","v217","v218","v219","v220","v221","v222","v223","v224","v225","v226","v227","v228","v229","v230","v231","v232","v233","v234","v235","v236","v237","v238","v239","v240","v241","v242","v243","v244","v245","v246","v247","v248","v249","v250","v251","v252","v253","v254","v255","a0","a1","a2","a3","a4","a5","a6","a7","a8","a9","a10","a11","a12","a13","a14","a15","a16","a17","a18","a19","a20","a21","a22","a23","a24","a25","a26","a27","a28","a29","a30","a31","a32","a33","a34","a35","a36","a37","a38","a39","a40","a41","a42","a43","a44","a45","a46","a47","a48","a49","a50","a51","a52","a53","a54","a55","a56","a57","a58","a59","a60","a61","a62","a63","a64","a65","a66","a67","a68","a69","a70","a71","a72","a73","a74","a75","a76","a77","a78","a79","a80","a81","a82","a83","a84","a85","a86","a87","a88","a89","a90","a91","a92","a93","a94","a95","a96","a97","a98","a99","a100","a101","a102","a103","a104","a105","a106","a107","a108","a109","a110","a111","a112","a113","a114","a115","a116","a117","a118","a119","a120","a121","a122","a123","a124","a125","a126","a127","a128","a129","a130","a131","a132","a133","a134","a135","a136","a137","a138","a139","a140","a141","a142","a143","a144","a145","a146","a147","a148","a149","a150","a151","a152","a153","a154","a155","a156","a157","a158","a159","a160","a161","a162","a163","a164","a165","a166","a167","a168","a169","a170","a171","a172","a173","a174","a175","a176","a177","a178","a179","a180","a181","a182","a183","a184","a185","a186","a187","a188","a189","a190","a191","a192","a193","a194","a195","a196","a197","a198","a199","a200","a201","a202","a203","a204","a205","a206","a207","a208","a209","a210","a211","a212","a213","a214","a215","a216","a217","a218","a219","a220","a221","a222","a223","a224","a225","a226","a227","a228","a229","a230","a231","a232","a233","a234","a235","a236","a237","a238","a239","a240","a241","a242","a243","a244","a245","a246","a247","a248","a249","a250","a251","a252","a253","a254","a255","s4","s5","s6","s7","s8","s9","s10","s11","s12","s13","s14","s15","s16","s17","s18","s19","s20","s21","s22","s23","s24","s25","s26","s27","s28","s29","s30","s31","s33","s34","s35","s36","s37","s38","s39","s40","s41","s42","s43","s44","s45","s46","s47","s48","s49","s50","s51","s52","s53","s54","s55","s56","s57","s58","s59","s60","s61","s62","s63","s64","s65","s66","s67","s68","s69","s70","s71","s72","s73","s74","s75","s76","s77","s78","s79","s80","s81","s82","s83","s84","s85","s86","s87","s88","s89","s90","s91","s92","s93","s94","s95","s96","s97","s98","s99","vcc","scc","memory");
}

extern "C" void kernel_launch(void* const* d_in, const int* in_sizes, int n_in, void* d_out, int out_size, void* d_ws, size_t ws_size,
                              hipStream_t stream) {
  const unsigned F = 0x20000u;
  const void *q = d_in[0], *k = d_in[1], *v = d_in[2], *sk = d_in[3], *ssmk = d_in[4], *ssmD = d_in[5];
  const void *wg = d_in[7], *wu = d_in[8], *wd = d_in[9], *gw = d_in[10];
  void *out = d_out, *ws = d_ws;
  hipLaunchKernelGGL(gated_mix_fused, dim3(256), dim3(256), 0, stream, q, 2048u * 1152u, F, k, 2048u * 1152u, F, v, 2048u * 1152u, F, ssmk, 576u * 4096u, F, wg, 576u * 9216u, F, wu, 576u * 9216u, F, ws, 33427456u, F, wd, 4608u * 1152u, F, out, 2048u * 1152u, F, sk, gw, ssmD);
}
```
